```python
import torch

def _softmax(x, axis=-1):
    return torch.softmax(x, dim=axis)
def _gelu(x):
    return torch.nn.functional.gelu(x, approximate="tanh")
def _silu(x):
    return torch.nn.functional.silu(x)
def _sigmoid(x):
    return torch.sigmoid(x)
def _layernorm(x, axis=-1, eps=1e-5):
    m = x.mean(dim=axis, keepdim=True)
    v = x.var(dim=axis, keepdim=True, unbiased=False)
    return (x - m) / torch.sqrt(v + eps)
def _scatter_add(tgt, idx, src, axis=0):
    return tgt.clone().index_add_(axis, idx, src)
def _segment_sum(src, seg, n):
    out = torch.zeros((n,) + src.shape[1:], dtype=src.dtype)
    return out.index_add_(0, seg, src)
def _pad(x, pw):
    flat = tuple(v for pair in reversed(pw) for v in pair)
    return torch.nn.functional.pad(x, flat)

kernel_name = 'parallel_sum__sliding_block__glu_gated_glu__prompt'

def setup_inputs():

    _w = (lambda a: (lambda t: t.to(torch.bfloat16) if t.is_floating_point() else t)(torch.as_tensor(a)))
    def _A_setup():
        B, S, D = 1, 2048, 640
        return {
            "x": _w((lambda *s: torch.randn(*s, dtype=torch.bfloat16) * 0.05)(B, S, D)),
            "wqkv": _w((lambda *s: torch.randn(*s, dtype=torch.bfloat16) * 0.05)(D, 3 * D)),
            "wo": _w((lambda *s: torch.randn(*s, dtype=torch.bfloat16) * 0.05)(D, D)),
        }
    _A_in = _A_setup()

    def _B_setup():
        B, S, D, F = 1, 2048, 640, 1536
        return {
            "x": _w((lambda *s: torch.randn(*s, dtype=torch.bfloat16) * 0.05)(B, S, D)),
            "w_gate": _w((lambda *s: torch.randn(*s, dtype=torch.bfloat16) * 0.05)(D, F)),
            "w_up": _w((lambda *s: torch.randn(*s, dtype=torch.bfloat16) * 0.05)(D, F)),
            "w_down": _w((lambda *s: torch.randn(*s, dtype=torch.bfloat16) * 0.05)(F, D)),
        }
    _B_in = _B_setup()

    return {
        "A_x": _A_in["x"],
        "A_wqkv": _A_in["wqkv"],
        "A_wo": _A_in["wo"],
        "B_x": _B_in["x"],
        "B_w_gate": _B_in["w_gate"],
        "B_w_up": _B_in["w_up"],
        "B_w_down": _B_in["w_down"],
    }

def reference(A_x, A_wqkv, A_wo, B_x, B_w_gate, B_w_up, B_w_down):
    def _A_ref(x, wqkv, wo):
        B, S, D = x.shape
        H = 32; Dh = D // H; W = 256
        qkv = (lambda x, *d: x.permute(*d))((x @ wqkv).reshape(B, S, 3, H, Dh), 2, 0, 3, 1, 4)
        q, k, v = qkv[0], qkv[1], qkv[2]
        scores = torch.einsum("bhqd,bhkd->bhqk", q, k) * (Dh ** -0.5)
        idx = torch.arange(S)
        mask = (idx[:, None] - idx[None, :] >= 0) & (idx[:, None] - idx[None, :] < W)
        scores = torch.where(mask[None, None], scores, -1e9)
        p = _softmax(scores, axis=-1)
        o = torch.einsum("bhqk,bhkd->bhqd", p, v)
        return (lambda x, *d: x.permute(*d))(o, 0, 2, 1, 3).reshape(B, S, D) @ wo

    def _B_ref(x, w_gate, w_up, w_down):
        g = torch.einsum("bsd,df->bsf", x, w_gate)
        u = torch.einsum("bsd,df->bsf", x, w_up)
        h = _sigmoid(g) * u
        return torch.einsum("bsf,fd->bsd", h, w_down)

    x = A_x
    xn = _layernorm(x, axis=-1)
    return x + _A_ref(xn, A_wqkv, A_wo) + _B_ref(xn, B_w_gate, B_w_up, B_w_down)
```

```cpp
#include <hip/hip_runtime.h>
#include <hip/hip_bf16.h>
typedef unsigned short u16;
__device__ __forceinline__ float b2f(u16 v){ return __uint_as_float(((unsigned)v)<<16); }
__device__ __forceinline__ u16 f2b(float f){ unsigned u=__float_as_uint(f); u += 0x7fffu + ((u>>16)&1u); return (u16)(u>>16); }
__device__ __forceinline__ float rb(float f){ return b2f(f2b(f)); }
constexpr int S=2048,D=640,F=1536,H=32,DH=20,W=256;

__global__ void k_ln(const u16* x, u16* xn){
  int r=blockIdx.x; __shared__ float red[256];
  float s=0; for(int i=threadIdx.x;i<D;i+=256) s+=b2f(x[r*D+i]);
  red[threadIdx.x]=s; __syncthreads();
  for(int o=128;o>0;o>>=1){ if(threadIdx.x<o) red[threadIdx.x]+=red[threadIdx.x+o]; __syncthreads(); }
  float m=red[0]/D; __syncthreads();
  float v=0; for(int i=threadIdx.x;i<D;i+=256){ float d=b2f(x[r*D+i])-m; v+=d*d; }
  red[threadIdx.x]=v; __syncthreads();
  for(int o=128;o>0;o>>=1){ if(threadIdx.x<o) red[threadIdx.x]+=red[threadIdx.x+o]; __syncthreads(); }
  float var=rb(red[0]/D); float mb=rb(m);
  float sd=rb(sqrtf(rb(var+1e-5f)));
  for(int i=threadIdx.x;i<D;i+=256){ float d=rb(b2f(x[r*D+i])-mb); xn[r*D+i]=f2b(d/sd); }
}
__global__ void k_gemm(const u16* A,const u16* B,float* C,int M,int N,int K){
  int n=blockIdx.x*64+threadIdx.x%64, m=blockIdx.y*4+threadIdx.x/64;
  if(n>=N||m>=M) return;
  float acc=0; for(int k=0;k<K;k++) acc+=b2f(A[m*K+k])*b2f(B[k*N+n]);
  C[m*N+n]=acc;
}
__global__ void k_attn(const float* qkv,u16* o){
  int q=blockIdx.x, h=threadIdx.x;
  float qv[DH]; for(int d=0;d<DH;d++) qv[d]=rb(qkv[q*3*D+h*DH+d]);
  int k0=q-W+1; if(k0<0)k0=0;
  float mx=-1e30f;
  for(int k=k0;k<=q;k++){ float s=0; for(int d=0;d<DH;d++) s+=qv[d]*rb(qkv[k*3*D+D+h*DH+d]); s=rb(rb(s)*rb(0.22360679775f)); mx=fmaxf(mx,s);}
  float sum=0, acc[DH]; for(int d=0;d<DH;d++) acc[d]=0;
  for(int k=k0;k<=q;k++){ float s=0; for(int d=0;d<DH;d++) s+=qv[d]*rb(qkv[k*3*D+D+h*DH+d]); s=rb(rb(s)*rb(0.22360679775f)); float p=__expf(s-mx); sum+=p; for(int d=0;d<DH;d++) acc[d]+=p*rb(qkv[k*3*D+2*D+h*DH+d]); }
  for(int d=0;d<DH;d++) o[q*D+h*DH+d]=f2b(acc[d]/sum);
}
__global__ void k_h(const float* g,const float* u,u16* h,int n){ int i=blockIdx.x*256+threadIdx.x; if(i<n){ float gg=rb(g[i]); float s=rb(1.f/(1.f+__expf(-gg))); h[i]=f2b(s*rb(u[i])); } }
__global__ void k_fin(const u16* x,const float* a,const float* b,u16* out,int n){ int i=blockIdx.x*256+threadIdx.x; if(i<n){ out[i]=f2b(rb(b2f(x[i])+rb(a[i]))+rb(b[i])); } }

extern "C" void kernel_launch(void* const* d_in, const int* in_sizes, int n_in, void* d_out, int out_size, void* d_ws, size_t ws_size, hipStream_t stream) {
  const u16* x=(const u16*)d_in[0]; const u16* wqkv=(const u16*)d_in[1]; const u16* wo=(const u16*)d_in[2];
  const u16* wg=(const u16*)d_in[4]; const u16* wu=(const u16*)d_in[5]; const u16* wd=(const u16*)d_in[6];
  char* ws=(char*)d_ws;
  u16* xn=(u16*)ws; float* qkv=(float*)(ws+(4<<20)); float* g=(float*)(ws+(32<<20)); float* u=(float*)(ws+(48<<20));
  u16* o=(u16*)(ws+(64<<20)); u16* h=(u16*)(ws+(72<<20)); float* a=(float*)(ws+(80<<20)); float* b=(float*)(ws+(96<<20));
  k_ln<<<S,256,0,stream>>>(x,xn);
  k_gemm<<<dim3(3*D/64,S/4),256,0,stream>>>(xn,wqkv,qkv,S,3*D,D);
  k_gemm<<<dim3(F/64,S/4),256,0,stream>>>(xn,wg,g,S,F,D);
  k_gemm<<<dim3(F/64,S/4),256,0,stream>>>(xn,wu,u,S,F,D);
  k_attn<<<S,32,0,stream>>>(qkv,o);
  k_h<<<S*F/256,256,0,stream>>>(g,u,h,S*F);
  k_gemm<<<dim3(D/64,S/4),256,0,stream>>>(o,wo,a,S,D,D);
  k_gemm<<<dim3(D/64,S/4),256,0,stream>>>(h,wd,b,S,D,F);
  k_fin<<<S*D/256,256,0,stream>>>(x,a,b,(u16*)d_out,S*D);
}
```

```cpp
#include <hip/hip_runtime.h>

typedef unsigned short u16;
typedef int i32x4 __attribute__((ext_vector_type(4)));
typedef unsigned u32x4 __attribute__((ext_vector_type(4)));
typedef unsigned u32x2 __attribute__((ext_vector_type(2)));
typedef float f32x4 __attribute__((ext_vector_type(4)));
typedef float f32x2 __attribute__((ext_vector_type(2)));
typedef float f32x16 __attribute__((ext_vector_type(16)));
typedef __bf16 bf16x8 __attribute__((ext_vector_type(8)));
typedef __bf16 bf16x2 __attribute__((ext_vector_type(2)));
typedef short s16x4 __attribute__((ext_vector_type(4)));
#define LAS __attribute__((address_space(3)))
#define DEVI __device__ __forceinline__

constexpr int S_ = 2048, D_ = 640, F_ = 1536;
constexpr unsigned X_BYTES = S_ * D_ * 2, WQ_BYTES = D_ * 3 * D_ * 2, WF_BYTES = D_ * F_ * 2, WO_BYTES = D_ * D_ * 2;
constexpr unsigned QKV_BYTES = S_ * 3 * D_ * 2;
constexpr unsigned AF_BYTES = 128u * 68u * 1024u;
constexpr unsigned WS_QKV = 1u << 20, WS_AF = 16u << 20;
constexpr unsigned LDS_BYTES = 163840;

DEVI i32x4 mk_srd(const void* p, unsigned n) {
  unsigned long long a = (unsigned long long)p;
  i32x4 r;
  r.x = __builtin_amdgcn_readfirstlane((int)(unsigned)a);
  r.y = __builtin_amdgcn_readfirstlane((int)(unsigned)(a >> 32));
  r.z = (int)n; r.w = 0x00020000;
  return r;
}
DEVI i32x4 sel_srd(bool c, i32x4 a, i32x4 b) { i32x4 r; r.x = c ? a.x : b.x; r.y = c ? a.y : b.y; r.z = c ? a.z : b.z; r.w = a.w; return r; }
DEVI void dma16(unsigned ldsaddr, unsigned voff, i32x4 srd, unsigned soff) {
  asm volatile("s_mov_b32 m0, %0\n\ts_nop 0\n\tbuffer_load_dwordx4 %1, %2, %3 offen lds"
               :: "s"(ldsaddr), "v"(voff), "s"(srd), "s"(soff) : "memory");
}
template <int SC> DEVI u32x4 ld16(unsigned voff, i32x4 srd, unsigned soff) {
  u32x4 r;
  if (SC) asm volatile("buffer_load_dwordx4 %0, %1, %2, %3 offen sc1" : "=v"(r) : "v"(voff), "s"(srd), "s"(soff) : "memory");
  else    asm volatile("buffer_load_dwordx4 %0, %1, %2, %3 offen" : "=v"(r) : "v"(voff), "s"(srd), "s"(soff) : "memory");
  return r;
}
template <int SC> DEVI u32x2 ld8(unsigned voff, i32x4 srd, unsigned soff) {
  u32x2 r;
  if (SC) asm volatile("buffer_load_dwordx2 %0, %1, %2, %3 offen sc1" : "=v"(r) : "v"(voff), "s"(srd), "s"(soff) : "memory");
  else    asm volatile("buffer_load_dwordx2 %0, %1, %2, %3 offen" : "=v"(r) : "v"(voff), "s"(srd), "s"(soff) : "memory");
  return r;
}
template <int SC> DEVI void st16(u32x4 v, unsigned voff, i32x4 srd, unsigned soff) {
  if (SC) asm volatile("buffer_store_dwordx4 %0, %1, %2, %3 offen sc1\n\ts_nop 0" :: "v"(v), "v"(voff), "s"(srd), "s"(soff) : "memory");
  else    asm volatile("buffer_store_dwordx4 %0, %1, %2, %3 offen\n\ts_nop 0" :: "v"(v), "v"(voff), "s"(srd), "s"(soff) : "memory");
}
template <int SC> DEVI void st8(u32x2 v, unsigned voff, i32x4 srd, unsigned soff) {
  if (SC) asm volatile("buffer_store_dwordx2 %0, %1, %2, %3 offen sc1" :: "v"(v), "v"(voff), "s"(srd), "s"(soff) : "memory");
  else    asm volatile("buffer_store_dwordx2 %0, %1, %2, %3 offen" :: "v"(v), "v"(voff), "s"(srd), "s"(soff) : "memory");
}
#define WAIT_VM(n) asm volatile("s_waitcnt vmcnt(" #n ")" ::: "memory")
#define WAIT_LGKM0() asm volatile("s_waitcnt lgkmcnt(0)" ::: "memory")
DEVI void bar() { asm volatile("s_barrier" ::: "memory"); }

DEVI unsigned pk2(float a, float b) { bf16x2 v = {(__bf16)a, (__bf16)b}; return __builtin_bit_cast(unsigned, v); }
DEVI float rbf(float a) { return (float)(__bf16)a; }
DEVI bf16x8 asbf(u32x4 v) { return __builtin_bit_cast(bf16x8, v); }
DEVI u32x2 trd(LAS char* p) { return __builtin_bit_cast(u32x2, __builtin_amdgcn_ds_read_tr16_b64_v4i16((LAS s16x4*)p)); }
DEVI u32x4 cat(u32x2 a, u32x2 b) { return __builtin_shufflevector(a, b, 0, 1, 2, 3); }
DEVI f32x4 mfma16(u32x4 a, u32x4 b, f32x4 c) { return __builtin_amdgcn_mfma_f32_16x16x32_bf16(asbf(a), asbf(b), c, 0, 0, 0); }
DEVI f32x16 mfma32(u32x4 a, u32x4 b, f32x16 c) { return __builtin_amdgcn_mfma_f32_32x32x16_bf16(asbf(a), asbf(b), c, 0, 0, 0); }

struct Params {
  const char *x, *wqkv, *wo, *wg, *wu, *wd;
  char *out, *ws;
};

extern __shared__ __attribute__((aligned(16))) char smem_[];

constexpr unsigned G1_W = 0, G1_X = 122880, G1_SP = 155648;

template <int SC>
DEVI void gemm1_phase(const Params& p) {
  LAS char* sm = (LAS char*)smem_;
  const unsigned lb = (unsigned)(size_t)sm;
  const int bid = blockIdx.x, xcd = bid & 7, loc = bid >> 3;
  const int rb = (xcd & 1) * 8 + (loc & 7), cq = xcd >> 1, ci = loc >> 3;
  const int lane = threadIdx.x & 63, wave = __builtin_amdgcn_readfirstlane(threadIdx.x >> 6);
  const int mw = wave >> 2, nw = wave & 3, t16 = lane & 15, g = lane >> 4;
  const bool ftype = ci < 3;
  const int fi_ = cq * 3 + ci;
  const int qb = (cq < 2) ? 12 + 5 * cq : 22 + 4 * (cq - 2) - 1;
  const int u0 = ftype ? fi_ : ((cq < 2) ? qb : qb + 1);
  const i32x4 sx = mk_srd(p.x, X_BYTES), sq = mk_srd(p.wqkv, WQ_BYTES);
  const i32x4 sg = sel_srd(ftype, mk_srd(p.wg, WF_BYTES), sq), su = sel_srd(ftype, mk_srd(p.wu, WF_BYTES), sq);
  const unsigned pf = ftype ? 3072u : 3840u;
  const unsigned c0 = u0 * 128u;
  const unsigned c1 = ftype ? fi_ * 256u : (qb + 1) * 128u, c3 = ftype ? fi_ * 256u : (qb + 3) * 128u;
  const unsigned vx = (rb * 128 + 8 * wave + (lane >> 3)) * 1280u + (((lane & 7) ^ (((wave & 1) << 2) | (lane >> 4))) * 16u);
  const unsigned wcol = (((((lane & 7) >> 1) ^ (((lane >> 4) & 1) | ((wave & 1) << 1))) * 32u) + (lane & 1) * 16u);
  const unsigned vq = (8 * wave + (lane >> 3)) * 3840u + wcol, vf = (8 * wave + (lane >> 3)) * pf + wcol;
  const unsigned ldw = lb + wave * 1024u;

  auto issue_x = [&](int s, int b) { dma16(ldw + G1_X + (s & 1) * 16384u + b * 8192u, vx, sx, b * 81920u + s * 128u); };
  auto issue_w = [&](int s, int b) {
    const unsigned dst = ldw + G1_W + (s % 3) * 40960u + b * 8192u;
    if (b == 0) dma16(dst, vq, sq, s * 64u * 3840u + c0);
    else if (b < 3) dma16(dst, vf, sg, s * 64u * pf + c1 + (b - 1) * 128u);
    else dma16(dst, vf, su, s * 64u * pf + c3 + (b - 3) * 128u);
  };
  issue_x(0, 0); issue_x(0, 1);
#pragma unroll
  for (int b = 0; b < 5; b++) issue_w(0, b);
  issue_x(1, 0); issue_x(1, 1);
#pragma unroll
  for (int b = 0; b < 5; b++) issue_w(1, b);
#pragma unroll
  for (int b = 0; b < 5; b++) issue_w(2, b);

  unsigned xa[4][2];
#pragma unroll
  for (int i = 0; i < 4; i++)
#pragma unroll
    for (int ks = 0; ks < 2; ks++) xa[i][ks] = G1_X + mw * 8192u + (i ^ nw) * 2048u + t16 * 128u + (((4 * ks + g) ^ (t16 >> 1)) * 16u);
  const unsigned fl = ((t16 >> 3) & 1) | ((g & 1) << 1);
  const unsigned wl = g * 1024u + (t16 >> 2) * 128u + (t16 & 3) * 8u;
  unsigned wa[5];
  wa[0] = wl + ((nw ^ fl) * 32u);
  wa[1] = wl + (1 + 2 * mw + (nw >> 1)) * 8192u + (((2 * (nw & 1)) ^ fl) * 32u);
  wa[2] = wa[1] ^ 32u;
  wa[3] = wl + (3 - 2 * mw + (nw >> 1)) * 8192u + (((2 * (nw & 1)) ^ fl) * 32u);
  wa[4] = wa[3] ^ 32u;

  f32x4 acc[4][5], cs[3];
#pragma unroll
  for (int i = 0; i < 4; i++)
#pragma unroll
    for (int u = 0; u < 5; u++) acc[i][u] = f32x4{0, 0, 0, 0};
#pragma unroll
  for (int u = 0; u < 3; u++) cs[u] = f32x4{0, 0, 0, 0};
  float s0 = 0, s1 = 0, q0 = 0, q1 = 0;
  const u32x4 ones = {0x3f803f80u, 0x3f803f80u, 0x3f803f80u, 0x3f803f80u};

  u32x4 xf[2][4], wf[2][5];
  auto rd = [&](int s, int ks) {
#pragma unroll
    for (int i = 0; i < 4; i++) xf[ks][i] = *(LAS u32x4*)(sm + xa[i][ks] + (s & 1) * 16384u);
#pragma unroll
    for (int u = 0; u < 5; u++) {
      LAS char* b = sm + wa[u] + (G1_W + (s % 3) * 40960u + ks * 4096u);
      wf[ks][u] = cat(trd(b), trd(b + 512));
    }
  };
  auto mm = [&](int ks) {
#pragma unroll
    for (int u = 0; u < 5; u++) {
#pragma unroll
      for (int i = 0; i < 4; i++) acc[i][u] = mfma16(wf[ks][u], xf[ks][i], acc[i][u]);
      if (u < 3) cs[u] = mfma16(wf[ks][u], ones, cs[u]);
    }
#pragma unroll
    for (int j = 0; j < 4; j++) {
      const unsigned r = xf[ks][0][j];
      const float lo = __uint_as_float(r << 16), hi = __uint_as_float(r & 0xffff0000u);
      s0 += lo; s1 += hi; q0 = __builtin_fmaf(lo, lo, q0); q1 = __builtin_fmaf(hi, hi, q1);
    }
  };
  WAIT_VM(12);
  bar();
  rd(0, 0);
#pragma unroll
  for (int s = 0; s < 10; s++) {
    rd(s, 1);
    mm(0);
    if (s < 9) {
      WAIT_LGKM0();
      if (s < 8) WAIT_VM(5); else WAIT_VM(0);
      bar();
      if (s + 2 < 10) { issue_x(s + 2, 0); issue_x(s + 2, 1); }
      if (s + 3 < 10) {
#pragma unroll
        for (int b = 0; b < 5; b++) issue_w(s + 3, b);
      }
      rd(s + 1, 0);
    }
    mm(1);
  }
  {
    float sm_ = s0 + s1, sq_ = q0 + q1;
    sm_ += __shfl_xor(sm_, 16); sq_ += __shfl_xor(sq_, 16);
    sm_ += __shfl_xor(sm_, 32); sq_ += __shfl_xor(sq_, 32);
    const float mean = sm_ * (1.0f / 640.0f);
    float var = __builtin_fmaf(-mean, mean, sq_ * (1.0f / 640.0f));
    var = var > 0.f ? var : 0.f;
    const float sd = rbf(__builtin_sqrtf(rbf(rbf(var) + 1e-5f)));
    f32x2 st = {rbf(mean), 1.0f / sd};
    if (g == 0) *(LAS f32x2*)(sm + G1_SP + (mw * 64 + nw * 16 + t16) * 8) = st;
    if (t16 == 0) {
      *(LAS f32x4*)(sm + G1_SP + 1024 + ((nw * 2 + mw) * 2 + 0) * 64 + g * 16) = cs[1];
      *(LAS f32x4*)(sm + G1_SP + 1024 + ((nw * 2 + mw) * 2 + 1) * 64 + g * 16) = cs[2];
    }
  }
  WAIT_LGKM0();
  bar();
  f32x2 st[4];
#pragma unroll
  for (int i = 0; i < 4; i++) st[i] = *(LAS f32x2*)(sm + G1_SP + (mw * 64 + (i ^ nw) * 16 + t16) * 8);
  f32x4 c5[5];
  c5[0] = cs[0]; c5[1] = cs[1]; c5[2] = cs[2];
  c5[3] = *(LAS f32x4*)(sm + G1_SP + 1024 + ((nw * 2 + (mw ^ 1)) * 2 + 0) * 64 + g * 16);
  c5[4] = *(LAS f32x4*)(sm + G1_SP + 1024 + ((nw * 2 + (mw ^ 1)) * 2 + 1) * 64 + g * 16);

  const i32x4 sqkv = mk_srd(p.ws + WS_QKV, QKV_BYTES), saf = mk_srd(p.ws + WS_AF, AF_BYTES);
  const unsigned vl = lane * 16u;
  auto st_qkv = [&](int u, unsigned col) {
    const float qs = col < 640u ? 0.22360679775f * 1.44269504089f : 1.0f;
#pragma unroll
    for (int pr = 0; pr < 2; pr++) {
      unsigned w[2][2];
#pragma unroll
      for (int h = 0; h < 2; h++) {
        const int i = pr * 2 + h;
        const float r = st[i][1] * qs, m = -st[i][0];
        const f32x4 a = acc[i][u], c = c5[u];
        w[h][0] = pk2(__builtin_fmaf(m, c[0], a[0]) * r, __builtin_fmaf(m, c[1], a[1]) * r);
        w[h][1] = pk2(__builtin_fmaf(m, c[2], a[2]) * r, __builtin_fmaf(m, c[3], a[3]) * r);
      }
      const bool sw = nw & 1;
      u32x4 v = {sw ? w[1][0] : w[0][0], sw ? w[1][1] : w[0][1], sw ? w[0][0] : w[1][0], sw ? w[0][1] : w[1][1]};
      const unsigned tb = rb * 4 + mw * 2 + (pr ^ (nw >> 1));
      st16<SC>(v, vl, sqkv, (tb * 480u + (col >> 2)) * 256u);
    }
  };
  auto st_h = [&](int ug, int uu) {
#pragma unroll
    for (int i = 0; i < 4; i++) {
      const float r = st[i][1], m = -st[i][0], rg = -1.44269504089f * r;
      unsigned w[4];
#pragma unroll
      for (int h = 0; h < 2; h++) {
        float hv[4];
#pragma unroll
        for (int j = 0; j < 4; j++) {
          const float e = __builtin_amdgcn_exp2f(__builtin_fmaf(m, c5[ug + h][j], acc[i][ug + h][j]) * rg);
          const float uv = __builtin_fmaf(m, c5[uu + h][j], acc[i][uu + h][j]) * r;
          hv[j] = uv * __builtin_amdgcn_rcpf(1.0f + e);
        }
        w[h * 2] = pk2(hv[0], hv[1]); w[h * 2 + 1] = pk2(hv[2], hv[3]);
      }
      u32x4 v = {w[0], w[1], w[2], w[3]};
      const unsigned tile = rb * 8 + mw * 4 + (i ^ nw);
      st16<SC>(v, vl, saf, (tile * 68u + 4 * fi_ + nw) * 1024u);
    }
  };
  if (ftype) {
    st_qkv(0, u0 * 64u + nw * 16u);
    if (mw == 0) st_h(1, 3); else st_h(3, 1);
  } else {
    if (cq < 2) st_qkv(0, u0 * 64u + nw * 16u);
    const unsigned ca = (qb + 1 + 2 * mw) * 64u + nw * 32u, cb = (qb + 3 - 2 * mw) * 64u + nw * 32u;
    st_qkv(1, ca); st_qkv(2, ca + 16u); st_qkv(3, cb); st_qkv(4, cb + 16u);
  }
}

constexpr unsigned AT_K = 0, AT_V = 20480;

template <int SC>
DEVI void attn_phase(const Params& p) {
  LAS char* sm = (LAS char*)smem_;
  const int bid = blockIdx.x, xcd = bid & 7, loc = bid >> 3;
  const int head = xcd * 4 + (loc & 3), ch = loc >> 2;
  const int lane = threadIdx.x & 63, wave = __builtin_amdgcn_readfirstlane(threadIdx.x >> 6);
  const int t16 = lane & 15, g = lane >> 4, q32 = lane & 31, hi = lane >> 5;
  const i32x4 sqkv = mk_srd(p.ws + WS_QKV, QKV_BYTES), saf = mk_srd(p.ws + WS_AF, AF_BYTES);
  const int tbq = ch * 8 + wave;
  const int kt0 = ch * 8 - 8 + 2 * wave;
  const unsigned kb = (unsigned)(kt0 * 480 + 160 + 5 * head) * 256u;
  const unsigned va = kb + lane * 16u;
  const unsigned vb = va + 122880u;
  const unsigned vt = kb + (g & 1) * 122880u + (g >> 1) * 40960u + 1024u + t16 * 16u;
  u32x4 k0 = ld16<SC>(va, sqkv, 0), k1 = ld16<SC>(vb, sqkv, 0), v0 = ld16<SC>(va, sqkv, 40960u), v1 = ld16<SC>(vb, sqkv, 40960u);
  u32x4 tl = ld16<SC>(vt, sqkv, 0);
  const unsigned qa = (unsigned)(tbq * 480 + 5 * head) * 256u + (q32 & 15) * 16u + (q32 >> 4) * 8u;
  u32x2 qa0 = ld8<SC>(qa + hi * 512u, sqkv, 0), qa1 = ld8<SC>(qa + hi * 512u, sqkv, 256u), qa2 = ld8<SC>(qa, sqkv, 1024u);
  {
    const u32x4 on = {0x3f803f80u, 0x3f803f80u, 0x3f803f80u, 0x3f803f80u};
    if (lane < 32) *(LAS u32x4*)(sm + AT_V + (2 * wave + g) * 1920u + 1600u + t16 * 16u) = on;
  }
  asm volatile("s_waitcnt vmcnt(3)" : "+v"(k0), "+v"(k1), "+v"(v0), "+v"(v1), "+v"(tl) :: "memory");
  *(LAS u32x4*)(sm + AT_K + (2 * wave) * 1280u + lane * 16u) = k0;
  *(LAS u32x4*)(sm + AT_K + (2 * wave + 1) * 1280u + lane * 16u) = k1;
  *(LAS u32x4*)(sm + AT_V + (2 * wave) * 1920u + g * 320u + t16 * 16u) = v0;
  *(LAS u32x4*)(sm + AT_V + (2 * wave + 1) * 1920u + g * 320u + t16 * 16u) = v1;
  {
    const unsigned blk = 2 * wave + (g & 1);
    const unsigned d = (g >> 1) ? AT_V + blk * 1920u + 1280u : AT_K + blk * 1280u + 1024u;
    *(LAS u32x4*)(sm + d + t16 * 16u) = tl;
  }
  asm volatile("s_waitcnt vmcnt(0)" : "+v"(qa0), "+v"(qa1), "+v"(qa2) :: "memory");
  const u32x4 Q1 = cat(qa0, qa1);
  const u32x4 Q2 = {hi ? 0u : qa2[0], hi ? 0u : qa2[1], hi ? 0u : 0x00003f80u, 0u};
  WAIT_LGKM0();
  bar();

  LAS char* kp = sm + AT_K + wave * 1280u + (q32 & 15) * 16u + (q32 >> 4) * 8u;
  LAS char* kp1 = kp + hi * 512u;
  const int vq_ = 4 * (g & 1) + (t16 & 3);
  LAS char* vp = sm + AT_V + wave * 1920u + (vq_ > 5 ? 5 : vq_) * 320u + (4 * (g >> 1) + (t16 >> 2)) * 16u;
  auto sc = [&](int j, f32x16 c) {
    const u32x2 a = *(LAS u32x2*)(kp1 + j * 1280), b = *(LAS u32x2*)(kp1 + j * 1280 + 256), t = *(LAS u32x2*)(kp + j * 1280 + 1024);
    const unsigned mk = (tbq - 8 + j < 0) ? 0x0000fe00u : 0u;
    const u32x4 K2 = {t[0], t[1], mk, 0u};
    c = mfma32(cat(a, b), Q1, c);
    return mfma32(K2, Q2, c);
  };
  const int dl = q32 - 4 * hi;
  f32x16 z;
#pragma unroll
  for (int r = 0; r < 16; r++) z[r] = 0.f;
  float m;
  {
    const f32x16 sa = sc(0, z), sb = sc(8, z);
    m = -3.0e38f;
#pragma unroll
    for (int r = 0; r < 16; r += 2) {
      const float a = ((r & 3) + 8 * (r >> 2) <= dl) ? sb[r] : sa[r];
      const float b = (((r + 1) & 3) + 8 * (r >> 2) <= dl) ? sb[r + 1] : sa[r + 1];
      m = __builtin_fmaxf(__builtin_fmaxf(m, a), b);
    }
#pragma unroll
    for (int j = 1; j < 8; j++) {
      const f32x16 s = sc(j, z);
#pragma unroll
      for (int r = 0; r < 16; r += 2) m = __builtin_fmaxf(__builtin_fmaxf(m, s[r]), s[r + 1]);
    }
    m = __builtin_fmaxf(m, __shfl_xor(m, 32));
  }
  f32x16 nm;
#pragma unroll
  for (int r = 0; r < 16; r++) nm[r] = -m;
  f32x16 o = z;
  auto pv = [&](int j, const unsigned* pk) {
    const u32x4 V1 = cat(trd(vp + j * 1920), trd(vp + j * 1920 + 128)), V2 = cat(trd(vp + j * 1920 + 8), trd(vp + j * 1920 + 136));
    o = mfma32(V1, u32x4{pk[0], pk[1], pk[2], pk[3]}, o);
    o = mfma32(V2, u32x4{pk[4], pk[5], pk[6], pk[7]}, o);
  };
  {
    const f32x16 sa = sc(0, nm), sb = sc(8, nm);
    unsigned pa[8], pb[8];
#pragma unroll
    for (int r = 0; r < 16; r += 2) {
      const bool c0 = (r & 3) + 8 * (r >> 2) <= dl, c1 = ((r + 1) & 3) + 8 * (r >> 2) <= dl;
      const float e0 = __builtin_amdgcn_exp2f(c0 ? sb[r] : sa[r]), e1 = __builtin_amdgcn_exp2f(c1 ? sb[r + 1] : sa[r + 1]);
      const unsigned e = pk2(e0, e1), mk = (c0 ? 0xffffu : 0u) | (c1 ? 0xffff0000u : 0u);
      pb[r >> 1] = e & mk; pa[r >> 1] = e & ~mk;
    }
    pv(0, pa); pv(8, pb);
#pragma unroll
    for (int j = 1; j < 8; j++) {
      const f32x16 s = sc(j, nm);
      unsigned pk[8];
#pragma unroll
      for (int r = 0; r < 16; r += 2) pk[r >> 1] = pk2(__builtin_amdgcn_exp2f(s[r]), __builtin_amdgcn_exp2f(s[r + 1]));
      pv(j, pk);
    }
  }
  const float inv = 1.0f / o[12];
  const unsigned tb_ = (unsigned)(tbq * 2 + (q32 >> 4)) * 68u + 48u;
  auto st_o = [&](int r0, unsigned quad) {
    const unsigned Q = 5 * head + quad;
    const unsigned a = (tb_ + (Q >> 3)) * 1024u + (Q & 3) * 256u + (q32 & 15) * 16u + ((Q >> 2) & 1) * 8u;
    st8<SC>(u32x2{pk2(o[r0] * inv, o[r0 + 1] * inv), pk2(o[r0 + 2] * inv, o[r0 + 3] * inv)}, a, saf, 0);
  };
  st_o(0, hi); st_o(4, 2 + hi);
  if (hi == 0) st_o(8, 4);
}

template <int SC>
DEVI void final_phase(const Params& p) {
  LAS char* sm = (LAS char*)smem_;
  const unsigned lb = (unsigned)(size_t)sm;
  const int bid = blockIdx.x, xcd = bid & 7, loc = bid >> 3;
  const int rgp = xcd & 3, cgp = xcd >> 2;
  const int nrt = rgp < 2 ? 6 : 5, rt0 = rgp < 2 ? 6 * rgp : 12 + 5 * (rgp - 2);
  if (loc >= nrt * 5) return;
  const int cc = loc / nrt, rt = rt0 + (loc - cc * nrt), cg = cgp * 5 + cc;
  const int lane = threadIdx.x & 63, wave = __builtin_amdgcn_readfirstlane(threadIdx.x >> 6);
  const int kq = wave & 3, mh = wave >> 2, t16 = lane & 15, g = lane >> 4;
  const i32x4 saf = mk_srd(p.ws + WS_AF, AF_BYTES), sd = mk_srd(p.wd, WF_BYTES), so = mk_srd(p.wo, WO_BYTES);
  const i32x4 sx = mk_srd(p.x, X_BYTES), sout = mk_srd(p.out, X_BYTES);
  const unsigned vw = (kq * 32 + 16 * mh + (lane >> 3)) * 1280u + cg * 128u + ((((lane & 7) >> 1) ^ (lane >> 4)) * 32u) + (lane & 1) * 16u;
  const unsigned ldw = lb + kq * 4096u + mh * 2048u;
  const int tile0 = rt * 6 + mh * 3;
  unsigned vA[3];
#pragma unroll
  for (int i = 0; i < 3; i++) vA[i] = ((tile0 + i) * 68u + kq) * 1024u + lane * 16u;
  auto issue_w = [&](int r) {
    const unsigned dst = ldw + (r % 9) * 16384u;
    if (r < 12) { dma16(dst, vw, sd, r * 163840u); dma16(dst + 1024u, vw, sd, r * 163840u + 10240u); }
    else { dma16(dst, vw, so, (r - 12) * 163840u); dma16(dst + 1024u, vw, so, (r - 12) * 163840u + 10240u); }
  };
  u32x4 A[17][3];
  auto issue_a = [&](int r) {
#pragma unroll
    for (int i = 0; i < 3; i++) A[r][i] = ld16<SC>(vA[i], saf, r * 4096u);
  };
  unsigned wa[4];
#pragma unroll
  for (int u = 0; u < 4; u++) wa[u] = kq * 4096u + (4 * g + (t16 >> 2)) * 128u + (((u ^ kq) ^ ((2 * g + (t16 >> 3)) & 3)) * 32u) + (t16 & 3) * 8u;
  f32x4 acc[3][4];
#pragma unroll
  for (int i = 0; i < 3; i++)
#pragma unroll
    for (int u = 0; u < 4; u++) acc[i][u] = f32x4{0, 0, 0, 0};
  auto comp = [&](int r) {
#pragma unroll
    for (int u = 0; u < 4; u++) {
      LAS char* b = sm + wa[u] + (r % 9) * 16384u;
      const u32x4 w = cat(trd(b), trd(b + 2048));
#pragma unroll
      for (int i = 0; i < 3; i++) acc[i][u] = mfma16(w, A[r][i], acc[i][u]);
    }
  };
#define PIN3(r) "+v"(A[r][0]), "+v"(A[r][1]), "+v"(A[r][2])
  issue_w(0); issue_w(1); issue_w(2); issue_a(0); issue_a(1); issue_a(2);
  issue_w(3); issue_w(4); issue_w(5); issue_a(3); issue_a(4); issue_a(5);
  issue_w(6); issue_w(7); issue_w(8);
  asm volatile("s_waitcnt vmcnt(21)" : PIN3(0), PIN3(1), PIN3(2) :: "memory");
  bar();
  issue_a(6); issue_a(7); issue_a(8);
  comp(0); comp(1); comp(2);
  asm volatile("s_waitcnt vmcnt(15) lgkmcnt(0)" : PIN3(3), PIN3(4), PIN3(5) :: "memory");
  bar();
  issue_w(9); issue_w(10); issue_w(11); issue_a(9); issue_a(10); issue_a(11);
  comp(3); comp(4); comp(5);
  asm volatile("s_waitcnt vmcnt(15) lgkmcnt(0)" : PIN3(6), PIN3(7), PIN3(8) :: "memory");
  bar();
  issue_w(12); issue_w(13); issue_w(14); issue_a(12); issue_a(13); issue_a(14);
  comp(6); comp(7); comp(8);
  asm volatile("s_waitcnt vmcnt(15) lgkmcnt(0)" : PIN3(9), PIN3(10), PIN3(11) :: "memory");
  bar();
  issue_w(15); issue_w(16); issue_a(15); issue_a(16);
  comp(9); comp(10); comp(11);
  asm volatile("s_waitcnt vmcnt(10) lgkmcnt(0)" : PIN3(12), PIN3(13), PIN3(14) :: "memory");
  bar();
  u32x2 xr[3];
  const unsigned vo = (tile0 * 16 + t16) * 1280u + cg * 128u + kq * 32u + g * 8u;
#pragma unroll
  for (int i = 0; i < 3; i++) xr[i] = ld8<0>(vo + i * 20480u, sx, 0);
  comp(12); comp(13); comp(14);
  asm volatile("s_waitcnt vmcnt(3) lgkmcnt(0)" : PIN3(15), PIN3(16) :: "memory");
  bar();
  comp(15); comp(16);
#pragma unroll
  for (int u = 1; u < 4; u++)
#pragma unroll
    for (int i = 0; i < 3; i++)
      *(LAS f32x4*)(sm + ((mh * 4 + (u ^ kq)) * 9 + (u - 1) * 3 + i) * 1024u + lane * 16u) = acc[i][u];
  WAIT_LGKM0();
  bar();
  asm volatile("s_waitcnt vmcnt(0)" : "+v"(xr[0]), "+v"(xr[1]), "+v"(xr[2]) :: "memory");
#pragma unroll
  for (int i = 0; i < 3; i++) {
    f32x4 a = acc[i][0];
#pragma unroll
    for (int u = 0; u < 3; u++) a += *(LAS f32x4*)(sm + ((mh * 4 + kq) * 9 + u * 3 + i) * 1024u + lane * 16u);
    const float x0 = __uint_as_float(xr[i][0] << 16), x1 = __uint_as_float(xr[i][0] & 0xffff0000u);
    const float x2 = __uint_as_float(xr[i][1] << 16), x3 = __uint_as_float(xr[i][1] & 0xffff0000u);
    st8<0>(u32x2{pk2(a[0] + x0, a[1] + x1), pk2(a[2] + x2, a[3] + x3)}, vo + i * 20480u, sout, 0);
  }
}

__global__ __launch_bounds__(512, 2) void k_gemm1(Params p) { gemm1_phase<0>(p); }
__global__ __launch_bounds__(512, 2) void k_attn(Params p) { attn_phase<0>(p); }
__global__ __launch_bounds__(512, 2) void k_final(Params p) { final_phase<0>(p); }

extern "C" void kernel_launch(void* const* d_in, const int* in_sizes, int n_in, void* d_out, int out_size, void* d_ws,
                              size_t ws_size, hipStream_t stream) {
  Params p;
  p.x = (const char*)d_in[0]; p.wqkv = (const char*)d_in[1]; p.wo = (const char*)d_in[2];
  p.wg = (const char*)d_in[4]; p.wu = (const char*)d_in[5]; p.wd = (const char*)d_in[6];
  p.out = (char*)d_out; p.ws = (char*)d_ws;
  (void)hipFuncSetAttribute((const void*)k_gemm1, hipFuncAttributeMaxDynamicSharedMemorySize, LDS_BYTES);
  (void)hipFuncSetAttribute((const void*)k_attn, hipFuncAttributeMaxDynamicSharedMemorySize, LDS_BYTES);
  (void)hipFuncSetAttribute((const void*)k_final, hipFuncAttributeMaxDynamicSharedMemorySize, LDS_BYTES);
  k_gemm1<<<256, 512, LDS_BYTES, stream>>>(p);
  k_attn<<<256, 512, 51200, stream>>>(p);
  k_final<<<256, 512, 147456, stream>>>(p);
}
```

```cpp
#include <hip/hip_runtime.h>

typedef unsigned short u16;
typedef int i32x4 __attribute__((ext_vector_type(4)));
typedef unsigned u32x4 __attribute__((ext_vector_type(4)));
typedef unsigned u32x2 __attribute__((ext_vector_type(2)));
typedef float f32x4 __attribute__((ext_vector_type(4)));
typedef float f32x2 __attribute__((ext_vector_type(2)));
typedef float f32x16 __attribute__((ext_vector_type(16)));
typedef __bf16 bf16x8 __attribute__((ext_vector_type(8)));
typedef __bf16 bf16x2 __attribute__((ext_vector_type(2)));
typedef short s16x4 __attribute__((ext_vector_type(4)));
#define LAS __attribute__((address_space(3)))
#define DEVI __device__ __forceinline__

constexpr int S_ = 2048, D_ = 640, F_ = 1536;
constexpr unsigned X_BYTES = S_ * D_ * 2, WQ_BYTES = D_ * 3 * D_ * 2, WF_BYTES = D_ * F_ * 2, WO_BYTES = D_ * D_ * 2;
constexpr unsigned QKV_BYTES = S_ * 3 * D_ * 2;
constexpr unsigned AF_BYTES = 128u * 68u * 1024u;
constexpr unsigned WS_QKV = 1u << 20, WS_AF = 16u << 20;
constexpr unsigned LDS_BYTES = 163840;

DEVI i32x4 mk_srd(const void* p, unsigned n) {
  unsigned long long a = (unsigned long long)p;
  i32x4 r;
  r.x = __builtin_amdgcn_readfirstlane((int)(unsigned)a);
  r.y = __builtin_amdgcn_readfirstlane((int)(unsigned)(a >> 32));
  r.z = (int)n; r.w = 0x00020000;
  return r;
}
DEVI i32x4 sel_srd(bool c, i32x4 a, i32x4 b) { i32x4 r; r.x = c ? a.x : b.x; r.y = c ? a.y : b.y; r.z = c ? a.z : b.z; r.w = a.w; return r; }
DEVI void dma16(unsigned ldsaddr, unsigned voff, i32x4 srd, unsigned soff) {
  asm volatile("s_mov_b32 m0, %0\n\ts_nop 0\n\tbuffer_load_dwordx4 %1, %2, %3 offen lds"
               :: "s"(ldsaddr), "v"(voff), "s"(srd), "s"(soff) : "memory");
}
template <int SC> DEVI u32x4 ld16(unsigned voff, i32x4 srd, unsigned soff) {
  u32x4 r;
  if (SC) asm volatile("buffer_load_dwordx4 %0, %1, %2, %3 offen sc1" : "=v"(r) : "v"(voff), "s"(srd), "s"(soff) : "memory");
  else    asm volatile("buffer_load_dwordx4 %0, %1, %2, %3 offen" : "=v"(r) : "v"(voff), "s"(srd), "s"(soff) : "memory");
  return r;
}
template <int SC> DEVI u32x2 ld8(unsigned voff, i32x4 srd, unsigned soff) {
  u32x2 r;
  if (SC) asm volatile("buffer_load_dwordx2 %0, %1, %2, %3 offen sc1" : "=v"(r) : "v"(voff), "s"(srd), "s"(soff) : "memory");
  else    asm volatile("buffer_load_dwordx2 %0, %1, %2, %3 offen" : "=v"(r) : "v"(voff), "s"(srd), "s"(soff) : "memory");
  return r;
}
template <int SC> DEVI void st16(u32x4 v, unsigned voff, i32x4 srd, unsigned soff) {
  if (SC) asm volatile("buffer_store_dwordx4 %0, %1, %2, %3 offen sc1\n\ts_nop 0" :: "v"(v), "v"(voff), "s"(srd), "s"(soff) : "memory");
  else    asm volatile("buffer_store_dwordx4 %0, %1, %2, %3 offen\n\ts_nop 0" :: "v"(v), "v"(voff), "s"(srd), "s"(soff) : "memory");
}
template <int SC> DEVI void st8(u32x2 v, unsigned voff, i32x4 srd, unsigned soff) {
  if (SC) asm volatile("buffer_store_dwordx2 %0, %1, %2, %3 offen sc1" :: "v"(v), "v"(voff), "s"(srd), "s"(soff) : "memory");
  else    asm volatile("buffer_store_dwordx2 %0, %1, %2, %3 offen" :: "v"(v), "v"(voff), "s"(srd), "s"(soff) : "memory");
}
#define WAIT_VM(n) asm volatile("s_waitcnt vmcnt(" #n ")" ::: "memory")
#define WAIT_LGKM0() asm volatile("s_waitcnt lgkmcnt(0)" ::: "memory")
DEVI void bar() { asm volatile("s_barrier" ::: "memory"); }

DEVI unsigned pk2(float a, float b) { bf16x2 v = {(__bf16)a, (__bf16)b}; return __builtin_bit_cast(unsigned, v); }
DEVI float rbf(float a) { return (float)(__bf16)a; }
DEVI bf16x8 asbf(u32x4 v) { return __builtin_bit_cast(bf16x8, v); }
DEVI u32x2 trd(LAS char* p) { return __builtin_bit_cast(u32x2, __builtin_amdgcn_ds_read_tr16_b64_v4i16((LAS s16x4*)p)); }
DEVI u32x4 cat(u32x2 a, u32x2 b) { return __builtin_shufflevector(a, b, 0, 1, 2, 3); }
DEVI f32x4 mfma16(u32x4 a, u32x4 b, f32x4 c) { return __builtin_amdgcn_mfma_f32_16x16x32_bf16(asbf(a), asbf(b), c, 0, 0, 0); }
DEVI f32x16 mfma32(u32x4 a, u32x4 b, f32x16 c) { return __builtin_amdgcn_mfma_f32_32x32x16_bf16(asbf(a), asbf(b), c, 0, 0, 0); }

#define TS(i)
#define TSDECL
#define TSOUT(ph)
template <int I> struct IC { static constexpr int v = I; };
template <int B, int E, class F> DEVI void sfor(F&& f) { if constexpr (B < E) { f(IC<B>{}); sfor<B + 1, E>(f); } }

struct Params {
  const char *x, *wqkv, *wo, *wg, *wu, *wd;
  char *out, *ws;
};

extern __shared__ __attribute__((aligned(16))) char smem_[];

constexpr unsigned G1_W = 0, G1_X = 122880, G1_SP = 155648;

template <int SC>
DEVI void gemm1_phase(const Params& p) {
  TSDECL;
  LAS char* sm = (LAS char*)smem_;
  const unsigned lb = (unsigned)(size_t)sm;
  const int bid = blockIdx.x, xcd = bid & 7, loc = bid >> 3;
  const int rb = (xcd & 1) * 8 + (loc & 7), cq = xcd >> 1, ci = loc >> 3;
  const int lane = threadIdx.x & 63, wave = __builtin_amdgcn_readfirstlane(threadIdx.x >> 6);
  const int mw = wave >> 2, nw = wave & 3, t16 = lane & 15, g = lane >> 4;
  const bool ftype = ci < 3;
  const int fi_ = cq * 3 + ci;
  const int qb = (cq < 2) ? 12 + 5 * cq : 22 + 4 * (cq - 2) - 1;
  const int u0 = ftype ? fi_ : ((cq < 2) ? qb : qb + 1);
  const i32x4 sx = mk_srd(p.x, X_BYTES), sq = mk_srd(p.wqkv, WQ_BYTES);
  const i32x4 sg = sel_srd(ftype, mk_srd(p.wg, WF_BYTES), sq), su = sel_srd(ftype, mk_srd(p.wu, WF_BYTES), sq);
  const unsigned pf = ftype ? 3072u : 3840u, pf64 = pf * 64u;
  const unsigned c0 = u0 * 128u;
  const unsigned c1 = ftype ? fi_ * 256u : (qb + 1) * 128u, c3 = ftype ? fi_ * 256u : (qb + 3) * 128u;
  const unsigned vx = (rb * 128 + 8 * wave + (lane >> 3)) * 1280u + (((lane & 7) ^ (((wave & 1) << 2) | (lane >> 4))) * 16u);
  const unsigned wcol = (((((lane & 7) >> 1) ^ (((lane >> 4) & 1) | ((wave & 1) << 1))) * 32u) + (lane & 1) * 16u);
  const unsigned vq = (8 * wave + (lane >> 3)) * 3840u + wcol, vf = (8 * wave + (lane >> 3)) * pf + wcol;
  const unsigned ldw = lb + wave * 1024u;

#define ROTM 0
  const int rot = ROTM == 0 ? 0 : ROTM == 1 ? __builtin_amdgcn_readfirstlane(((loc & 7) + 3 * ci) % 10) : __builtin_amdgcn_readfirstlane((((loc & 7) + ci) % ROTM) * (10 / ROTM));
  auto sidx = [&](int s) { return (unsigned)(s + rot >= 10 ? s + rot - 10 : s + rot); };
  auto issue_x = [&](int s, int b) { dma16(ldw + G1_X + (s & 1) * 16384u + b * 8192u, vx, sx, b * 81920u + sidx(s) * 128u); };
  auto issue_w = [&](int s, int b) {
    const unsigned dst = ldw + G1_W + (s % 3) * 40960u + b * 8192u;
    if (b == 0) dma16(dst, vq, sq, sidx(s) * (64u * 3840u) + c0);
    else if (b < 3) dma16(dst, vf, sg, sidx(s) * pf64 + c1 + (b - 1) * 128u);
    else dma16(dst, vf, su, sidx(s) * pf64 + c3 + (b - 3) * 128u);
  };
  issue_x(0, 0); issue_x(0, 1);
#pragma unroll
  for (int b = 0; b < 5; b++) issue_w(0, b);
  issue_x(1, 0); issue_x(1, 1);
#pragma unroll
  for (int b = 0; b < 5; b++) issue_w(1, b);

  LAS char* xa[4][2];
#pragma unroll
  for (int i = 0; i < 4; i++)
#pragma unroll
    for (int ks = 0; ks < 2; ks++) xa[i][ks] = sm + (G1_X + mw * 8192u + (i ^ nw) * 2048u + t16 * 128u + (((4 * ks + g) ^ (t16 >> 1)) * 16u));
  const unsigned fl = ((t16 >> 3) & 1) | ((g & 1) << 1);
  const unsigned wl = g * 1024u + (t16 >> 2) * 128u + (t16 & 3) * 8u;
  LAS char* wa[2][5];
  {
    const unsigned w0 = wl + ((nw ^ fl) * 32u);
    const unsigned w1 = wl + (1 + 2 * mw + (nw >> 1)) * 8192u + (((2 * (nw & 1)) ^ fl) * 32u);
    const unsigned w3 = wl + (3 - 2 * mw + (nw >> 1)) * 8192u + (((2 * (nw & 1)) ^ fl) * 32u);
    wa[0][0] = sm + w0; wa[0][1] = sm + w1; wa[0][2] = sm + (w1 ^ 32u); wa[0][3] = sm + w3; wa[0][4] = sm + (w3 ^ 32u);
#pragma unroll
    for (int u = 0; u < 5; u++) { wa[1][u] = wa[0][u] + 81920; asm volatile("" : "+v"(wa[1][u])); }
  }

  f32x4 acc[4][5], cs[3];
#pragma unroll
  for (int i = 0; i < 4; i++)
#pragma unroll
    for (int u = 0; u < 5; u++) acc[i][u] = f32x4{0, 0, 0, 0};
#pragma unroll
  for (int u = 0; u < 3; u++) cs[u] = f32x4{0, 0, 0, 0};
  f32x2 ss = {0, 0}, qq = {0, 0}, un;
  u32x4 ones = {0x3f803f80u, 0x3f803f80u, 0x3f803f80u, 0x3f803f80u};
  asm volatile("" : "+v"(ones));

  u32x4 xf[2][4];
  u32x2 wf[2][5][2];
  auto rd = [&](int s, int ks, int r) {
    if (r < 4) xf[ks][r] = *(LAS u32x4*)(xa[r][ks] + (s & 1) * 16384);
    else {
      const int u = (r - 4) >> 1, h = r & 1, sl = s % 3;
      wf[ks][u][h] = trd(wa[sl == 2][u] + ((sl == 1) * 40960 + ks * 4096 + h * 512));
    }
  };
  auto stat = [&](int ks, int t) {
    const unsigned r = xf[ks][0][t >> 2];
    if ((t & 3) == 0) un[0] = __uint_as_float(r << 16);
    else if ((t & 3) == 1) un[1] = __uint_as_float(r & 0xffff0000u);
    else if ((t & 3) == 2) ss += un;
    else qq = __builtin_elementwise_fma(un, un, qq);
  };
  auto mf = [&](int ks, int n) {
    const int u = n < 15 ? n / 5 : 3 + (n - 15) / 4, i = n < 15 ? n % 5 : (n - 15) % 4;
    const u32x4 w = cat(wf[ks][u][0], wf[ks][u][1]);
    if (i < 4) acc[i][u] = mfma16(w, xf[ks][i], acc[i][u]);
    else cs[u] = mfma16(w, ones, cs[u]);
  };
#define NORD 0
#define NOST 0
#define NODMA 0
#define SB() __builtin_amdgcn_sched_barrier(0)
  TS(1);
  WAIT_VM(7);
  bar();
  TS(2);
#pragma unroll
  for (int r = 0; r < 14; r++) { rd(0, 0, r); if (NORD) rd(0, 1, r); }
  SB();
  sfor<0, 10>([&](auto S_) {
    constexpr int s = S_.v;
#pragma unroll
    for (int n = 0; n < 23; n++) {
      mf(0, n);
      if (!NORD && n < 14) rd(s, 1, n);
      if (!NOST && n >= 4 && n < 20) stat(0, n - 4);
      if (NODMA) {} else if (s == 0) { if (n == 1 || n == 5 || n == 10 || n == 14 || n == 19) issue_w(2, n / 4); }
      else if (s + 2 < 10) { if (n == 3 || n == 10 || n == 17) issue_w(s + 2, 2 + n / 7); }
      SB();
    }
    if (s < 9) {
      WAIT_LGKM0();
      if (s < 8) WAIT_VM(5); else WAIT_VM(0);
      bar();
      TS(3 + s);
      SB();
#pragma unroll
      for (int n = 0; n < 23; n++) {
        mf(1, n);
        if (!NORD && n < 14) rd(s + 1, 0, n);
        if (!NOST && n >= 4 && n < 20) stat(1, n - 4);
        if (!NODMA && s + 2 < 10) { if (n == 1 || n == 7) issue_x(s + 2, n / 7); }
        if (!NODMA && s + 3 < 10) { if (n == 13 || n == 19) issue_w(s + 3, n / 19); }
        SB();
      }
    }
  });
  f32x4 st[4], c5[5];
  {
    mf(1, 4); stat(1, 0); stat(1, 1); stat(1, 2); SB();
    mf(1, 9); stat(1, 3); stat(1, 4); stat(1, 5); SB();
    mf(1, 14); stat(1, 6); stat(1, 7); stat(1, 8); SB();
    mf(1, 0); stat(1, 9); stat(1, 10); stat(1, 11); SB();
    mf(1, 1); stat(1, 12); stat(1, 13); stat(1, 14); SB();
    mf(1, 2); stat(1, 15);
    float a = ss[0] + ss[1], b = qq[0] + qq[1];
    SB();
    mf(1, 3);
    auto r1 = __builtin_amdgcn_permlane32_swap(__float_as_uint(a), __float_as_uint(b), false, false);
    float c = __uint_as_float(r1[0]) + __uint_as_float(r1[1]);
    SB();
    mf(1, 5);
    auto r2 = __builtin_amdgcn_permlane16_swap(__float_as_uint(c), __float_as_uint(c), false, false);
    c = __uint_as_float(r2[0]) + __uint_as_float(r2[1]);
    SB();
    mf(1, 6);
    auto r3 = __builtin_amdgcn_permlane32_swap(__float_as_uint(c), __float_as_uint(c), false, false);
    const float mean = __uint_as_float(r3[0]) * (1.0f / 640.0f);
    float var = __builtin_fmaf(-mean, mean, __uint_as_float(r3[1]) * (1.0f / 640.0f));
    SB();
    mf(1, 7);
    if (t16 == 0) {
      *(LAS f32x4*)(sm + G1_SP + 2048 + ((nw * 2 + mw) * 2 + 0) * 64 + g * 16) = cs[1];
      *(LAS f32x4*)(sm + G1_SP + 2048 + ((nw * 2 + mw) * 2 + 1) * 64 + g * 16) = cs[2];
    }
    var = var > 0.f ? var : 0.f;
    SB();
    mf(1, 8);
    const float sd = rbf(__builtin_amdgcn_sqrtf(rbf(rbf(var) + 1e-5f)));
    SB();
    mf(1, 10);
    const float rs = __builtin_amdgcn_rcpf(sd);
    const f32x4 sv = {-rbf(mean), rs, sd, -1.44269504089f * rs};
    SB();
    mf(1, 11);
    if (g == 0) *(LAS f32x4*)(sm + G1_SP + (mw * 64 + nw * 16 + t16) * 16) = sv;
    SB();
    mf(1, 12); mf(1, 13);
    SB();
    WAIT_LGKM0();
    bar();
    SB();
    mf(1, 15);
#pragma unroll
    for (int i = 0; i < 4; i++) st[i] = *(LAS f32x4*)(sm + G1_SP + (mw * 64 + (i ^ nw) * 16 + t16) * 16);
    c5[3] = *(LAS f32x4*)(sm + G1_SP + 2048 + ((nw * 2 + (mw ^ 1)) * 2 + 0) * 64 + g * 16);
    c5[4] = *(LAS f32x4*)(sm + G1_SP + 2048 + ((nw * 2 + (mw ^ 1)) * 2 + 1) * 64 + g * 16);
    SB();
#pragma unroll
    for (int n = 16; n < 23; n++) mf(1, n);
    SB();
  }
  c5[0] = cs[0]; c5[1] = cs[1]; c5[2] = cs[2];
  TS(12);

  const i32x4 sqkv = mk_srd(p.ws + WS_QKV, QKV_BYTES), saf = mk_srd(p.ws + WS_AF, AF_BYTES);
  const unsigned vl = lane * 16u;
  auto lo2 = [](f32x4 v) { return f32x2{v[0], v[1]}; };
  auto hi2 = [](f32x4 v) { return f32x2{v[2], v[3]}; };
  auto st_qkv = [&](int u, unsigned col) {
    const float qs = col < 640u ? 0.22360679775f * 1.44269504089f : 1.0f;
#pragma unroll
    for (int pr = 0; pr < 2; pr++) {
      unsigned w[2][2];
#pragma unroll
      for (int h = 0; h < 2; h++) {
        const int i = pr * 2 + h;
        const float r = st[i][1] * qs;
        const f32x2 m2 = {st[i][0], st[i][0]}, r2 = {r, r};
        const f32x2 a = __builtin_elementwise_fma(m2, lo2(c5[u]), lo2(acc[i][u])) * r2;
        const f32x2 b = __builtin_elementwise_fma(m2, hi2(c5[u]), hi2(acc[i][u])) * r2;
        w[h][0] = pk2(a[0], a[1]); w[h][1] = pk2(b[0], b[1]);
      }
      const bool sw = nw & 1;
      u32x4 v = {sw ? w[1][0] : w[0][0], sw ? w[1][1] : w[0][1], sw ? w[0][0] : w[1][0], sw ? w[0][1] : w[1][1]};
      const unsigned tb = rb * 4 + mw * 2 + (pr ^ (nw >> 1));
      st16<SC>(v, vl, sqkv, (tb * 480u + (col >> 2)) * 256u);
    }
  };
  auto st_h = [&](int ug, int uu) {
#pragma unroll
    for (int i = 0; i < 4; i++) {
      const f32x2 m2 = {st[i][0], st[i][0]}, sd2 = {st[i][2], st[i][2]}, rg2 = {st[i][3], st[i][3]};
      unsigned w[4];
#pragma unroll
      for (int q = 0; q < 4; q++) {
        const int h = q >> 1;
        const f32x2 ag = (q & 1) ? hi2(acc[i][ug + h]) : lo2(acc[i][ug + h]), cg = (q & 1) ? hi2(c5[ug + h]) : lo2(c5[ug + h]);
        const f32x2 au = (q & 1) ? hi2(acc[i][uu + h]) : lo2(acc[i][uu + h]), cu = (q & 1) ? hi2(c5[uu + h]) : lo2(c5[uu + h]);
        const f32x2 t = __builtin_elementwise_fma(m2, cg, ag) * rg2;
        const f32x2 e = {__builtin_amdgcn_exp2f(t[0]), __builtin_amdgcn_exp2f(t[1])};
        const f32x2 d = __builtin_elementwise_fma(e, sd2, sd2);
        const f32x2 rc = {__builtin_amdgcn_rcpf(d[0]), __builtin_amdgcn_rcpf(d[1])};
        const f32x2 hv = __builtin_elementwise_fma(m2, cu, au) * rc;
        w[q] = pk2(hv[0], hv[1]);
      }
      u32x4 v = {w[0], w[1], w[2], w[3]};
      const unsigned tile = rb * 8 + mw * 4 + (i ^ nw);
      st16<SC>(v, vl, saf, (tile * 68u + 4 * fi_ + nw) * 1024u);
    }
  };
  if (ftype) {
    st_qkv(0, u0 * 64u + nw * 16u);
    if (mw == 0) st_h(1, 3); else st_h(3, 1);
  } else {
    if (cq < 2) st_qkv(0, u0 * 64u + nw * 16u);
    const unsigned ca = (qb + 1 + 2 * mw) * 64u + nw * 32u, cb = (qb + 3 - 2 * mw) * 64u + nw * 32u;
    st_qkv(1, ca); st_qkv(2, ca + 16u); st_qkv(3, cb); st_qkv(4, cb + 16u);
  }
  TSOUT(0);
}

constexpr unsigned AT_K = 0, AT_V = 20480;

template <int SC>
DEVI void attn_phase(const Params& p) {
  TSDECL;
  LAS char* sm = (LAS char*)smem_;
  const int bid = blockIdx.x, xcd = bid & 7, loc = bid >> 3;
  const int head = xcd * 4 + (loc & 3), ch = loc >> 2;
  const int lane = threadIdx.x & 63, wave = __builtin_amdgcn_readfirstlane(threadIdx.x >> 6);
  const int t16 = lane & 15, g = lane >> 4, q32 = lane & 31, hi = lane >> 5;
  const i32x4 sqkv = mk_srd(p.ws + WS_QKV, QKV_BYTES), saf = mk_srd(p.ws + WS_AF, AF_BYTES);
  const int tbq = ch * 8 + wave;
  const int kt0 = ch * 8 - 8 + 2 * wave;
  const unsigned kb = (unsigned)(kt0 * 480 + 160 + 5 * head) * 256u;
  const unsigned va = kb + lane * 16u;
  const unsigned vb = va + 122880u;
  const unsigned vt = kb + (g & 1) * 122880u + (g >> 1) * 40960u + 1024u + t16 * 16u;
  u32x4 k0 = ld16<SC>(va, sqkv, 0), k1 = ld16<SC>(vb, sqkv, 0), v0 = ld16<SC>(va, sqkv, 40960u), v1 = ld16<SC>(vb, sqkv, 40960u);
  u32x4 tl = ld16<SC>(vt, sqkv, 0);
  const unsigned qa = (unsigned)(tbq * 480 + 5 * head) * 256u + (q32 & 15) * 16u + (q32 >> 4) * 8u;
  u32x2 qa0 = ld8<SC>(qa + hi * 512u, sqkv, 0), qa1 = ld8<SC>(qa + hi * 512u, sqkv, 256u), qa2 = ld8<SC>(qa, sqkv, 1024u);
  {
    const u32x4 on = {0x3f803f80u, 0x3f803f80u, 0x3f803f80u, 0x3f803f80u};
    if (lane < 32) *(LAS u32x4*)(sm + AT_V + (2 * wave + g) * 1920u + 1600u + t16 * 16u) = on;
  }
  asm volatile("s_waitcnt vmcnt(3)" : "+v"(k0), "+v"(k1), "+v"(v0), "+v"(v1), "+v"(tl) :: "memory");
  *(LAS u32x4*)(sm + AT_K + (2 * wave) * 1280u + lane * 16u) = k0;
  *(LAS u32x4*)(sm + AT_K + (2 * wave + 1) * 1280u + lane * 16u) = k1;
  *(LAS u32x4*)(sm + AT_V + (2 * wave) * 1920u + g * 320u + t16 * 16u) = v0;
  *(LAS u32x4*)(sm + AT_V + (2 * wave + 1) * 1920u + g * 320u + t16 * 16u) = v1;
  {
    const unsigned blk = 2 * wave + (g & 1);
    const unsigned d = (g >> 1) ? AT_V + blk * 1920u + 1280u : AT_K + blk * 1280u + 1024u;
    *(LAS u32x4*)(sm + d + t16 * 16u) = tl;
  }
  asm volatile("s_waitcnt vmcnt(0)" : "+v"(qa0), "+v"(qa1), "+v"(qa2) :: "memory");
  const u32x4 Q1 = cat(qa0, qa1);
  const u32x4 Q2 = {hi ? 0u : qa2[0], hi ? 0u : qa2[1], hi ? 0u : 0x00003f80u, 0u};
  WAIT_LGKM0();
  TS(1);
  bar();
  TS(2);

  LAS char* kp = sm + AT_K + wave * 1280u + (q32 & 15) * 16u + (q32 >> 4) * 8u;
  LAS char* kp1 = kp + hi * 512u;
  const int vq_ = 4 * (g & 1) + (t16 & 3);
  LAS char* vp = sm + AT_V + wave * 1920u + (vq_ > 5 ? 5 : vq_) * 320u + (4 * (g >> 1) + (t16 >> 2)) * 16u;
  auto sc = [&](int j, f32x16 c) {
    const u32x2 a = *(LAS u32x2*)(kp1 + j * 1280), b = *(LAS u32x2*)(kp1 + j * 1280 + 256), t = *(LAS u32x2*)(kp + j * 1280 + 1024);
    const unsigned mk = (tbq - 8 + j < 0) ? 0x0000fe00u : 0u;
    const u32x4 K2 = {t[0], t[1], mk, 0u};
    c = mfma32(cat(a, b), Q1, c);
    return mfma32(K2, Q2, c);
  };
  const int dl = q32 - 4 * hi;
  f32x16 z;
#pragma unroll
  for (int r = 0; r < 16; r++) z[r] = 0.f;
  float m;
  {
    const f32x16 sa = sc(0, z), sb = sc(8, z);
    m = -3.0e38f;
#pragma unroll
    for (int r = 0; r < 16; r += 2) {
      const float a = ((r & 3) + 8 * (r >> 2) <= dl) ? sb[r] : sa[r];
      const float b = (((r + 1) & 3) + 8 * (r >> 2) <= dl) ? sb[r + 1] : sa[r + 1];
      m = __builtin_fmaxf(__builtin_fmaxf(m, a), b);
    }
#pragma unroll
    for (int j = 1; j < 8; j++) {
      const f32x16 s = sc(j, z);
#pragma unroll
      for (int r = 0; r < 16; r += 2) m = __builtin_fmaxf(__builtin_fmaxf(m, s[r]), s[r + 1]);
    }
    m = __builtin_fmaxf(m, __shfl_xor(m, 32));
  }
  TS(3);
  f32x16 nm;
#pragma unroll
  for (int r = 0; r < 16; r++) nm[r] = -m;
  f32x16 o = z;
  auto pv = [&](int j, const unsigned* pk) {
    const u32x4 V1 = cat(trd(vp + j * 1920), trd(vp + j * 1920 + 128)), V2 = cat(trd(vp + j * 1920 + 8), trd(vp + j * 1920 + 136));
    o = mfma32(V1, u32x4{pk[0], pk[1], pk[2], pk[3]}, o);
    o = mfma32(V2, u32x4{pk[4], pk[5], pk[6], pk[7]}, o);
  };
  {
    const f32x16 sa = sc(0, nm), sb = sc(8, nm);
    unsigned pa[8], pb[8];
#pragma unroll
    for (int r = 0; r < 16; r += 2) {
      const bool c0 = (r & 3) + 8 * (r >> 2) <= dl, c1 = ((r + 1) & 3) + 8 * (r >> 2) <= dl;
      const float e0 = __builtin_amdgcn_exp2f(c0 ? sb[r] : sa[r]), e1 = __builtin_amdgcn_exp2f(c1 ? sb[r + 1] : sa[r + 1]);
      const unsigned e = pk2(e0, e1), mk = (c0 ? 0xffffu : 0u) | (c1 ? 0xffff0000u : 0u);
      pb[r >> 1] = e & mk; pa[r >> 1] = e & ~mk;
    }
    pv(0, pa); pv(8, pb);
#pragma unroll
    for (int j = 1; j < 8; j++) {
      const f32x16 s = sc(j, nm);
      unsigned pk[8];
#pragma unroll
      for (int r = 0; r < 16; r += 2) pk[r >> 1] = pk2(__builtin_amdgcn_exp2f(s[r]), __builtin_amdgcn_exp2f(s[r + 1]));
      pv(j, pk);
    }
  }
  TS(4);
  const float inv = 1.0f / o[12];
  const unsigned tb_ = (unsigned)(tbq * 2 + (q32 >> 4)) * 68u + 48u;
  auto st_o = [&](int r0, unsigned quad) {
    const unsigned Q = 5 * head + quad;
    const unsigned a = (tb_ + (Q >> 3)) * 1024u + (Q & 3) * 256u + (q32 & 15) * 16u + ((Q >> 2) & 1) * 8u;
    st8<SC>(u32x2{pk2(o[r0] * inv, o[r0 + 1] * inv), pk2(o[r0 + 2] * inv, o[r0 + 3] * inv)}, a, saf, 0);
  };
  st_o(0, hi); st_o(4, 2 + hi);
  if (hi == 0) st_o(8, 4);
  TSOUT(1);
}

template <int SC>
DEVI void final_phase(const Params& p) {
  TSDECL;
  LAS char* sm = (LAS char*)smem_;
  const unsigned lb = (unsigned)(size_t)sm;
  const int bid = blockIdx.x, xcd = bid & 7, loc = bid >> 3;
  const int rgp = xcd & 3, cgp = xcd >> 2;
  const int nrt = rgp < 2 ? 6 : 5, rt0 = rgp < 2 ? 6 * rgp : 12 + 5 * (rgp - 2);
  if (loc >= nrt * 5) return;
  const int cc = loc / nrt, rt = rt0 + (loc - cc * nrt), cg = cgp * 5 + cc;
  const int lane = threadIdx.x & 63, wave = __builtin_amdgcn_readfirstlane(threadIdx.x >> 6);
  const int kq = wave & 3, mh = wave >> 2, t16 = lane & 15, g = lane >> 4;
  const i32x4 saf = mk_srd(p.ws + WS_AF, AF_BYTES), sd = mk_srd(p.wd, WF_BYTES), so = mk_srd(p.wo, WO_BYTES);
  const i32x4 sx = mk_srd(p.x, X_BYTES), sout = mk_srd(p.out, X_BYTES);
  const unsigned vw = (kq * 32 + 16 * mh + (lane >> 3)) * 1280u + cg * 128u + ((((lane & 7) >> 1) ^ (lane >> 4)) * 32u) + (lane & 1) * 16u;
  const unsigned ldw = lb + kq * 4096u + mh * 2048u;
  const int tile0 = rt * 6 + mh * 3;
  unsigned vA[3];
#pragma unroll
  for (int i = 0; i < 3; i++) vA[i] = ((tile0 + i) * 68u + kq) * 1024u + lane * 16u;
  auto issue_w = [&](int r) {
    const unsigned dst = ldw + (r % 9) * 16384u;
    if (r < 12) { dma16(dst, vw, sd, r * 163840u); dma16(dst + 1024u, vw, sd, r * 163840u + 10240u); }
    else { dma16(dst, vw, so, (r - 12) * 163840u); dma16(dst + 1024u, vw, so, (r - 12) * 163840u + 10240u); }
  };
  u32x4 A[17][3];
  auto issue_a = [&](int r) {
#pragma unroll
    for (int i = 0; i < 3; i++) A[r][i] = ld16<SC>(vA[i], saf, r * 4096u);
  };
  unsigned wa[4];
#pragma unroll
  for (int u = 0; u < 4; u++) wa[u] = kq * 4096u + (4 * g + (t16 >> 2)) * 128u + (((u ^ kq) ^ ((2 * g + (t16 >> 3)) & 3)) * 32u) + (t16 & 3) * 8u;
  f32x4 acc[3][4];
#pragma unroll
  for (int i = 0; i < 3; i++)
#pragma unroll
    for (int u = 0; u < 4; u++) acc[i][u] = f32x4{0, 0, 0, 0};
  auto comp = [&](int r) {
#pragma unroll
    for (int u = 0; u < 4; u++) {
      LAS char* b = sm + wa[u] + (r % 9) * 16384u;
      const u32x4 w = cat(trd(b), trd(b + 2048));
#pragma unroll
      for (int i = 0; i < 3; i++) acc[i][u] = mfma16(w, A[r][i], acc[i][u]);
    }
  };
#define PIN3(r) "+v"(A[r][0]), "+v"(A[r][1]), "+v"(A[r][2])
  issue_w(0); issue_w(1); issue_w(2); issue_a(0); issue_a(1); issue_a(2);
  issue_w(3); issue_w(4); issue_w(5); issue_a(3); issue_a(4); issue_a(5);
  issue_w(6); issue_w(7); issue_w(8);
  TS(1);
  asm volatile("s_waitcnt vmcnt(21)" : PIN3(0), PIN3(1), PIN3(2) :: "memory");
  bar();
  TS(2);
  issue_a(6); issue_a(7); issue_a(8);
  comp(0); comp(1); comp(2);
  asm volatile("s_waitcnt vmcnt(15) lgkmcnt(0)" : PIN3(3), PIN3(4), PIN3(5) :: "memory");
  bar();
  TS(3);
  issue_w(9); issue_w(10); issue_w(11); issue_a(9); issue_a(10); issue_a(11);
  comp(3); comp(4); comp(5);
  asm volatile("s_waitcnt vmcnt(15) lgkmcnt(0)" : PIN3(6), PIN3(7), PIN3(8) :: "memory");
  bar();
  TS(4);
  issue_w(12); issue_w(13); issue_w(14); issue_a(12); issue_a(13); issue_a(14);
  comp(6); comp(7); comp(8);
  asm volatile("s_waitcnt vmcnt(15) lgkmcnt(0)" : PIN3(9), PIN3(10), PIN3(11) :: "memory");
  bar();
  TS(5);
  issue_w(15); issue_w(16); issue_a(15); issue_a(16);
  comp(9); comp(10); comp(11);
  asm volatile("s_waitcnt vmcnt(10) lgkmcnt(0)" : PIN3(12), PIN3(13), PIN3(14) :: "memory");
  bar();
  TS(6);
  u32x2 xr[3];
  const unsigned vo = (tile0 * 16 + t16) * 1280u + cg * 128u + kq * 32u + g * 8u;
#pragma unroll
  for (int i = 0; i < 3; i++) xr[i] = ld8<0>(vo + i * 20480u, sx, 0);
  comp(12); comp(13); comp(14);
  asm volatile("s_waitcnt vmcnt(3) lgkmcnt(0)" : PIN3(15), PIN3(16) :: "memory");
  bar();
  TS(7);
  comp(15); comp(16);
#pragma unroll
  for (int u = 1; u < 4; u++)
#pragma unroll
    for (int i = 0; i < 3; i++)
      *(LAS f32x4*)(sm + ((mh * 4 + (u ^ kq)) * 9 + (u - 1) * 3 + i) * 1024u + lane * 16u) = acc[i][u];
  WAIT_LGKM0();
  bar();
  TS(8);
  asm volatile("s_waitcnt vmcnt(0)" : "+v"(xr[0]), "+v"(xr[1]), "+v"(xr[2]) :: "memory");
#pragma unroll
  for (int i = 0; i < 3; i++) {
    f32x4 a = acc[i][0];
#pragma unroll
    for (int u = 0; u < 3; u++) a += *(LAS f32x4*)(sm + ((mh * 4 + kq) * 9 + u * 3 + i) * 1024u + lane * 16u);
    const float x0 = __uint_as_float(xr[i][0] << 16), x1 = __uint_as_float(xr[i][0] & 0xffff0000u);
    const float x2 = __uint_as_float(xr[i][1] << 16), x3 = __uint_as_float(xr[i][1] & 0xffff0000u);
    st8<0>(u32x2{pk2(a[0] + x0, a[1] + x1), pk2(a[2] + x2, a[3] + x3)}, vo + i * 20480u, sout, 0);
  }
  TSOUT(2);
}

__global__ __launch_bounds__(512, 2) void k_gemm1(Params p) { gemm1_phase<0>(p); }
__global__ __launch_bounds__(512, 2) void k_attn(Params p) { attn_phase<0>(p); }
__global__ __launch_bounds__(512, 2) void k_final(Params p) { final_phase<0>(p); }

extern "C" void kernel_launch(void* const* d_in, const int* in_sizes, int n_in, void* d_out, int out_size, void* d_ws,
                              size_t ws_size, hipStream_t stream) {
  Params p;
  p.x = (const char*)d_in[0]; p.wqkv = (const char*)d_in[1]; p.wo = (const char*)d_in[2];
  p.wg = (const char*)d_in[4]; p.wu = (const char*)d_in[5]; p.wd = (const char*)d_in[6];
  p.out = (char*)d_out; p.ws = (char*)d_ws;
  (void)hipFuncSetAttribute((const void*)k_gemm1, hipFuncAttributeMaxDynamicSharedMemorySize, LDS_BYTES);
  (void)hipFuncSetAttribute((const void*)k_attn, hipFuncAttributeMaxDynamicSharedMemorySize, LDS_BYTES);
  (void)hipFuncSetAttribute((const void*)k_final, hipFuncAttributeMaxDynamicSharedMemorySize, LDS_BYTES);
  k_gemm1<<<256, 512, LDS_BYTES, stream>>>(p);
  k_attn<<<256, 512, 51200, stream>>>(p);
  k_final<<<256, 512, 147456, stream>>>(p);
}
```

```cpp
#include <hip/hip_runtime.h>

typedef int i32x4 __attribute__((ext_vector_type(4)));
typedef unsigned u32x4 __attribute__((ext_vector_type(4)));
typedef unsigned u32x2 __attribute__((ext_vector_type(2)));
typedef float f32x4 __attribute__((ext_vector_type(4)));
typedef float f32x2 __attribute__((ext_vector_type(2)));
typedef float f32x16 __attribute__((ext_vector_type(16)));
typedef __bf16 bf16x8 __attribute__((ext_vector_type(8)));
typedef __bf16 bf16x2 __attribute__((ext_vector_type(2)));
typedef short s16x4 __attribute__((ext_vector_type(4)));
#define LAS __attribute__((address_space(3)))
#define DEVI __device__ __forceinline__

constexpr int S_ = 2048, D_ = 640, F_ = 1536;
constexpr unsigned X_BYTES = S_ * D_ * 2, WQ_BYTES = D_ * 3 * D_ * 2, WF_BYTES = D_ * F_ * 2, WO_BYTES = D_ * D_ * 2;
constexpr unsigned QKV_BYTES = S_ * 3 * D_ * 2;
constexpr unsigned AF_BYTES = 128u * 68u * 1024u;
constexpr unsigned WS_QKV = 1u << 20, WS_AF = 16u << 20;
constexpr unsigned C_FLG = 4096, C_STAT = 0x40000, C_CS = 0x50000, C_BYTES = 1u << 20;
constexpr unsigned LDS_BYTES = 163840;
constexpr unsigned OOB = 0x80000000u;

DEVI i32x4 mk_srd(const void* p, unsigned n) {
  unsigned long long a = (unsigned long long)p;
  i32x4 r;
  r.x = __builtin_amdgcn_readfirstlane((int)(unsigned)a);
  r.y = __builtin_amdgcn_readfirstlane((int)(unsigned)(a >> 32));
  r.z = (int)n; r.w = 0x00020000;
  return r;
}
DEVI i32x4 sel_srd(bool c, i32x4 a, i32x4 b) { i32x4 r; r.x = c ? a.x : b.x; r.y = c ? a.y : b.y; r.z = c ? a.z : b.z; r.w = a.w; return r; }
DEVI void dma16(unsigned ldsaddr, unsigned voff, i32x4 srd, unsigned soff) {
  asm volatile("s_mov_b32 m0, %0\n\ts_nop 0\n\tbuffer_load_dwordx4 %1, %2, %3 offen lds"
               :: "s"(ldsaddr), "v"(voff), "s"(srd), "s"(soff) : "memory");
}
template <int SC, int OFF = 0> DEVI u32x4 ld16(unsigned voff, i32x4 srd, unsigned soff) {
  u32x4 r;
  if (SC) asm volatile("buffer_load_dwordx4 %0, %1, %2, %3 offen offset:%4 sc1" : "=v"(r) : "v"(voff), "s"(srd), "s"(soff), "i"(OFF) : "memory");
  else    asm volatile("buffer_load_dwordx4 %0, %1, %2, %3 offen offset:%4" : "=v"(r) : "v"(voff), "s"(srd), "s"(soff), "i"(OFF) : "memory");
  return r;
}
template <int SC, int OFF = 0> DEVI u32x2 ld8(unsigned voff, i32x4 srd, unsigned soff) {
  u32x2 r;
  if (SC) asm volatile("buffer_load_dwordx2 %0, %1, %2, %3 offen offset:%4 sc1" : "=v"(r) : "v"(voff), "s"(srd), "s"(soff), "i"(OFF) : "memory");
  else    asm volatile("buffer_load_dwordx2 %0, %1, %2, %3 offen offset:%4" : "=v"(r) : "v"(voff), "s"(srd), "s"(soff), "i"(OFF) : "memory");
  return r;
}
template <int SC, int OFF = 0> DEVI unsigned ld4(unsigned voff, i32x4 srd, unsigned soff) {
  unsigned r;
  if (SC) asm volatile("buffer_load_dword %0, %1, %2, %3 offen offset:%4 sc1" : "=v"(r) : "v"(voff), "s"(srd), "s"(soff), "i"(OFF) : "memory");
  else    asm volatile("buffer_load_dword %0, %1, %2, %3 offen offset:%4" : "=v"(r) : "v"(voff), "s"(srd), "s"(soff), "i"(OFF) : "memory");
  return r;
}
template <int SC> DEVI void st16(u32x4 v, unsigned voff, i32x4 srd, unsigned soff) {
  if (SC) asm volatile("buffer_store_dwordx4 %0, %1, %2, %3 offen sc1\n\ts_nop 1" :: "v"(v), "v"(voff), "s"(srd), "s"(soff) : "memory");
  else    asm volatile("buffer_store_dwordx4 %0, %1, %2, %3 offen\n\ts_nop 1" :: "v"(v), "v"(voff), "s"(srd), "s"(soff) : "memory");
}
template <int SC, int OFF = 0> DEVI void st8(u32x2 v, unsigned voff, i32x4 srd, unsigned soff) {
  if (SC) asm volatile("buffer_store_dwordx2 %0, %1, %2, %3 offen offset:%4 sc1" :: "v"(v), "v"(voff), "s"(srd), "s"(soff), "i"(OFF) : "memory");
  else    asm volatile("buffer_store_dwordx2 %0, %1, %2, %3 offen offset:%4" :: "v"(v), "v"(voff), "s"(srd), "s"(soff), "i"(OFF) : "memory");
}
DEVI void st4s(unsigned v, unsigned voff, i32x4 srd) {
  asm volatile("buffer_store_dword %0, %1, %2, 0 offen sc1" :: "v"(v), "v"(voff), "s"(srd) : "memory");
}
template <int N> DEVI void wait_vm() { asm volatile("s_waitcnt vmcnt(%0)" :: "i"(N) : "memory"); }
#define WAIT_LGKM0() asm volatile("s_waitcnt lgkmcnt(0)" ::: "memory")
DEVI void bar() { asm volatile("s_barrier" ::: "memory"); }
#define SB() __builtin_amdgcn_sched_barrier(0)
#define PIN(x) asm volatile("" : "+v"(x))

DEVI unsigned pk2(float a, float b) { bf16x2 v = {(__bf16)a, (__bf16)b}; return __builtin_bit_cast(unsigned, v); }
DEVI float rbf(float a) { return (float)(__bf16)a; }
DEVI bf16x8 asbf(u32x4 v) { return __builtin_bit_cast(bf16x8, v); }
DEVI u32x2 trd(LAS char* p) { return __builtin_bit_cast(u32x2, __builtin_amdgcn_ds_read_tr16_b64_v4i16((LAS s16x4*)p)); }
DEVI u32x4 cat(u32x2 a, u32x2 b) { return __builtin_shufflevector(a, b, 0, 1, 2, 3); }
DEVI f32x4 mfma16(u32x4 a, u32x4 b, f32x4 c) { return __builtin_amdgcn_mfma_f32_16x16x32_bf16(asbf(a), asbf(b), c, 0, 0, 0); }
DEVI void mfma16a(u32x4 a, u32x4 b, f32x4& c) { asm volatile("v_mfma_f32_16x16x32_bf16 %0, %1, %2, %0" : "+v"(c) : "v"(a), "v"(b)); }
DEVI void mfma16z(u32x4 a, u32x4 b, f32x4& c) { asm volatile("v_mfma_f32_16x16x32_bf16 %0, %1, %2, 0" : "=&v"(c) : "v"(a), "v"(b)); }
DEVI int lane_id() { int l; asm volatile("v_mbcnt_lo_u32_b32 %0, -1, 0\n\tv_mbcnt_hi_u32_b32 %0, -1, %0" : "=v"(l)); return l; }
DEVI f32x16 mfma32(u32x4 a, u32x4 b, f32x16 c) { return __builtin_amdgcn_mfma_f32_32x32x16_bf16(asbf(a), asbf(b), c, 0, 0, 0); }
DEVI float blo(unsigned r) { return __uint_as_float(r << 16); }
DEVI float bhi(unsigned r) { return __uint_as_float(r & 0xffff0000u); }
template <int C> DEVI float dppf(float v) { return __uint_as_float(__builtin_amdgcn_update_dpp(0, __float_as_uint(v), C, 0xf, 0xf, false)); }
DEVI float wave_sum(float v) {
  v += dppf<0x128>(v); v += dppf<0x124>(v); v += dppf<0x122>(v); v += dppf<0x121>(v);
  auto a = __builtin_amdgcn_permlane16_swap(__float_as_uint(v), __float_as_uint(v), false, false);
  v = __uint_as_float(a[0]) + __uint_as_float(a[1]);
  auto b = __builtin_amdgcn_permlane32_swap(__float_as_uint(v), __float_as_uint(v), false, false);
  return __uint_as_float(b[0]) + __uint_as_float(b[1]);
}
DEVI bool any_lane(bool c) { return __builtin_amdgcn_ballot_w64(c) != 0; }
constexpr int SPIN = 1 << 15;
DEVI void poll_rest(unsigned v, bool need, unsigned tag, unsigned voff, i32x4 srd) {
  if (any_lane(need && v != tag)) {
    for (int it = 0; it < SPIN; it++) {
      __builtin_amdgcn_s_sleep(1);
      v = ld4<1>(voff, srd, 0);
      asm volatile("s_waitcnt vmcnt(0)" : "+v"(v) :: "memory");
      if (!any_lane(need && v != tag)) break;
    }
  }
}

#define TS(i)
#define TSDECL
#define TSOUT()
template <int I> struct IC { static constexpr int v = I; };
template <int B, int E, class F> DEVI void sfor(F&& f) { if constexpr (B < E) { f(IC<B>{}); sfor<B + 1, E>(f); } }

struct Params {
  const char *x, *wqkv, *wo, *wg, *wu, *wd;
  char *out, *ws;
};

extern __shared__ __attribute__((aligned(16))) char smem_[];

constexpr unsigned G1_X = 122880, L_CS = 161792;
constexpr unsigned AT_K = 0, AT_V = 139264;
constexpr unsigned R_A = 40960, R_B = 81920, R_H = 24576, R_H3 = 122880, R_DUMP = 128000;

__global__ __launch_bounds__(512, 2) void k_fused(Params p) {
  TSDECL;
  LAS char* sm = (LAS char*)smem_;
  const unsigned lb = (unsigned)(size_t)sm;
  const int bid = blockIdx.x, xcd = bid & 7, loc = bid >> 3;
  const int wave = __builtin_amdgcn_readfirstlane(threadIdx.x >> 6);
  int lane = threadIdx.x & 63, t16 = lane & 15, g = lane >> 4;
  const i32x4 sx = mk_srd(p.x, X_BYTES), sc = mk_srd(p.ws, C_BYTES);
  const int rb = (xcd & 1) * 8 + (loc & 7), jg = (xcd >> 1) * 4 + (loc >> 3);
  const int tB = wave >> 2, mw = (wave >> 1) & 1, nw = tB * 2 + (wave & 1);
  unsigned ep = ld4<1>(0u, sc, 0);
  const unsigned srow = rb * 128 + jg * 8 + wave;
  u32x4 r0 = ld16<0>(lane * 16u, sx, srow * 1280u);
  unsigned r1 = ld4<0, 1024>(lane * 4u, sx, srow * 1280u);

  const i32x4 sq = mk_srd(p.wqkv, WQ_BYTES), sg = mk_srd(p.wg, WF_BYTES);
  const i32x4 sh = sel_srd(wave < 2, sg, mk_srd(p.wu, WF_BYTES));
  const unsigned vx = (rb * 128 + 8 * wave + (lane >> 3)) * 1280u + (((lane & 7) ^ (((wave & 1) << 2) | (lane >> 4))) * 16u);
  const unsigned wcol = (((((lane & 7) >> 1) ^ (((lane >> 4) & 1) | ((wave & 1) << 1))) * 32u) + (lane & 1) * 16u);
  const unsigned vq = jg < 15 ? (8 * wave + (lane >> 3)) * 3840u + wcol + jg * 256u : OOB;
  const unsigned vg = (8 * wave + (lane >> 3)) * 3072u + wcol + jg * 192u;
  const unsigned hcb = (wave >> 1) == 1 ? 0u : (wave >> 1) == 2 ? 64u : 128u;
  const unsigned vh = (32 * (wave & 1) + (lane >> 2)) * 3072u + jg * 192u + hcb + ((((lane >> 1) & 1) ^ (lane >> 5)) * 32u) + (lane & 1) * 16u;
  const unsigned ldw = lb + wave * 1024u;
  const int rot = (xcd & 1) * 5;
  auto sidx = [&](int s) { return (unsigned)(s < 5 ? s + rot : s - rot); };
  auto issue_x = [&](int s, int b) { dma16(ldw + G1_X + (s & 1) * 16384u + b * 8192u, vx, sx, b * 81920u + sidx(s) * 128u); };
  auto issue_w = [&](int s, int b) {
    const unsigned sl = (s % 3) * 40960u;
    if (b < 2) dma16(ldw + sl + b * 8192u, vq, sq, sidx(s) * 245760u + b * 128u);
    else if (b == 2) dma16(ldw + sl + 16384u, vg, sg, sidx(s) * 196608u);
    else dma16(ldw + wave * 1024u + sl + 24576u + (b - 3) * 1024u, vh, sh, sidx(s) * 196608u + (b - 3) * 49152u);
  };
  issue_x(0, 0); issue_x(0, 1);
#pragma unroll
  for (int b = 0; b < 5; b++) issue_w(0, b);
  const int rt = xcd * 4 + (loc & 3), cg = loc >> 2;
  const i32x4 sd = mk_srd(p.wd, WF_BYTES), so = mk_srd(p.wo, WO_BYTES);
  unsigned vW[5];
  auto mk_vw = [&]() {
#pragma unroll
    for (int b = 0; b < 5; b++) { const unsigned q = b * 64 + lane, r = (q * 6554u) >> 16; vW[b] = (wave * 32 + r) * 1280u + (q - r * 10) * 16u + cg * 160u; }
  };
  mk_vw();
  u32x4 RW[4][5];
  auto raw_w = [&](int n, int b) { RW[(n - 2) & 3][b] = ld16<0>(vW[b], n < 6 ? sd : so, (n < 6 ? n : n - 6) * 327680u); };
  auto dma_w = [&](int n, int b, unsigned dst) { dma16(dst + b * 1024u, vW[b], n < 6 ? sd : so, (n < 6 ? n : n - 6) * 327680u); };
  TS(1);
  asm volatile("s_waitcnt vmcnt(7)" : "+v"(ep), "+v"(r0), "+v"(r1) :: "memory");
  TS(2);
  const unsigned tag = __builtin_amdgcn_readfirstlane(ep) + 1u;
  {
    float e[10];
#pragma unroll
    for (int i = 0; i < 4; i++) { e[2 * i] = blo(r0[i]); e[2 * i + 1] = bhi(r0[i]); }
    e[8] = blo(r1); e[9] = bhi(r1);
    float s = ((e[0] + e[1]) + (e[2] + e[3])) + ((e[4] + e[5]) + (e[6] + e[7])) + (e[8] + e[9]);
    SB(); issue_x(1, 0); issue_x(1, 1); issue_w(1, 0); SB();
    const float mean = wave_sum(s) * (1.0f / 640.0f);
    float q = 0.f;
#pragma unroll
    for (int i = 0; i < 10; i++) { const float d = e[i] - mean; q = __builtin_fmaf(d, d, q); }
    SB(); issue_w(1, 1); issue_w(1, 2); issue_w(1, 3); issue_w(1, 4); SB();
    const float var = wave_sum(q) * (1.0f / 640.0f);
    const float sdv = __builtin_amdgcn_sqrtf(rbf(rbf(var) + 1e-5f));
    st8<1>(u32x2{pk2(mean, sdv), tag}, lane == 0 ? C_STAT + srow * 8u : OOB, sc, 0);
  }
  LAS char* xa[2];
#pragma unroll
  for (int ks = 0; ks < 2; ks++) xa[ks] = sm + (G1_X + mw * 8192u + t16 * 128u + (((4 * ks + g) ^ (t16 >> 1)) * 16u));
  LAS char* wa[2][5];
  {
    const unsigned fl = ((t16 >> 3) & 1) | ((g & 1) << 1);
    const unsigned wl = g * 1024u + (t16 >> 2) * 128u + (t16 & 3) * 8u, hl = 24576u + g * 512u + (t16 >> 2) * 64u + (t16 & 3) * 8u;
    auto ua = [&](unsigned q) { return wl + (q >> 2) * 8192u + (((q & 3) ^ fl) * 32u); };
    const unsigned nl = wave & 1;
    wa[0][0] = sm + ua(tB ? 2 + 3 * nl : nl);
    wa[0][1] = sm + ua(tB ? 3 + 3 * nl : 8 + 2 * nl);
    wa[0][2] = sm + ua(tB ? 4 + 3 * nl : 9 + 2 * nl);
    wa[0][3] = sm + (hl + (tB ? ((nl ^ (g & 1)) * 32u) : (1 + nl) * 4096u + ((g & 1) * 32u)));
    wa[0][4] = sm + (hl + (tB ? 12288u + ((nl ^ (g & 1)) * 32u) : (1 + nl) * 4096u + (((g & 1) ^ 1) * 32u)));
#pragma unroll
    for (int u = 0; u < 5; u++) { wa[1][u] = wa[0][u] + 81920; PIN(wa[1][u]); }
  }
  f32x4 acc[4][5], cs[5];
  u32x4 xf[2][4];
  u32x2 wf[2][5][2];
  auto rd = [&](int s, int ks, int r) {
    if (r < 4) xf[ks][r] = *(LAS u32x4*)(xa[ks] + ((s & 1) * 16384 + r * 2048));
    else {
      const int u = (r - 4) >> 1, h = r & 1, sl = s % 3;
      wf[ks][u][h] = trd(wa[sl == 2][u] + ((sl == 1) * 40960 + (u < 3 ? ks * 4096 + h * 512 : ks * 2048 + h * 256)));
    }
  };
  auto mf0 = [&](int n) { mfma16z(cat(wf[0][n >> 2][0], wf[0][n >> 2][1]), xf[0][n & 3], acc[n & 3][n >> 2]); };
  auto mf = [&](int ks, int n) { mfma16a(cat(wf[ks][n >> 2][0], wf[ks][n >> 2][1]), xf[ks][n & 3], acc[n & 3][n >> 2]); };
  const int cprod = mw == tB ? (loc & 7) : -1;
  const unsigned vcs = C_CS + jg * 25600u;
  unsigned vco = g == 0 ? vcs + (rot * 320u + nw * 80u + t16) * 8u : OOB;
  PIN(vco);
  auto csum = [&](int s, int ks) {
    if (cprod == s) {
      const u32x4 ones = {0x3f803f80u, 0x3f803f80u, 0x3f803f80u, 0x3f803f80u};
#pragma unroll
      for (int u = 0; u < 5; u++) cs[u] = mfma16(ones, cat(wf[ks][u][0], wf[ks][u][1]), ks ? cs[u] : f32x4{0, 0, 0, 0});
      if (ks) {
        sfor<0, 5>([&](auto U) { st8<1, U.v * 128>(u32x2{__float_as_uint(cs[U.v][0]), tag}, vco, sc, s * 2560u); });
      }
    }
  };
  u32x2 gs[4], gc[5];
  const unsigned vgs = C_STAT + (rb * 128 + mw * 64 + t16) * 8u;
  const unsigned vgc = lane < 40 ? vcs + (wave * 40 + lane) * 8u : OOB;
  constexpr int PB[2][5] = {{0, 1, 2, 5, 6}, {3, 4, 7, 8, 9}};
  bool bad = false;
  float csv = 0.f;
  unsigned sv[4];
  f32x4 c5[5];
  float nm[4], rs[4], sdr[4];

  wait_vm<8>();
  bar();
  TS(3);
#pragma unroll
  for (int r = 0; r < 14; r++) rd(0, 0, r);
  SB();
  sfor<0, 10>([&](auto S_) {
    constexpr int s = S_.v;
    if constexpr (s < 5) { csum(s, 0); SB(); }
    sfor<0, 20>([&](auto N_) {
      constexpr int n = N_.v;
      if constexpr (s == 0) mf0(n); else mf(0, n);
      if constexpr (n < 14) rd(s, 1, n);
      if constexpr (s == 0) { if constexpr (n % 4 == 1) issue_w(2, n / 4); }
      else if constexpr (s < 8) { if constexpr (n == 3 || n == 9 || n == 15) issue_w(s + 2, 2 + n / 6); }
      if constexpr (s >= 2 && s <= 6 && n == 12) raw_w(2 + (2 * s - 3) / 5, (2 * s - 3) % 5);
      if constexpr (s == 6 && n >= 4 && n < 9) gc[n - 4] = ld8<1>(vgc, sc, PB[0][n - 4] * 2560u);
      if constexpr (s == 7 && (n == 5 || n == 7)) gc[n == 7] = ld8<1>(vgc, sc, PB[1][n == 7] * 2560u);
      if constexpr (s == 8 && n % 6 == 2) dma_w(0, 2 + n / 6, lb + R_A + wave * 5120u);
      if constexpr (s == 9 && n % 6 == 2) dma_w(1, 2 + n / 6, lb + R_B + wave * 5120u);
      SB();
    });
    WAIT_LGKM0();
    if constexpr (s == 0) wait_vm<6>();
    else if constexpr (s == 1) wait_vm<5>();
    else if constexpr (s == 5) wait_vm<11>();
    else if constexpr (s == 6) asm volatile("s_waitcnt vmcnt(12)" : "+v"(gs[0]), "+v"(gs[1]), "+v"(gs[2]), "+v"(gs[3]) :: "memory");
    else if constexpr (s == 8) asm volatile("s_waitcnt vmcnt(5)" : "+v"(gc[0]), "+v"(gc[1]), "+v"(gc[2]), "+v"(gc[3]), "+v"(gc[4]) :: "memory");
    else if constexpr (s < 8) wait_vm<7>();
    bar();
    TS(4 + s);
    SB();
    if constexpr (s < 5) { csum(s, 1); SB(); }
    sfor<0, 20>([&](auto N_) {
      constexpr int n = N_.v;
      mf(1, n);
      if constexpr (s < 9 && n < 14) rd(s + 1, 0, n);
      if constexpr (s < 7) { if constexpr (n == 1 || n == 5) issue_x(s + 2, n / 4); }
      if constexpr (s >= 1 && s <= 5 && n == 9) raw_w(2 + (2 * s - 2) / 5, (2 * s - 2) % 5);
      if constexpr (s < 7) { if constexpr (n == 13 || n == 17) issue_w(s + 3, n / 16); }
      if constexpr (s == 4 && n >= 14 && n < 19 && n != 17) { constexpr int i = n < 17 ? n - 14 : 3; gs[i] = ld8<1, i * 128>(vgs, sc, 0); }
      if constexpr (s == 6 && n >= 2 && n < 6) { bad |= gs[n - 2][1] != tag; sv[n - 2] = gs[n - 2][0]; }
      if constexpr (s == 6 && n == 18) {
        asm volatile("s_waitcnt vmcnt(7)" : "+v"(gc[0]), "+v"(gc[1]), "+v"(gc[2]), "+v"(gc[3]), "+v"(gc[4]) :: "memory");
#pragma unroll
        for (int k = 0; k < 5; k++) { bad |= gc[k][1] != tag && lane < 40; csv += __uint_as_float(gc[k][0]); }
      }
      if constexpr (s == 7) {
        if constexpr (n == 0 || n == 2 || n == 4) gc[2 + n / 2] = ld8<1>(vgc, sc, PB[1][2 + n / 2] * 2560u);
        if constexpr (n == 6 || n == 9) issue_x(9, n / 9);
        if constexpr (n == 13 || n == 17) dma_w(0, n / 16, lb + R_A + wave * 5120u);
      }
      if constexpr (s == 8) {
        if constexpr (n >= 1 && n < 6) { bad |= gc[n - 1][1] != tag && lane < 40; csv += __uint_as_float(gc[n - 1][0]); }
        if constexpr (n == 7) {
          if (any_lane(bad)) {
            for (int it = 0; it < SPIN; it++) {
              __builtin_amdgcn_s_sleep(1);
              bad = false; csv = 0.f;
              sfor<0, 4>([&](auto I) { gs[I.v] = ld8<1, I.v * 128>(vgs, sc, 0); });
              asm volatile("s_waitcnt vmcnt(0)" : "+v"(gs[0]), "+v"(gs[1]), "+v"(gs[2]), "+v"(gs[3]) :: "memory");
#pragma unroll
              for (int k = 0; k < 4; k++) { bad |= gs[k][1] != tag; sv[k] = gs[k][0]; }
#pragma unroll
              for (int hb = 0; hb < 2; hb++) {
#pragma unroll
                for (int k = 0; k < 5; k++) gc[k] = ld8<1>(vgc, sc, PB[hb][k] * 2560u);
                asm volatile("s_waitcnt vmcnt(0)" : "+v"(gc[0]), "+v"(gc[1]), "+v"(gc[2]), "+v"(gc[3]), "+v"(gc[4]) :: "memory");
#pragma unroll
                for (int k = 0; k < 5; k++) { bad |= gc[k][1] != tag && lane < 40; csv += __uint_as_float(gc[k][0]); }
              }
              if (!any_lane(bad)) break;
            }
          }
          if (lane < 40) *(LAS float*)(sm + L_CS + (wave * 40 + lane) * 4) = csv;
        }
        if constexpr (n == 9 || n == 16) dma_w(1, n / 16, lb + R_B + wave * 5120u);
        if constexpr (n >= 8 && n < 12) { nm[n - 8] = -blo(sv[n - 8]); sdr[n - 8] = bhi(sv[n - 8]); }
        if constexpr (n >= 12 && n < 16) rs[n - 12] = __builtin_amdgcn_rcpf(sdr[n - 12]);
      }
      if constexpr (s == 9) {
        if constexpr (n < 5) c5[n] = *(LAS f32x4*)(sm + L_CS + (nw * 80 + n * 16 + g * 4) * 4);
        if constexpr (n % 4 == 1) dma_w(8, n / 4, lb + (wave < 3 ? R_H + wave * 5120u : wave == 3 ? R_H3 : R_DUMP));
        if constexpr (n % 4 == 3) raw_w(4, n / 4);
      }
      SB();
    });
  });
  TS(14);
#pragma unroll
  for (int b = 0; b < 5; b++) raw_w(5, b);
  asm volatile("s_nop 7" ::: "memory");
  SB();
  lane = lane_id(); t16 = lane & 15; g = lane >> 4;
  const i32x4 sqkv = mk_srd(p.ws + WS_QKV, QKV_BYTES), saf = mk_srd(p.ws + WS_AF, AF_BYTES);
  {
    const unsigned vl = lane * 16u, vlq = jg < 15 ? vl : OOB;
    auto lo2 = [](f32x4 v) { return f32x2{v[0], v[1]}; };
    auto hi2 = [](f32x4 v) { return f32x2{v[2], v[3]}; };
    auto st_qkv = [&](int u, unsigned col) {
      const float qs = col < 640u ? 0.22360679775f * 1.44269504089f : 1.0f;
#pragma unroll
      for (int pr = 0; pr < 2; pr++) {
        u32x4 v;
#pragma unroll
        for (int h = 0; h < 2; h++) {
          const int i = pr * 2 + h;
          const float r = rs[i] * qs;
          const f32x2 m2 = {nm[i], nm[i]}, r2 = {r, r};
          const f32x2 a = __builtin_elementwise_fma(m2, lo2(c5[u]), lo2(acc[i][u])) * r2;
          const f32x2 b = __builtin_elementwise_fma(m2, hi2(c5[u]), hi2(acc[i][u])) * r2;
          v[2 * h] = pk2(a[0], a[1]); v[2 * h + 1] = pk2(b[0], b[1]);
        }
        st16<1>(v, vlq, sqkv, ((rb * 4 + mw * 2 + pr) * 480u + (col >> 2)) * 256u);
      }
    };
    auto glu = [&](int i, int ug, int uu) {
      const float rg = -1.44269504089f * rs[i];
      const f32x2 m2 = {nm[i], nm[i]}, sd2 = {sdr[i], sdr[i]}, rg2 = {rg, rg};
      u32x2 w;
#pragma unroll
      for (int q = 0; q < 2; q++) {
        const f32x2 ag = q ? hi2(acc[i][ug]) : lo2(acc[i][ug]), cgv = q ? hi2(c5[ug]) : lo2(c5[ug]);
        const f32x2 au = q ? hi2(acc[i][uu]) : lo2(acc[i][uu]), cu = q ? hi2(c5[uu]) : lo2(c5[uu]);
        const f32x2 t = __builtin_elementwise_fma(m2, cgv, ag) * rg2;
        const f32x2 e = {__builtin_amdgcn_exp2f(t[0]), __builtin_amdgcn_exp2f(t[1])};
        const f32x2 d = __builtin_elementwise_fma(e, sd2, sd2);
        const f32x2 rc = {__builtin_amdgcn_rcpf(d[0]), __builtin_amdgcn_rcpf(d[1])};
        const f32x2 hv = __builtin_elementwise_fma(m2, cu, au) * rc;
        w[q] = pk2(hv[0], hv[1]);
      }
      return w;
    };
    const unsigned tile0 = rb * 8 + mw * 4, nl = wave & 1;
    if (tB == 0) {
      st_qkv(0, jg * 128u + nl * 16u);
#pragma unroll
      for (int i = 0; i < 4; i++) st16<1>(cat(glu(i, 1, 3), glu(i, 2, 4)), vl, saf, ((tile0 + i) * 68u + 3 * jg + nl) * 1024u);
    } else {
      const unsigned c0 = jg * 128u + (2 + 3 * nl) * 16u;
      st_qkv(0, c0); st_qkv(1, c0 + 16u); st_qkv(2, c0 + 32u);
#pragma unroll
      for (int i = 0; i < 4; i++) st8<1>(glu(i, 3, 4), vl + nl * 8u, saf, ((tile0 + i) * 68u + 3 * jg + 2) * 1024u);
    }
  }
  TS(15);
  wait_vm<4>();
  bar();
  TS(16);
  *(LAS u32x2*)(sm + AT_V + (wave * 64 + lane) * 48 + 40) = u32x2{0x3f803f80u, 0x3f803f80u};
  *(LAS u32x2*)(sm + AT_K + (wave * 64 + lane) * 48 + 40) = u32x2{((loc >> 2) == 0 && wave < 4) ? 0x3f80fe00u : 0x3f800000u, 0u};
  if (wave == 0) {
    const unsigned hs = (jg * 128u * 3277u >> 16) + (lane >> 1), dch = lane & 1;
    const unsigned sec = hs >> 5, h = hs & 31, ch2 = (rb >> 1) + dch;
    const bool ok = lane < 16 && hs * 20u < jg * 128u + 128u && jg < 15 && ch2 < 8 && (sec > 0 || dch == 0);
    const unsigned tb_ = (h >> 2) + 8 * ((h & 3) + 4 * ch2);
    st4s(tag, ok ? C_FLG + tb_ * 512u + (sec * 8 + ((rb & 1) + 2 - 2 * dch) * 2 + ((hs * 20u >> 7) != (unsigned)jg)) * 4u : OOB, sc);
  }
  wait_vm<0>();
  bar();
  TS(17);
  if (wave == 0) {
    const unsigned rt2 = rb * 2 + (lane & 1), cg2 = (lane >> 1) & 7, tb_ = (rt2 >> 2) + 8 * ((rt2 & 3) + 4 * cg2);
    st4s(tag, lane < 16 ? C_FLG + tb_ * 512u + 128u + jg * 4u : (lane == 16 && jg == 0) ? C_FLG + 192u + rb * 4u : OOB, sc);
  }
  const int head = xcd * 4 + (loc & 3), ch = loc >> 2;
  const int q32 = lane & 31, hi = lane >> 5;
  const unsigned vfl = C_FLG + bid * 512u + lane * 4u;
  const int kq = wave & 3, mh = wave >> 2;
  const unsigned vA = ((rt * 4 + mh * 2) * 68u + kq) * 1024u + lane * 16u;
  u32x4 A[9][2][2];
  auto ld_a = [&](int n, int rr, int i) { A[n][rr][i] = ld16<1>(vA, saf, (2 * n + rr) * 4096u + i * 69632u); };
  {
    const unsigned sec = lane >> 3, rbi = (lane >> 1) & 3, c0 = sec * 640u + head * 20u;
    const bool need = lane < 24 && (sec == 0 ? rbi == 2u + (wave >> 2) : (rbi == (unsigned)(wave >> 1) && (ch > 0 || wave >= 4))) &&
                      ((lane & 1) == 0 || (c0 >> 7) != ((c0 + 19u) >> 7));
    unsigned v = ld4<1>(vfl, sc, 0);
    asm volatile("s_waitcnt vmcnt(0)" : "+v"(v) :: "memory");
    poll_rest(v, need, tag, vfl, sc);
  }
  TS(18);
  f32x16 o;
  {
    const int tbq = ch * 8 + wave, kt0 = ch * 8 - 8 + 2 * wave;
    const unsigned kb = kt0 < 0 ? OOB : (unsigned)(kt0 * 480 + 160 + 5 * head) * 256u;
    const unsigned va = kb + lane * 16u, vb = va + 122880u;
    const unsigned vt = kb + (g & 1) * 122880u + (g >> 1) * 40960u + 1024u + t16 * 16u;
    u32x4 k0 = ld16<1>(va, sqkv, 0), k1 = ld16<1>(vb, sqkv, 0), v0 = ld16<1>(va, sqkv, 40960u), v1 = ld16<1>(vb, sqkv, 40960u);
    u32x4 tl = ld16<1>(vt, sqkv, 0);
    const unsigned qa = (unsigned)(tbq * 480 + 5 * head) * 256u + (q32 & 15) * 16u + (q32 >> 4) * 8u;
    u32x2 qa0 = ld8<1>(qa + hi * 512u, sqkv, 0), qa1 = ld8<1, 256>(qa + hi * 512u, sqkv, 0), qa2 = ld8<1, 1024>(qa, sqkv, 0);
    unsigned hf = ld4<1, 128>(vfl, sc, 0);
    asm volatile("s_waitcnt vmcnt(4)" : "+v"(k0), "+v"(k1), "+v"(v0), "+v"(v1), "+v"(tl) :: "memory");
    {
      LAS char* kw = sm + (wave * 3072 + t16 * 48 + g * 8);
      auto put = [&](LAS char* a, u32x4 v) { *(LAS u32x2*)a = u32x2{v[0], v[1]}; *(LAS u32x2*)(a + 768) = u32x2{v[2], v[3]}; };
      put(kw + AT_K, k0); put(kw + AT_K + 1536, k1); put(kw + AT_V, v0); put(kw + AT_V + 1536, v1);
      put(sm + (wave * 3072 + (g & 1) * 1536 + (g >> 1) * AT_V + t16 * 48 + 32), tl);
    }
    asm volatile("s_waitcnt vmcnt(0)" : "+v"(qa0), "+v"(qa1), "+v"(qa2), "+v"(hf) :: "memory");
    const u32x4 Q1 = cat(qa0, qa1);
    u32x4 Q2 = {hi ? 0u : qa2[0], hi ? 0u : qa2[1], hi ? 0u : 0x00003f80u, 0u};
    WAIT_LGKM0();
    TS(19);
    bar();
    TS(20);
    LAS char* kp = sm + AT_K + wave * 1536 + q32 * 48;
    LAS char* kp1 = kp + hi * 16;
    const int vq_ = 4 * (g & 1) + (t16 & 3);
    LAS char* vp = sm + AT_V + wave * 1536 + (4 * (g >> 1) + (t16 >> 2)) * 48 + (vq_ > 5 ? 5 : vq_) * 8;
    f32x16 z;
#pragma unroll
    for (int r = 0; r < 16; r++) z[r] = 0.f;
    auto scq = [&](int off, u32x4 q2) {
      const u32x4 a = *(LAS u32x4*)(kp1 + off), b = *(LAS u32x4*)(kp + off + 32);
      return mfma32(b, q2, mfma32(a, Q1, z));
    };
    auto pv = [&](int off, const unsigned* pk) {
      const u32x4 V1 = cat(trd(vp + off), trd(vp + off + 384)), V2 = cat(trd(vp + off + 768), trd(vp + off + 1152));
      o = mfma32(V1, u32x4{pk[0], pk[1], pk[2], pk[3]}, o);
      o = mfma32(V2, u32x4{pk[4], pk[5], pk[6], pk[7]}, o);
    };
    const int dl = q32 - 4 * hi;
    auto xmax = [&](float m) {
      auto r = __builtin_amdgcn_permlane32_swap(__float_as_uint(m), __float_as_uint(m), false, false);
      return __builtin_fmaxf(__uint_as_float(r[0]), __uint_as_float(r[1]));
    };
    auto edge = [&](const f32x16& e, float m) {
      unsigned pa[8], pb[8];
#pragma unroll
      for (int r = 0; r < 16; r += 2) {
        const bool c0 = (r & 3) + 8 * (r >> 2) <= dl, c1 = ((r + 1) & 3) + 8 * (r >> 2) <= dl;
        const unsigned x = pk2(__builtin_amdgcn_exp2f(e[r] - m), __builtin_amdgcn_exp2f(e[r + 1] - m));
        const unsigned mk = (c0 ? 0xffffu : 0u) | (c1 ? 0xffff0000u : 0u);
        pb[r >> 1] = x & mk; pa[r >> 1] = x & ~mk;
      }
      pv(0, pa); pv(8 * 1536, pb);
    };
    auto merged = [&]() {
      const f32x16 sa = scq(0, Q2), sb = scq(8 * 1536, Q2);
      f32x16 e;
#pragma unroll
      for (int r = 0; r < 16; r++) e[r] = ((r & 3) + 8 * (r >> 2) <= dl) ? sb[r] : sa[r];
      return e;
    };
    auto vmax = [&](const f32x16& e, float m) {
#pragma unroll
      for (int r = 0; r < 16; r += 2) m = __builtin_fmaxf(__builtin_fmaxf(m, e[r]), e[r + 1]);
      return m;
    };
    o = z;
    {
      const f32x16 e = merged();
      const unsigned mb = pk2(xmax(vmax(e, -3.0e38f)), 0.f) << 16;
      const float m = __uint_as_float(mb);
      const u32x4 Q3 = {Q2[0], Q2[1], hi ? 0u : ((mb ^ 0x80000000u) | 0x3f80u), 0u};
      f32x16 sn = scq(1536, Q3);
      edge(e, m);
      poll_rest(hf, lane < 16, tag, vfl + 128u, sc);
      sfor<1, 8>([&](auto J) {
        constexpr int j = J.v;
        const f32x16 s = sn;
        if constexpr (j < 7) sn = scq((j + 1) * 1536, Q3);
        if constexpr (j <= 6) { ld_a((j - 1) / 2, (j - 1) & 1, 0); ld_a((j - 1) / 2, (j - 1) & 1, 1); }
        unsigned pk[8];
#pragma unroll
        for (int r = 0; r < 16; r += 2) pk[r >> 1] = pk2(__builtin_amdgcn_exp2f(s[r]), __builtin_amdgcn_exp2f(s[r + 1]));
        pv(j * 1536, pk);
      });
    }
    {
      float mx = 0.f;
#pragma unroll
      for (int r = 0; r < 12; r += 2) mx = __builtin_fmaxf(__builtin_fmaxf(mx, __builtin_fabsf(o[r])), __builtin_fabsf(o[r + 1]));
      if (any_lane(!(o[12] >= 5.9604645e-8f && o[12] <= 16777216.f && mx < 3.0e38f))) {
        float m = vmax(merged(), -3.0e38f);
#pragma nounroll
        for (int j = 1; j < 8; j++) m = vmax(scq(j * 1536, Q2), m);
        m = xmax(m);
        o = z;
        edge(merged(), m);
#pragma nounroll
        for (int j = 1; j < 8; j++) {
          const f32x16 s = scq(j * 1536, Q2);
          unsigned pk[8];
#pragma unroll
          for (int r = 0; r < 16; r += 2) pk[r >> 1] = pk2(__builtin_amdgcn_exp2f(s[r] - m), __builtin_amdgcn_exp2f(s[r + 1] - m));
          pv(j * 1536, pk);
        }
      }
    }
    TS(21);
    const float inv = __builtin_amdgcn_rcpf(o[12]);
    const unsigned tb_ = (unsigned)(tbq * 2 + (q32 >> 4)) * 68u + 48u;
    auto st_o = [&](int r0_, unsigned quad, bool ok) {
      const unsigned Q = 5 * head + quad;
      const unsigned a = (tb_ + (Q >> 3)) * 1024u + (Q & 3) * 256u + (q32 & 15) * 16u + ((Q >> 2) & 1) * 8u;
      st8<1>(u32x2{pk2(o[r0_] * inv, o[r0_ + 1] * inv), pk2(o[r0_ + 2] * inv, o[r0_ + 3] * inv)}, ok ? a : OOB, saf, 0);
    };
    st_o(0, hi, true); st_o(4, 2 + hi, true); st_o(8, 4, hi == 0);
  }
  ld_a(3, 0, 0); ld_a(3, 0, 1); ld_a(3, 1, 0); ld_a(3, 1, 1);
  lane = lane_id(); t16 = lane & 15; g = lane >> 4;
  mk_vw();
  LAS char* wb0 = sm + (kq * 5120 + (4 * g + (t16 >> 2)) * 160 + (t16 & 3) * 8);
  LAS char* wb1 = wb0 + R_B; PIN(wb1);
  LAS char* wbh = wb0 + (kq < 3 ? R_H : R_H3 - 3 * 5120); PIN(wbh);
  LAS char* wwr = sm + (R_A + wave * 5120 + lane * 16); PIN(wwr);
  LAS char* wwc = sm + ((wave < 4 ? AT_K : AT_V) + kq * 5120 + lane * 16); PIN(wwc);
  f32x4 fa[2][5];
  u32x2 w3[2][5][2];
  auto rd3 = [&](int r, int k) {
    const int n = r >> 1, rg = n == 8 ? 3 : n % 3, off = (k >> 1) * 32 + (k & 1) * 2560;
    w3[r & 1][k >> 1][k & 1] = rg == 0 ? trd(wb0 + (R_A + (r & 1) * 20480 + off)) : rg == 1 ? trd(wb1 + ((r & 1) * 20480 + off))
                             : rg == 3 ? trd(wbh + off) : (r & 1) ? trd(wb1 + (AT_V - R_B + off)) : trd(wb0 + (AT_K + off));
  };
  auto wr3 = [&](int n, int b) {
    const int rg = n % 3;
    if (rg == 2) *(LAS u32x4*)(wwc + b * 1024) = RW[(n - 2) & 3][b];
    else *(LAS u32x4*)(wwr + (rg * 40960 + b * 1024)) = RW[(n - 2) & 3][b];
  };
#define PINA(n) "+v"(A[n][0][0]), "+v"(A[n][0][1]), "+v"(A[n][1][0]), "+v"(A[n][1][1])
#define PINW(n) "+v"(RW[n][0]), "+v"(RW[n][1]), "+v"(RW[n][2]), "+v"(RW[n][3]), "+v"(RW[n][4])
  asm volatile("s_waitcnt vmcnt(4) lgkmcnt(0)" : PINA(0), PINA(1), PINA(2) :: "memory");
  asm volatile("" : PINW(0), PINW(1), PINW(2), PINW(3));
  TS(22);
  bar();
  TS(23);
  if (wave == 0) {
    const unsigned rt2 = ch * 4 + (lane & 3), cg2 = (lane >> 2) & 7, tb_ = (rt2 >> 2) + 8 * ((rt2 & 3) + 4 * cg2);
    st4s(tag, lane < 32 ? C_FLG + tb_ * 512u + 256u + head * 4u : OOB, sc);
  }
#pragma unroll
  for (int k = 0; k < 10; k++) rd3(0, k);
  SB();
  unsigned of = 0;
  u32x2 xr[3];
  const unsigned vo = (rt * 64 + mh * 32 + (kq & 1) * 16 + t16) * 1280u + cg * 160u + g * 8u;
  sfor<0, 17>([&](auto R_) {
    constexpr int r = R_.v, n = r >> 1, rr = r & 1;
    if constexpr (rr == 0 && n > 0) {
      if constexpr (n == 3) asm volatile("s_waitcnt vmcnt(18) lgkmcnt(0)" : PINA(3) :: "memory");
      else if constexpr (n == 4) asm volatile("s_waitcnt vmcnt(10) lgkmcnt(0)" : PINA(4), PINW(0) :: "memory");
      else if constexpr (n == 5) asm volatile("s_waitcnt vmcnt(8) lgkmcnt(0)" : PINA(5), PINW(1) :: "memory");
      else if constexpr (n == 6) asm volatile("s_waitcnt vmcnt(9) lgkmcnt(0)" : PINA(6) :: "memory");
      else if constexpr (n == 7) asm volatile("s_waitcnt vmcnt(5) lgkmcnt(0)" : PINA(7) :: "memory");
      else if constexpr (n == 8) asm volatile("s_waitcnt vmcnt(3)" : "+v"(A[8][0][0]), "+v"(A[8][0][1]) :: "memory");
      else WAIT_LGKM0();
      if constexpr (n < 8) bar();
      if constexpr (n == 2) TS(24); if constexpr (n == 4) TS(25); if constexpr (n == 6) TS(26);
      SB();
    }
    sfor<0, 10>([&](auto K_) {
      constexpr int k = K_.v, u = k >> 1, i = k & 1;
      if constexpr (r == 0) mfma16z(cat(w3[rr][u][0], w3[rr][u][1]), A[n][rr][i], fa[i][u]);
      else mfma16a(cat(w3[rr][u][0], w3[rr][u][1]), A[n][rr][i], fa[i][u]);
      if constexpr (r < 16) rd3(r + 1, k);
      if constexpr (rr == 0 && n <= 5 && (k & 1)) wr3(n + 2, k >> 1);
      if constexpr (rr == 1) {
        if constexpr (n <= 1 && (k & 1)) raw_w(n + 6, k >> 1);
        if constexpr (n == 3 && k == 8) {
          asm volatile("s_waitcnt vmcnt(0)" : "+v"(of) :: "memory");
          poll_rest(of, lane >= 16 ? lane < 48 : bid == 0, tag, vfl + 192u, sc);
          if (bid == 0 && wave == 0) st4s(tag, lane == 0 ? 0u : OOB, sc);
        }
      }
      if constexpr (rr == 0 && (n == 1 || n == 2) && k < 8 && !(k & 1)) ld_a(n + 3, k >> 2, (k >> 1) & 1);
      if constexpr (r == 6 && k == 0) of = ld4<1, 192>(vfl, sc, 0);
      if constexpr (r == 7 && k == 9) { ld_a(6, 0, 0); ld_a(6, 0, 1); }
      if constexpr (r == 8 && k < 6) { if constexpr (k < 2) ld_a(6, 1, k); else ld_a(7, (k - 2) >> 1, k & 1); }
      if constexpr (r == 10 && k < 2) ld_a(8, 0, k);
      if constexpr (r == 10 && k == 3) xr[0] = ld8<0>(vo + (kq >> 1) * 64u, sx, 0);
      if constexpr (r == 10 && k == 4) xr[1] = ld8<0, 32>(vo + (kq >> 1) * 64u, sx, 0);
      if constexpr (r == 10 && k == 5) xr[2] = ld8<0, 128>(vo, sx, 0);
      SB();
    });
  });
  asm volatile("s_nop 7" ::: "memory");
  SB();
  TS(27);
  {
    LAS char* rw = sm + ((mh ? 128000 : R_A) + lane * 16);
#pragma unroll
    for (int i = 0; i < 2; i++)
#pragma unroll
      for (int u = 0; u < 5; u++) {
        const int own = u < 4 ? i + 2 * (u >> 1) : i;
        if (kq != own) *(LAS f32x4*)(rw + ((i * 5 + u) * 3 + (kq > own ? kq - 1 : kq)) * 1024) = fa[i][u];
      }
    WAIT_LGKM0();
    bar();
    TS(28);
    asm volatile("s_waitcnt vmcnt(0)" : "+v"(xr[0]), "+v"(xr[1]), "+v"(xr[2]) :: "memory");
    const i32x4 sout = mk_srd(p.out, X_BYTES);
    auto fin = [&](f32x4 a, int t, u32x2 x, unsigned off) {
#pragma unroll
      for (int k = 0; k < 3; k++) a += *(LAS f32x4*)(rw + (t * 3 + k) * 1024);
      st8<0>(u32x2{pk2(a[0] + blo(x[0]), a[1] + bhi(x[0])), pk2(a[2] + blo(x[1]), a[3] + bhi(x[1]))}, vo + off, sout, 0);
    };
    const int i = kq & 1, ub = kq >> 1;
    const f32x4 a0 = i ? (ub ? fa[1][2] : fa[1][0]) : (ub ? fa[0][2] : fa[0][0]);
    const f32x4 a1 = i ? (ub ? fa[1][3] : fa[1][1]) : (ub ? fa[0][3] : fa[0][1]);
    fin(a0, i * 5 + 2 * ub, xr[0], ub * 64u);
    fin(a1, i * 5 + 2 * ub + 1, xr[1], ub * 64u + 32u);
    if (ub == 0) fin(i ? fa[1][4] : fa[0][4], i * 5 + 4, xr[2], 128u);
  }
  TSOUT();
}

extern "C" void kernel_launch(void* const* d_in, const int* in_sizes, int n_in, void* d_out, int out_size, void* d_ws,
                              size_t ws_size, hipStream_t stream) {
  Params p;
  p.x = (const char*)d_in[0]; p.wqkv = (const char*)d_in[1]; p.wo = (const char*)d_in[2];
  p.wg = (const char*)d_in[4]; p.wu = (const char*)d_in[5]; p.wd = (const char*)d_in[6];
  p.out = (char*)d_out; p.ws = (char*)d_ws;
  (void)hipFuncSetAttribute((const void*)k_fused, hipFuncAttributeMaxDynamicSharedMemorySize, LDS_BYTES);
  k_fused<<<256, 512, LDS_BYTES, stream>>>(p);
}
```

```cpp
#include <hip/hip_runtime.h>

typedef int i32x4 __attribute__((ext_vector_type(4)));
typedef unsigned u32x4 __attribute__((ext_vector_type(4)));
typedef unsigned u32x2 __attribute__((ext_vector_type(2)));
typedef float f32x4 __attribute__((ext_vector_type(4)));
typedef float f32x2 __attribute__((ext_vector_type(2)));
typedef float f32x16 __attribute__((ext_vector_type(16)));
typedef __bf16 bf16x8 __attribute__((ext_vector_type(8)));
typedef __bf16 bf16x2 __attribute__((ext_vector_type(2)));
typedef short s16x4 __attribute__((ext_vector_type(4)));
#define LAS __attribute__((address_space(3)))
#define DEVI __device__ __forceinline__

constexpr int S_ = 2048, D_ = 640, F_ = 1536;
constexpr unsigned X_BYTES = S_ * D_ * 2, WQ_BYTES = D_ * 3 * D_ * 2, WF_BYTES = D_ * F_ * 2, WO_BYTES = D_ * D_ * 2;
constexpr unsigned QKV_BYTES = S_ * 3 * D_ * 2;
constexpr unsigned AF_BYTES = 128u * 68u * 1024u;
constexpr unsigned W3_BYTES = 68u * 40u * 1024u;
constexpr unsigned WS_QKV = 1u << 20, WS_AF = 16u << 20, WS_W3 = 32u << 20;
constexpr unsigned C_FQ = 0x1000, C_FH = 0x8000, C_FO = 0xC000, C_PF = 0x30000, C_STAT = 0x40000, C_CS = 0x50000, C_BYTES = 1u << 20;
constexpr unsigned LDS_BYTES = 163840;
constexpr unsigned OOB = 0x80000000u;

DEVI i32x4 mk_srd(const void* p, unsigned n) {
  unsigned long long a = (unsigned long long)p;
  i32x4 r;
  r.x = __builtin_amdgcn_readfirstlane((int)(unsigned)a);
  r.y = __builtin_amdgcn_readfirstlane((int)(unsigned)(a >> 32));
  r.z = (int)n; r.w = 0x00020000;
  return r;
}
DEVI i32x4 sel_srd(bool c, i32x4 a, i32x4 b) { i32x4 r; r.x = c ? a.x : b.x; r.y = c ? a.y : b.y; r.z = c ? a.z : b.z; r.w = a.w; return r; }
template <int SC = 0> DEVI void dma16(unsigned ldsaddr, unsigned voff, i32x4 srd, unsigned soff) {
  if (SC) asm volatile("s_mov_b32 m0, %0\n\ts_nop 0\n\tbuffer_load_dwordx4 %1, %2, %3 offen sc1 lds"
                       :: "s"(ldsaddr), "v"(voff), "s"(srd), "s"(soff) : "memory");
  else    asm volatile("s_mov_b32 m0, %0\n\ts_nop 0\n\tbuffer_load_dwordx4 %1, %2, %3 offen lds"
                       :: "s"(ldsaddr), "v"(voff), "s"(srd), "s"(soff) : "memory");
}
template <int SC, int OFF = 0> DEVI u32x4 ld16(unsigned voff, i32x4 srd, unsigned soff) {
  u32x4 r;
  if (SC) asm volatile("buffer_load_dwordx4 %0, %1, %2, %3 offen offset:%4 sc1" : "=v"(r) : "v"(voff), "s"(srd), "s"(soff), "i"(OFF) : "memory");
  else    asm volatile("buffer_load_dwordx4 %0, %1, %2, %3 offen offset:%4" : "=v"(r) : "v"(voff), "s"(srd), "s"(soff), "i"(OFF) : "memory");
  return r;
}
template <int SC, int OFF = 0> DEVI u32x2 ld8(unsigned voff, i32x4 srd, unsigned soff) {
  u32x2 r;
  if (SC) asm volatile("buffer_load_dwordx2 %0, %1, %2, %3 offen offset:%4 sc1" : "=v"(r) : "v"(voff), "s"(srd), "s"(soff), "i"(OFF) : "memory");
  else    asm volatile("buffer_load_dwordx2 %0, %1, %2, %3 offen offset:%4" : "=v"(r) : "v"(voff), "s"(srd), "s"(soff), "i"(OFF) : "memory");
  return r;
}
template <int SC, int OFF = 0> DEVI unsigned ld4(unsigned voff, i32x4 srd, unsigned soff) {
  unsigned r;
  if (SC) asm volatile("buffer_load_dword %0, %1, %2, %3 offen offset:%4 sc1" : "=v"(r) : "v"(voff), "s"(srd), "s"(soff), "i"(OFF) : "memory");
  else    asm volatile("buffer_load_dword %0, %1, %2, %3 offen offset:%4" : "=v"(r) : "v"(voff), "s"(srd), "s"(soff), "i"(OFF) : "memory");
  return r;
}
template <int SC> DEVI void st16(u32x4 v, unsigned voff, i32x4 srd, unsigned soff) {
  if (SC) asm volatile("buffer_store_dwordx4 %0, %1, %2, %3 offen sc1\n\ts_nop 1" :: "v"(v), "v"(voff), "s"(srd), "s"(soff) : "memory");
  else    asm volatile("buffer_store_dwordx4 %0, %1, %2, %3 offen\n\ts_nop 1" :: "v"(v), "v"(voff), "s"(srd), "s"(soff) : "memory");
}
template <int SC, int OFF = 0> DEVI void st8(u32x2 v, unsigned voff, i32x4 srd, unsigned soff) {
  if (SC) asm volatile("buffer_store_dwordx2 %0, %1, %2, %3 offen offset:%4 sc1" :: "v"(v), "v"(voff), "s"(srd), "s"(soff), "i"(OFF) : "memory");
  else    asm volatile("buffer_store_dwordx2 %0, %1, %2, %3 offen offset:%4" :: "v"(v), "v"(voff), "s"(srd), "s"(soff), "i"(OFF) : "memory");
}
DEVI void st4s(unsigned v, unsigned voff, i32x4 srd) {
  asm volatile("buffer_store_dword %0, %1, %2, 0 offen sc1" :: "v"(v), "v"(voff), "s"(srd) : "memory");
}
template <int N> DEVI void wait_vm() { asm volatile("s_waitcnt vmcnt(%0)" :: "i"(N) : "memory"); }
#define WAIT_LGKM0() asm volatile("s_waitcnt lgkmcnt(0)" ::: "memory")
DEVI void bar() { asm volatile("s_barrier" ::: "memory"); }
#define SB() __builtin_amdgcn_sched_barrier(0)
#define PIN(x) asm volatile("" : "+v"(x))

DEVI unsigned pk2(float a, float b) { bf16x2 v = {(__bf16)a, (__bf16)b}; return __builtin_bit_cast(unsigned, v); }
DEVI float rbf(float a) { return (float)(__bf16)a; }
DEVI bf16x8 asbf(u32x4 v) { return __builtin_bit_cast(bf16x8, v); }
DEVI u32x2 trd(LAS char* p) { return __builtin_bit_cast(u32x2, __builtin_amdgcn_ds_read_tr16_b64_v4i16((LAS s16x4*)p)); }
DEVI u32x4 cat(u32x2 a, u32x2 b) { return __builtin_shufflevector(a, b, 0, 1, 2, 3); }
DEVI f32x4 mfma16(u32x4 a, u32x4 b, f32x4 c) { return __builtin_amdgcn_mfma_f32_16x16x32_bf16(asbf(a), asbf(b), c, 0, 0, 0); }
DEVI void mfma16a(u32x4 a, u32x4 b, f32x4& c) { asm volatile("v_mfma_f32_16x16x32_bf16 %0, %1, %2, %0" : "+v"(c) : "v"(a), "v"(b)); }
DEVI void mfma16z(u32x4 a, u32x4 b, f32x4& c) { asm volatile("v_mfma_f32_16x16x32_bf16 %0, %1, %2, 0" : "=&v"(c) : "v"(a), "v"(b)); }
DEVI int lane_id() { int l; asm volatile("v_mbcnt_lo_u32_b32 %0, -1, 0\n\tv_mbcnt_hi_u32_b32 %0, -1, %0" : "=v"(l)); return l; }
DEVI f32x16 mfma32(u32x4 a, u32x4 b, f32x16 c) { return __builtin_amdgcn_mfma_f32_32x32x16_bf16(asbf(a), asbf(b), c, 0, 0, 0); }
DEVI float blo(unsigned r) { return __uint_as_float(r << 16); }
DEVI float bhi(unsigned r) { return __uint_as_float(r & 0xffff0000u); }
template <int C> DEVI float dppf(float v) { return __uint_as_float(__builtin_amdgcn_update_dpp(0, __float_as_uint(v), C, 0xf, 0xf, false)); }
DEVI float wave_sum(float v) {
  v += dppf<0x128>(v); v += dppf<0x124>(v); v += dppf<0x122>(v); v += dppf<0x121>(v);
  auto a = __builtin_amdgcn_permlane16_swap(__float_as_uint(v), __float_as_uint(v), false, false);
  v = __uint_as_float(a[0]) + __uint_as_float(a[1]);
  auto b = __builtin_amdgcn_permlane32_swap(__float_as_uint(v), __float_as_uint(v), false, false);
  return __uint_as_float(b[0]) + __uint_as_float(b[1]);
}
DEVI float v_fma(float a, float b, float c) { float r; asm volatile("v_fma_f32 %0, %1, %2, %3" : "=v"(r) : "v"(a), "v"(b), "v"(c)); return r; }
DEVI float v_mul(float a, float b) { float r; asm volatile("v_mul_f32 %0, %1, %2" : "=v"(r) : "v"(a), "v"(b)); return r; }
DEVI float v_exp(float a) { float r; asm volatile("v_exp_f32 %0, %1" : "=v"(r) : "v"(a)); return r; }
DEVI float v_rcp(float a) { float r; asm volatile("v_rcp_f32 %0, %1" : "=v"(r) : "v"(a)); return r; }
DEVI unsigned v_cvt(float a, float b) { unsigned r; asm volatile("v_cvt_pk_bf16_f32 %0, %1, %2" : "=v"(r) : "v"(a), "v"(b)); return r; }
DEVI float dot2(unsigned w, unsigned sel, float c) { return __builtin_amdgcn_fdot2_f32_bf16(__builtin_bit_cast(bf16x2, w), __builtin_bit_cast(bf16x2, sel), c, false); }
DEVI bool any_lane(bool c) { return __builtin_amdgcn_ballot_w64(c) != 0; }
constexpr int SPIN = 1 << 15;
DEVI void poll_rest(unsigned v, bool need, unsigned tag, unsigned voff, i32x4 srd) {
  if (any_lane(need && v != tag)) {
    for (int it = 0; it < SPIN; it++) {
      __builtin_amdgcn_s_sleep(1);
      v = ld4<1>(voff, srd, 0);
      asm volatile("s_waitcnt vmcnt(0)" : "+v"(v) :: "memory");
      if (!any_lane(need && v != tag)) break;
    }
  }
}

#define TS(i)
#define TSDECL
#define TSOUT()
template <int I> struct IC { static constexpr int v = I; };
template <int B, int E, class F> DEVI void sfor(F&& f) { if constexpr (B < E) { f(IC<B>{}); sfor<B + 1, E>(f); } }

struct Params {
  const char *x, *wqkv, *wo, *wg, *wu, *wd;
  char *out, *ws;
};

extern __shared__ __attribute__((aligned(16))) char smem_[];

constexpr unsigned G1_X = 122880, L_PK = 155648, L_CS = 161792;
constexpr unsigned AT_K = 0, AT_V = 139264;
constexpr unsigned R_A = 40960, R_B = 81920, R_H = 24576, R_H3 = 122880;
constexpr int TU(int m) { return m < 12 ? m / 4 : m < 36 ? (m - 12) / 8 : 3 + (m - 36) / 12; }
constexpr int TK(int m) { return m < 12 ? 0 : m < 36 ? 1 + (((m - 12) / 4) & 1) : ((m - 36) / 4) % 3; }
constexpr int cl(int v, int lo, int hi) { return v < lo ? lo : v > hi ? hi : v; }

__global__ __launch_bounds__(512, 2) void k_fused(Params p) {
  TSDECL;
  LAS char* sm = (LAS char*)smem_;
  const unsigned lb = (unsigned)(size_t)sm;
  const int bid = blockIdx.x, xcd = bid & 7, loc = bid >> 3;
  const int wave = __builtin_amdgcn_readfirstlane(threadIdx.x >> 6);
  int lane = threadIdx.x & 63, t16 = lane & 15, g = lane >> 4;
  const int rb = (xcd & 1) * 8 + (loc & 7), jg = (xcd >> 1) * 4 + (loc >> 3), pr = jg >> 1, tyB = jg & 1;
  const int mw = (wave >> 1) & 1, nw = (wave >> 2) * 2 + (wave & 1);
  const bool isQ = tyB && nw >= 2;
  const i32x4 sx = mk_srd(p.x, X_BYTES), sc = mk_srd(p.ws, C_BYTES);
  const i32x4 sq = mk_srd(p.wqkv, WQ_BYTES), sg = mk_srd(p.wg, WF_BYTES), su = mk_srd(p.wu, WF_BYTES);
  const int rot = (xcd & 1) * 5;
  auto sidx = [&](int s) { return (unsigned)(s < 5 ? s + rot : s - rot); };
  const unsigned vx = (rb * 128 + 8 * wave + (lane >> 3)) * 1280u + (((lane & 7) ^ (((wave & 1) << 2) | (lane >> 4))) * 16u);
  const unsigned wcol = (((((lane & 7) >> 1) ^ (((lane >> 4) & 1) | ((wave & 1) << 1))) * 32u) + (lane & 1) * 16u);
  const unsigned vq = (8 * wave + (lane >> 3)) * 3840u + wcol, vg = (8 * wave + (lane >> 3)) * 3072u + wcol;
  const i32x4 s1 = sel_srd(tyB, sq, sg), s3 = sel_srd(tyB, sg, su);
  const unsigned v12 = tyB ? (pr == 7 ? OOB : vq) : vg;
  const unsigned c0 = (4 * pr + tyB) * 128u, c1 = tyB ? (4 * pr + 2) * 128u : 3 * pr * 128u;
  const unsigned c3 = tyB ? (3 * pr + 2) * 128u : 3 * pr * 128u, c4 = tyB ? c3 : c3 + 128u;
  const unsigned st1 = tyB ? 245760u : 196608u;
  const unsigned ldw = lb + wave * 1024u;
  auto issue_x = [&](int s, int b) { dma16(ldw + G1_X + (s & 1) * 16384u + b * 8192u, vx, sx, b * 81920u + sidx(s) * 128u); };
  auto issue_w = [&](int s, int b) {
    const unsigned d = ldw + (s % 3) * 40960u + b * 8192u;
    if (b == 0) dma16(d, vq, sq, sidx(s) * 245760u + c0);
    else if (b < 3) dma16(d, v12, s1, sidx(s) * st1 + c1 + (b - 1) * 128u);
    else if (b == 3) dma16(d, vg, s3, sidx(s) * 196608u + c3);
    else dma16(d, vg, su, sidx(s) * 196608u + c4);
  };
  issue_x(0, 0); issue_x(0, 1);
#pragma unroll
  for (int b = 0; b < 5; b++) issue_w(0, b);
  unsigned ep = ld4<1>(0u, sc, 0);
  issue_x(1, 0); issue_x(1, 1);
#pragma unroll
  for (int b = 0; b < 5; b++) issue_w(1, b);
  TS(1);
  const i32x4 sd = mk_srd(p.wd, WF_BYTES), so = mk_srd(p.wo, WO_BYTES);
  const i32x4 sw3 = mk_srd(p.ws + WS_W3, W3_BYTES);
  const unsigned kA = bid * 205u >> 10, cA = bid - kA * 5u, kB = (bid + 256u) * 205u >> 10, cB = bid + 256u - kB * 5u;
  i32x4 soB = so, sw3B = sw3;
  soB.z = bid < 84 ? (int)WO_BYTES : 0; sw3B.z = bid < 84 ? (int)W3_BYTES : 0;
  const unsigned vpk = (4 * wave + g) * 1280u + t16 * 16u;
  dma16(ldw + L_PK, vpk, sel_srd(kA < 48, sd, so), (kA < 48 ? kA : kA - 48) * 40960u + cA * 256u);
  unsigned tch;
  {
    const unsigned role = wave & 3, l = lane & 15;
    unsigned st = 2 + 2 * (wave >> 2) + (role == 3 ? lane >> 5 : (lane >> 4) & 1) + rot;
    st = st >= 10 ? st - 10 : st;
    const unsigned idx = loc * (role ? 12u : 16u) + l, row = role ? idx * 10923u >> 16 : idx >> 3, ln = idx - row * (role ? 6u : 8u);
    unsigned a = (st * 64u + row) * (role ? 3072u : 3840u) + ((xcd >> 1) * (role ? 6u : 8u) + ln) * 128u;
    if (lane >= 32 || (role && l >= 12)) a = OOB;
    if (role == 3) a = ((xcd & 1) * 1024u + loc * 32u + (lane & 31)) * 1280u + st * 128u;
    tch = ld4<0>(a, sel_srd(role < 2, sel_srd(role == 0, sq, sg), sel_srd(role == 2, su, sx)), 0);
  }
  unsigned tl[5];
  {
    const unsigned gb = (tyB ? 12 : 4) + 2 * nw, ub = (tyB ? 16 : 12) + 2 * nw, qb = 5 * nw - 8;
    tl[0] = isQ ? qb : nw; tl[1] = isQ ? qb + 1 : gb; tl[2] = isQ ? qb + 2 : gb + 1; tl[3] = isQ ? qb + 3 : ub; tl[4] = isQ ? qb + 4 : ub + 1;
  }
  LAS char* xa[2];
#pragma unroll
  for (int ks = 0; ks < 2; ks++) xa[ks] = sm + (G1_X + mw * 8192u + t16 * 128u + (((4 * ks + g) ^ (t16 >> 1)) * 16u));
  LAS char* wa[2][5];
  {
    const unsigned fl = ((t16 >> 3) & 1) | ((g & 1) << 1);
    const unsigned wl = g * 1024u + (t16 >> 2) * 128u + (t16 & 3) * 8u;
#pragma unroll
    for (int u = 0; u < 5; u++) {
      wa[0][u] = sm + (wl + (tl[u] >> 2) * 8192u + (((tl[u] & 3) ^ fl) * 32u));
      wa[1][u] = wa[0][u] + 81920; PIN(wa[1][u]);
    }
  }
  LAS char* pkr = sm + (L_PK + (4 * g + (t16 >> 2)) * 256u + wave * 32u + (t16 & 3) * 8u);
  unsigned vl = lane * 16u;
  f32x4 acc[4][5], cs[5];
  u32x4 xf[2][4];
  u32x2 wf[2][5][2];
  auto rd = [&](int s, int ks, int r) {
    if (r < 4) xf[ks][r] = *(LAS u32x4*)(xa[ks] + ((s & 1) * 16384 + r * 2048));
    else {
      const int u = (r - 4) >> 1, h = r & 1, sl = s % 3;
      wf[ks][u][h] = trd(wa[sl == 2][u] + ((sl == 1) * 40960 + ks * 4096 + h * 512));
    }
  };
  auto mf0 = [&](int n) { mfma16z(cat(wf[0][n >> 2][0], wf[0][n >> 2][1]), xf[0][n & 3], acc[n & 3][n >> 2]); };
  auto mf = [&](int ks, int n) { mfma16a(cat(wf[ks][n >> 2][0], wf[ks][n >> 2][1]), xf[ks][n & 3], acc[n & 3][n >> 2]); };
  const int cprod = mw == (wave >> 2) ? (loc & 7) : -1;
  const unsigned vcs = C_CS + jg * 25600u;
  unsigned vco = g == 0 ? vcs + (rot * 320u + t16) * 8u : OOB;
  PIN(vco);
  unsigned tag = 0;
  auto csum = [&](int s, int ks) {
    if (cprod == s) {
      const u32x4 ones = {0x3f803f80u, 0x3f803f80u, 0x3f803f80u, 0x3f803f80u};
#pragma unroll
      for (int u = 0; u < 5; u++) cs[u] = mfma16(ones, cat(wf[ks][u][0], wf[ks][u][1]), ks ? cs[u] : f32x4{0, 0, 0, 0});
      if (ks) {
#pragma unroll
        for (int u = 0; u < 5; u++) st8<1>(u32x2{__float_as_uint(cs[u][0]), tag}, vco, sc, s * 2560u + tl[u] * 128u);
      }
    }
  };
  const unsigned srow = rb * 128 + jg * 8 + wave;
  u32x4 r0; unsigned r1;
  float e[10], sacc = 0.f, mean = 0.f;
  auto stat = [&](int k) {
    if (k < 5) { const unsigned w = k < 4 ? r0[k & 3] : r1; e[2 * k] = blo(w); e[2 * k + 1] = bhi(w); sacc += e[2 * k] + e[2 * k + 1]; }
    else if (k >= 11 && k < 16) {
      const float d0 = e[2 * k - 22] - mean, d1 = e[2 * k - 21] - mean;
      sacc = __builtin_fmaf(d0, d0, sacc); PIN(sacc); sacc = __builtin_fmaf(d1, d1, sacc);
    } else if (k < 22) {
      const int q = k < 11 ? k - 5 : k - 16;
      if (q == 0) sacc += dppf<0x128>(sacc);
      else if (q == 1) sacc += dppf<0x124>(sacc);
      else if (q == 2) sacc += dppf<0x122>(sacc);
      else if (q == 3) sacc += dppf<0x121>(sacc);
      else if (q == 4) { auto a = __builtin_amdgcn_permlane16_swap(__float_as_uint(sacc), __float_as_uint(sacc), false, false); sacc = __uint_as_float(a[0]) + __uint_as_float(a[1]); }
      else {
        auto a = __builtin_amdgcn_permlane32_swap(__float_as_uint(sacc), __float_as_uint(sacc), false, false);
        sacc = (__uint_as_float(a[0]) + __uint_as_float(a[1])) * (1.0f / 640.0f);
        if (k == 10) { mean = sacc; sacc = 0.f; PIN(mean); }
      }
    } else if (k == 22) sacc = __builtin_amdgcn_sqrtf(rbf(rbf(sacc) + 1e-5f));
    else st8<1>(u32x2{pk2(mean, sacc), tag}, lane == 0 ? C_STAT + srow * 8u : OOB, sc, 0);
    PIN(sacc);
  };
  u32x2 gs[4], gc[10], pk[2];
  unsigned pf;
  const unsigned vgs = C_STAT + (rb * 128 + mw * 64 + t16) * 8u;
  const unsigned vgc = vcs + (wave * 40 + (lane < 40 ? lane : lane - 40)) * 8u;
  const unsigned vpf = C_PF + (wave * 32 + (lane & 31)) * 4u;
  constexpr int PB[10] = {0, 1, 2, 5, 6, 3, 4, 7, 8, 9};
  bool bad = false;
  float csv = 0.f;
  float nm[4], rs[4], sdr[4], rg[4];
  const int rt = xcd * 4 + (loc & 3), cg = loc >> 2;
  const unsigned w3b = (wave * 40 + cg * 5) * 1024u;
  u32x4 RW[4][5];
  auto ld_w3 = [&](int buf, int j, int u) { RW[buf][u] = ld16<1>(vl, sw3, w3b + j * 327680u + u * 1024u); };
  auto dma_w3 = [&](unsigned dst, int j, int u) { dma16<1>(dst + u * 1024u, vl, sw3, w3b + j * 327680u + u * 1024u); };

  wait_vm<10>();
  bar();
  TS(2);
#pragma unroll
  for (int r = 0; r < 14; r++) rd(0, 0, r);
  SB();
  sfor<0, 9>([&](auto S_) {
    constexpr int s = S_.v;
    if constexpr (s < 5) { csum(s, 0); SB(); }
    sfor<0, 20>([&](auto N_) {
      constexpr int n = N_.v;
      if constexpr (s == 0) mf0(n); else mf(0, n);
      if constexpr (n < 14) rd(s, 1, n);
      if constexpr (s == 0) { if constexpr (n % 4 == 1) issue_w(2, n / 4); }
      else if constexpr (s < 8) { if constexpr (n == 3 || n == 9 || n == 15) issue_w(s + 2, 2 + n / 6); }
      if constexpr (s == 2 && n == 17) r0 = ld16<0>(vl, sx, srow * 1280u);
      if constexpr (s == 2 && n == 18) r1 = ld4<0, 1024>(lane * 4u, sx, srow * 1280u);
      if constexpr (s == 4 && n < 8 && (n & 1)) stat(20 + n / 2);
      if constexpr (s == 6 && n >= 4 && n < 8) gs[n - 4] = ld8<1, (n - 4) * 128>(vgs, sc, 0);
      if constexpr (s == 6 && n >= 10 && n < 15) gc[n - 10] = ld8<1>(vgc, sc, PB[n - 10] * 2560u);
      if constexpr (s == 7) {
        if constexpr (n == 10) {
          asm volatile("s_waitcnt vmcnt(4)" : "+v"(gs[0]), "+v"(gs[1]), "+v"(gs[2]), "+v"(gs[3]), "+v"(gc[0]), "+v"(gc[1]), "+v"(gc[2]), "+v"(gc[3]),
                       "+v"(gc[4]), "+v"(gc[5]), "+v"(gc[6]), "+v"(gc[7]), "+v"(gc[8]), "+v"(gc[9]), "+v"(pf) :: "memory");
          bad = pf != tag;
        }
        if constexpr (n >= 10 && n < 14) bad |= gs[n - 10][1] != tag;
        if constexpr (n >= 11 && n < 16) { bad |= gc[2 * n - 22][1] != tag || gc[2 * n - 21][1] != tag; csv += __uint_as_float(gc[2 * n - 22][0]); PIN(csv); csv += __uint_as_float(gc[2 * n - 21][0]); PIN(csv); }
        if constexpr (n == 17) {
          if (any_lane(bad)) {
            for (int it = 0; it < SPIN; it++) {
              __builtin_amdgcn_s_sleep(1);
              sfor<0, 4>([&](auto I) { gs[I.v] = ld8<1, I.v * 128>(vgs, sc, 0); });
#pragma unroll
              for (int k = 0; k < 10; k++) gc[k] = ld8<1>(vgc, sc, PB[k] * 2560u);
              pf = ld4<1>(vpf, sc, 0);
              asm volatile("s_waitcnt vmcnt(0)" : "+v"(gs[0]), "+v"(gs[1]), "+v"(gs[2]), "+v"(gs[3]), "+v"(gc[0]), "+v"(gc[1]), "+v"(gc[2]), "+v"(gc[3]),
                           "+v"(gc[4]), "+v"(gc[5]), "+v"(gc[6]), "+v"(gc[7]), "+v"(gc[8]), "+v"(gc[9]), "+v"(pf) :: "memory");
              bad = pf != tag; csv = 0.f;
#pragma unroll
              for (int k = 0; k < 4; k++) bad |= gs[k][1] != tag;
#pragma unroll
              for (int k = 0; k < 10; k++) { bad |= gc[k][1] != tag; csv += __uint_as_float(gc[k][0]); PIN(csv); }
              if (!any_lane(bad)) break;
            }
          }
          if (lane < 40) *(LAS float*)(sm + L_CS + (wave * 40 + lane) * 4) = csv;
        }
      }
      if constexpr (s == 8 && n % 4 == 2) ld_w3(0, 0, n / 4);
      if constexpr (s == 8 && n >= 14 && n < 18) { rg[n - 14] = -1.44269504089f * rs[n - 14]; PIN(rg[n - 14]); }
      SB();
    });
    WAIT_LGKM0();
    if constexpr (s == 0) { asm volatile("s_waitcnt vmcnt(6)" : "+v"(ep) :: "memory"); tag = __builtin_amdgcn_readfirstlane(ep) + 1u; }
    else if constexpr (s == 1) asm volatile("s_waitcnt vmcnt(6)" : "+v"(tch) :: "memory");
    else if constexpr (s == 2) wait_vm<7>();
    else if constexpr (s == 3) asm volatile("s_waitcnt vmcnt(6)" : "+v"(r0), "+v"(r1) :: "memory");
    else if constexpr (s == 4) wait_vm<6>();
    else if constexpr (s == 5) wait_vm<5>();
    else if constexpr (s == 6) wait_vm<14>();
    else if constexpr (s == 8) wait_vm<10>();
    bar();
    TS(3 + s);
    SB();
    if constexpr (s < 8) {
      if constexpr (s < 5) { csum(s, 1); SB(); }
      if constexpr (s == 4) { if (wave == 0) st4s(tag, lane == 0 ? C_PF + bid * 4u : OOB, sc); }
      if constexpr (s == 7) { if (bid == 0 && wave == 0) st4s(tag, lane == 0 ? 0u : OOB, sc); }
      sfor<0, 20>([&](auto N_) {
        constexpr int n = N_.v;
        mf(1, n);
        if constexpr (n < 14) rd(s + 1, 0, n);
        if constexpr (s == 1 && n == 0) dma16(ldw + L_PK, vpk, soB, (kB - 48) * 40960u + cB * 256u);
        if constexpr (n == 1 || n == 5) issue_x(s + 2, n / 4);
        if constexpr (s < 7) { if constexpr (n == 13 || n == 17) issue_w(s + 3, n / 16); }
        if constexpr ((s == 0 || s == 2) && (n == 14 || n == 15)) pk[n - 14] = trd(pkr + (n - 14) * 4096);
        if constexpr (s == 0 && n == 19) st16<1>(cat(pk[0], pk[1]), vl, sw3, (kA * 40u + cA * 8u + wave) * 1024u);
        if constexpr (s == 2 && n == 19) st16<1>(cat(pk[0], pk[1]), vl, sw3B, (kB * 40u + cB * 8u + wave) * 1024u);
        if constexpr (s == 3) stat(n);
        if constexpr (s == 6 && n >= 6 && n < 11) gc[n - 1] = ld8<1>(vgc, sc, PB[n - 1] * 2560u);
        if constexpr (s == 6 && n == 12) pf = ld4<1>(vpf, sc, 0);
        if constexpr (s == 7) {
          if constexpr (n >= 7 && n % 3 == 1) dma_w3(lb + R_A + wave * 5120u, 4, (n - 7) / 3);
          if constexpr (n >= 8 && n < 12) { nm[n - 8] = -blo(gs[n - 8][0]); sdr[n - 8] = bhi(gs[n - 8][0]); PIN(nm[n - 8]); PIN(sdr[n - 8]); }
          if constexpr (n >= 14 && n < 18) { rs[n - 14] = __builtin_amdgcn_rcpf(sdr[n - 14]); PIN(rs[n - 14]); }
        }
        SB();
      });
    }
  });
  const i32x4 sqkv = mk_srd(p.ws + WS_QKV, QKV_BYTES), saf = mk_srd(p.ws + WS_AF, AF_BYTES);
  const unsigned Rh = rb * 2 + mw;
  const unsigned qt0 = 16 * pr + 4 * tyB;
  {
    u32x4 xg[4], qv, hv[4];
    u32x2 w9[5][2][2];
    f32x4 c5[5];
    float t[4], rq[4];
    LAS char* csr = sm + (L_CS + g * 16);
    auto common = [&](auto M_) {
      constexpr int m = M_.v, u = TU(m), k = TK(m), i = m & 3;
      mfma16a(k == 0 ? cat(wf[1][u][0], wf[1][u][1]) : cat(w9[u][k ? k - 1 : 0][0], w9[u][k ? k - 1 : 0][1]), k == 0 ? xf[1][i] : k == 1 ? xf[0][i] : xg[i], acc[i][u]);
      if constexpr (m < 4) xf[0][m] = *(LAS u32x4*)(xa[0] + (16384 + m * 2048));
      if constexpr (m >= 6 && m < 10) xg[m - 6] = *(LAS u32x4*)(xa[1] + (16384 + (m - 6) * 2048));
      constexpr int q = m == 4 || m == 5 ? m - 4 : m >= 10 && m < 16 ? m - 8 : m >= 20 && m < 24 ? m - 12 : m >= 30 && m < 34 ? m - 18 : m >= 42 && m < 46 ? m - 26 : -1;
      if constexpr (q >= 0) w9[q >> 2][(q >> 1) & 1][q & 1] = trd(wa[0][q >> 2] + (((q >> 1) & 1) * 4096 + (q & 1) * 512));
      if constexpr (m == 17 || m == 25 || m == 34 || m == 46 || m == 54) { constexpr int uu = m == 17 ? 0 : m == 25 ? 1 : m == 34 ? 2 : m == 46 ? 3 : 4; c5[uu] = *(LAS f32x4*)(csr + tl[uu] * 64u); }
      if constexpr (m % 3 == 1 && m < 15) dma_w3(lb + R_B + wave * 5120u, 5, m / 3);
    };
    auto qop = [&](auto U_, auto K_) {
      constexpr int u = U_.v, k = K_.v;
      const unsigned gt = qt0 + tl[u];
      if constexpr (k < 4) {
        const float qs = gt < 40u ? 0.22360679775f * 1.44269504089f : 1.0f;
        asm volatile("v_mul_f32 %0, %1, %2" : "=v"(rq[k]) : "s"(qs), "v"(rs[k]));
      } else if constexpr (k == 24 || k == 45) {
        i32x4 s = sqkv; s.z = gt < 120u ? (int)QKV_BYTES : 0;
        st16<1>(qv, vl, s, ((Rh * 2 + (k == 45)) * 480u + gt * 4u) * 256u);
      } else {
        constexpr int kk = k < 24 ? k - 4 : k - 5, i = kk / 10, j = kk % 10;
        if constexpr (j < 4) t[j] = v_fma(nm[i], c5[u][j], acc[i][u][j]);
        else if constexpr (j < 8) t[j - 4] = v_mul(t[j - 4], rq[i]);
        else qv[(i & 1) * 2 + j - 8] = v_cvt(t[2 * j - 16], t[2 * j - 15]);
      }
    };
    auto gop = [&](auto K_) {
      constexpr int k = K_.v;
      if constexpr (k < 160) {
        constexpr int u = 1 + k / 80, i = (k % 80) / 20, ph = (k % 20) / 4, j = k & 3;
        if constexpr (ph == 0) t[j] = v_fma(nm[i], c5[u][j], acc[i][u][j]);
        else if constexpr (ph == 1) t[j] = v_mul(t[j], rg[i]);
        else if constexpr (ph == 2) t[j] = v_exp(t[j]);
        else if constexpr (ph == 3) t[j] = v_fma(t[j], sdr[i], sdr[i]);
        else acc[i][u][j] = v_rcp(t[j]);
      } else {
        constexpr int h = k >= 200, kk = h ? k - 200 : k - 160, i = h ? kk / 11 : kk / 10, j = h ? kk % 11 : kk % 10;
        if constexpr (j < 4) t[j] = v_fma(nm[i], c5[3 + h][j], acc[i][3 + h][j]);
        else if constexpr (j < 8) t[j - 4] = v_mul(t[j - 4], acc[i][1 + h][j - 4]);
        else if constexpr (j < 10) hv[i][2 * h + j - 8] = v_cvt(t[2 * j - 16], t[2 * j - 15]);
        else st16<1>(hv[i], vl, saf, ((Rh * 4 + i) * 68u + 6 * pr + 4 * tyB + nw) * 1024u);
      }
    };
    if (!isQ) {
      sfor<0, 60>([&](auto M_) {
        constexpr int m = M_.v;
        common(M_);
        sfor<cl((m - 22) * 5, 0, 46), cl((m - 21) * 5, 0, 46)>([&](auto K_) { qop(IC<0>{}, K_); });
        sfor<cl((m - 32) * 7, 0, 200), cl((m - 31) * 7, 0, 200)>(gop);
        SB();
      });
      TS(12);
      sfor<196, 244>(gop);
      TS(13);
      wait_vm<4>();
#pragma unroll
      for (int u = 0; u < 5; u++) asm volatile("" : "=v"(acc[0][u]), "=v"(acc[1][u]), "=v"(acc[2][u]), "=v"(acc[3][u]), "=v"(wf[1][u][0]), "=v"(wf[1][u][1]));
      asm volatile("" : "=v"(xf[1][0]), "=v"(xf[1][1]), "=v"(xf[1][2]), "=v"(xf[1][3]));
    }
    int isQ2 = tyB & (wave >> 2);
    asm volatile("" : "+s"(isQ2));
    if (isQ2) {
      sfor<0, 60>([&](auto M_) {
        constexpr int m = M_.v;
        common(M_);
        constexpr int u = m < 30 ? 0 : m < 38 ? 1 : m < 50 ? 2 : 3, g0 = u == 0 ? 22 : u == 1 ? 30 : u == 2 ? 38 : 50;
        sfor<cl((m - g0) * 6, 0, 46), cl((m - g0 + 1) * 6, 0, 46)>([&](auto K_) { qop(IC<u>{}, K_); });
        SB();
      });
      TS(12);
      asm volatile("s_nop 3" ::: "memory");
      sfor<0, 46>([&](auto K_) { qop(IC<4>{}, K_); });
      TS(13);
      wait_vm<0>();
    }
  }
  TS(14);
  lane = lane_id(); t16 = lane & 15; g = lane >> 4; vl = lane * 16u;
  st4s(tag, lane < (isQ ? 5 : 1) ? C_FQ + (Rh * 128u + qt0 + tl[0] + lane) * 4u : OOB, sc);
  const int head = xcd * 4 + (loc & 3), ch = loc >> 2;
  const int q32 = lane & 31, hi = lane >> 5;
  const unsigned ab = (rt * 4 * 68 + wave) * 1024u;
  u32x4 A[4][4];
  auto ld_a = [&](int buf, int j, int i, unsigned v) { A[buf][i] = ld16<1>(v, saf, ab + j * 8192u + i * 69632u); };
  {
    const int sec = lane >> 1, rr = sec == 0 ? ch * 4 + (wave >> 1) : ch * 4 - 4 + wave;
    const bool need = lane < 6 && rr >= 0;
    const unsigned vf = need ? C_FQ + (rr * 128u + ((sec * 640u + head * 20u) >> 4) + (lane & 1)) * 4u : OOB;
    unsigned v = ld4<1>(vf, sc, 0);
#pragma unroll
    for (int u = 0; u < 5; u++) ld_w3(1, 1, u);
    asm volatile("s_waitcnt vmcnt(5)" : "+v"(v) :: "memory");
    TS(15);
    st4s(tag, (lane == 0 && !isQ) ? C_FH + (Rh * 64u + 6 * pr + 4 * tyB + nw) * 4u : OOB, sc);
    poll_rest(v, need, tag, vf, sc);
  }
  TS(16);
  f32x16 o;
  {
    const int tbq = ch * 8 + wave, kt0 = ch * 8 - 8 + 2 * wave;
    const unsigned kb = kt0 < 0 ? OOB : (unsigned)(kt0 * 480 + 160 + 5 * head) * 256u;
    const unsigned va = kb + vl, vb = va + 122880u;
    const unsigned vt = kb + (g & 1) * 122880u + (g >> 1) * 40960u + 1024u + t16 * 16u;
    u32x4 k0 = ld16<1>(va, sqkv, 0), k1 = ld16<1>(vb, sqkv, 0), v0 = ld16<1>(va, sqkv, 40960u), v1 = ld16<1>(vb, sqkv, 40960u);
    u32x4 tlq = ld16<1>(vt, sqkv, 0);
    const unsigned qa = (unsigned)(tbq * 480 + 5 * head) * 256u + (q32 & 15) * 16u + (q32 >> 4) * 8u;
    u32x2 qa0 = ld8<1>(qa + hi * 512u, sqkv, 0), qa1 = ld8<1, 256>(qa + hi * 512u, sqkv, 0), qa2 = ld8<1, 1024>(qa, sqkv, 0);
#pragma unroll
    for (int u = 0; u < 10; u++) ld_w3(2 + u / 5, 2 + u / 5, u % 5);
    bar();
    *(LAS u32x2*)(sm + AT_V + (wave * 64 + lane) * 48 + 40) = u32x2{0x3f803f80u, 0x3f803f80u};
    *(LAS u32x2*)(sm + AT_K + (wave * 64 + lane) * 48 + 40) = u32x2{(ch == 0 && wave < 4) ? 0x3f80fe00u : 0x3f800000u, 0u};
    asm volatile("s_waitcnt vmcnt(13)" : "+v"(k0), "+v"(k1), "+v"(v0), "+v"(v1), "+v"(tlq) :: "memory");
    TS(17);
    {
      LAS char* kw = sm + (wave * 3072 + t16 * 48 + g * 8);
      auto put = [&](LAS char* a, u32x4 v) { *(LAS u32x2*)a = u32x2{v[0], v[1]}; *(LAS u32x2*)(a + 768) = u32x2{v[2], v[3]}; };
      put(kw + AT_K, k0); put(kw + AT_K + 1536, k1); put(kw + AT_V, v0); put(kw + AT_V + 1536, v1);
      put(sm + (wave * 3072 + (g & 1) * 1536 + (g >> 1) * AT_V + t16 * 48 + 32), tlq);
    }
    const unsigned vfh = lane < 6 ? C_FH + (rt * 64u + wave + 8u * lane) * 4u : OOB;
    unsigned hf = ld4<1>(vfh, sc, 0);
    asm volatile("s_waitcnt vmcnt(11)" : "+v"(qa0), "+v"(qa1), "+v"(qa2) :: "memory");
    if (wave < 4) {
#pragma unroll
      for (int u = 0; u < 5; u++) dma_w3(lb + (wave < 3 ? R_H + wave * 5120u : R_H3), 8, u);
    }
    const u32x4 Q1 = cat(qa0, qa1);
    u32x4 Q2 = {hi ? 0u : qa2[0], hi ? 0u : qa2[1], hi ? 0u : 0x00003f80u, 0u};
    WAIT_LGKM0();
    bar();
    TS(18);
    LAS char* kp = sm + AT_K + wave * 1536 + q32 * 48;
    LAS char* kp1 = kp + hi * 16;
    const int vq_ = 4 * (g & 1) + (t16 & 3);
    LAS char* vp = sm + AT_V + wave * 1536 + (4 * (g >> 1) + (t16 >> 2)) * 48 + (vq_ > 5 ? 5 : vq_) * 8;
    f32x16 z;
#pragma unroll
    for (int r = 0; r < 16; r++) z[r] = 0.f;
    auto scq = [&](int off, u32x4 q2) {
      const u32x4 a = *(LAS u32x4*)(kp1 + off), b = *(LAS u32x4*)(kp + off + 32);
      return mfma32(b, q2, mfma32(a, Q1, z));
    };
    auto pv = [&](int off, const unsigned* pk_) {
      const u32x4 V1 = cat(trd(vp + off), trd(vp + off + 384)), V2 = cat(trd(vp + off + 768), trd(vp + off + 1152));
      o = mfma32(V1, u32x4{pk_[0], pk_[1], pk_[2], pk_[3]}, o);
      o = mfma32(V2, u32x4{pk_[4], pk_[5], pk_[6], pk_[7]}, o);
    };
    const int dl = q32 - 4 * hi;
    auto xmax = [&](float m) {
      auto r = __builtin_amdgcn_permlane32_swap(__float_as_uint(m), __float_as_uint(m), false, false);
      return __builtin_fmaxf(__uint_as_float(r[0]), __uint_as_float(r[1]));
    };
    auto edge = [&](const f32x16& ee, float m) {
      unsigned pa[8], pb[8];
#pragma unroll
      for (int r = 0; r < 16; r += 2) {
        const bool b0 = (r & 3) + 8 * (r >> 2) <= dl, b1 = ((r + 1) & 3) + 8 * (r >> 2) <= dl;
        const unsigned x = pk2(__builtin_amdgcn_exp2f(ee[r] - m), __builtin_amdgcn_exp2f(ee[r + 1] - m));
        const unsigned mk = (b0 ? 0xffffu : 0u) | (b1 ? 0xffff0000u : 0u);
        pb[r >> 1] = x & mk; pa[r >> 1] = x & ~mk;
      }
      pv(0, pa); pv(8 * 1536, pb);
    };
    auto merged = [&]() {
      const f32x16 sa = scq(0, Q2), sb = scq(8 * 1536, Q2);
      f32x16 ee;
#pragma unroll
      for (int r = 0; r < 16; r++) ee[r] = ((r & 3) + 8 * (r >> 2) <= dl) ? sb[r] : sa[r];
      return ee;
    };
    auto vmax = [&](const f32x16& ee, float m) {
#pragma unroll
      for (int r = 0; r < 16; r += 2) m = __builtin_fmaxf(__builtin_fmaxf(m, ee[r]), ee[r + 1]);
      return m;
    };
    o = z;
    {
      const f32x16 ee = merged();
      const unsigned mb = pk2(xmax(vmax(ee, -3.0e38f)), 0.f) << 16;
      const float m = __uint_as_float(mb);
      const u32x4 Q3 = {Q2[0], Q2[1], hi ? 0u : ((mb ^ 0x80000000u) | 0x3f80u), 0u};
      f32x16 sn = scq(1536, Q3);
      edge(ee, m);
      sfor<1, 8>([&](auto J) {
        constexpr int j = J.v;
        const f32x16 s = sn;
        if constexpr (j < 7) sn = scq((j + 1) * 1536, Q3);
        if constexpr (j == 2) {
          asm volatile("s_waitcnt vmcnt(0)" : "+v"(hf) :: "memory");
          TS(19);
          poll_rest(hf, lane < 6, tag, vfh, sc);
        }
        if constexpr (j >= 2 && j <= 7) { ld_a((j - 2) / 2, (j - 2) / 2, 2 * (j & 1), vl); ld_a((j - 2) / 2, (j - 2) / 2, 2 * (j & 1) + 1, vl); }
        unsigned pk_[8];
#pragma unroll
        for (int r = 0; r < 16; r += 2) pk_[r >> 1] = pk2(__builtin_amdgcn_exp2f(s[r]), __builtin_amdgcn_exp2f(s[r + 1]));
        pv(j * 1536, pk_);
      });
    }
    {
      float mx = 0.f;
#pragma unroll
      for (int r = 0; r < 12; r += 2) mx = __builtin_fmaxf(__builtin_fmaxf(mx, __builtin_fabsf(o[r])), __builtin_fabsf(o[r + 1]));
      if (any_lane(!(o[12] >= 5.9604645e-8f && o[12] <= 16777216.f && mx < 3.0e38f))) {
        float m = vmax(merged(), -3.0e38f);
#pragma nounroll
        for (int j = 1; j < 8; j++) m = vmax(scq(j * 1536, Q2), m);
        m = xmax(m);
        o = z;
        edge(merged(), m);
#pragma nounroll
        for (int j = 1; j < 8; j++) {
          const f32x16 s = scq(j * 1536, Q2);
          unsigned pk_[8];
#pragma unroll
          for (int r = 0; r < 16; r += 2) pk_[r >> 1] = pk2(__builtin_amdgcn_exp2f(s[r] - m), __builtin_amdgcn_exp2f(s[r + 1] - m));
          pv(j * 1536, pk_);
        }
      }
    }
    TS(20);
    const float inv = __builtin_amdgcn_rcpf(o[12]);
    const unsigned tb_ = (unsigned)(tbq * 2 + (q32 >> 4)) * 68u + 48u;
    auto st_o = [&](int r0_, unsigned quad, bool ok) {
      const unsigned Q = 5 * head + quad;
      const unsigned a = (tb_ + (Q >> 3)) * 1024u + (Q & 3) * 256u + (q32 & 15) * 16u + ((Q >> 2) & 1) * 8u;
      st8<1>(u32x2{pk2(o[r0_] * inv, o[r0_ + 1] * inv), pk2(o[r0_ + 2] * inv, o[r0_ + 3] * inv)}, ok ? a : OOB, saf, 0);
    };
    st_o(0, hi, true); st_o(4, 2 + hi, true); st_o(8, 4, hi == 0);
  }
#pragma unroll
  for (int i = 0; i < 4; i++) ld_a(3, 3, i, vl);
  lane = lane_id(); t16 = lane & 15; g = lane >> 4;
  LAS char* wl3 = sm + (R_A + wave * 5120 + lane * 16);
  LAS char* wlh = sm + ((wave < 3 ? R_H + wave * 5120 : R_H3) + lane * 16);
  const unsigned vl8 = wave < 4 ? vl : OOB;
  const unsigned vfo = C_FO + (rt * 64u + lane) * 4u;
  bool needo;
  {
    const int h5 = 5 * (lane & 31);
    auto ov = [&](int k) { return h5 + 4 >= 8 * k && h5 <= 8 * k + 7; };
    needo = ov(wave) || ov(wave + 8) || ov(wave + 16);
  }
  unsigned vo[3], lo_[3];
#pragma unroll
  for (int q = 0; q < 3; q++) {
    const unsigned c = ((wave < 4 ? 3 * wave : 4 + 2 * wave) + q) * 64u + lane, row = c * 3277u >> 16, pos = c - row * 20u;
    vo[q] = (q == 2 && wave >= 4) ? OOB : (rt * 64u + row) * 1280u + cg * 160u + pos * 8u;
    lo_[q] = R_A + (row >> 5) * 40960u + (row & 31) * 160u + ((pos ^ ((row >> 2) & 3)) * 8u);
    PIN(lo_[q]);
  }
  f32x4 fa[4][5];
  unsigned of = 0;
  u32x2 xr[3];
#define PINA(n) "+v"(A[n][0]), "+v"(A[n][1]), "+v"(A[n][2]), "+v"(A[n][3])
#define PINW(n) "+v"(RW[n][0]), "+v"(RW[n][1]), "+v"(RW[n][2]), "+v"(RW[n][3]), "+v"(RW[n][4])
  asm volatile("s_waitcnt vmcnt(15)" : PINA(0), PINW(0), PINW(1), PINW(2), PINW(3) :: "memory");
  TS(21);
  auto step = [&](auto J_) {
    constexpr int j = J_.v, wb = j < 4 ? j : j == 4 ? 2 : j == 5 ? 3 : j - 6, abuf = j & 3;
    if constexpr (j == 1) asm volatile("s_waitcnt vmcnt(14)" : PINA(1) :: "memory");
    if constexpr (j == 2) {
      asm volatile("s_waitcnt vmcnt(16)" : PINA(2) :: "memory");
      st4s(tag, lane == 0 ? C_FO + ((ch * 8 + wave) * 32u + head) * 4u : OOB, sc);
      TS(22);
    }
    if constexpr (j == 3) asm volatile("s_waitcnt vmcnt(22)" : PINA(3) :: "memory");
    if constexpr (j == 4) {
      asm volatile("s_waitcnt vmcnt(0)" : PINA(0), PINA(1), PINW(0), PINW(1), "+v"(of), "+v"(xr[0]), "+v"(xr[1]), "+v"(xr[2]) :: "memory");
      TS(23);
      poll_rest(of, needo, tag, vfo, sc);
      TS(24);
    }
    if constexpr (j == 6) { asm volatile("s_waitcnt vmcnt(8)" : PINA(2) :: "memory"); TS(25); }
    if constexpr (j == 7) asm volatile("s_waitcnt vmcnt(4)" : PINA(3) :: "memory");
    SB();
    sfor<0, 20>([&](auto K_) {
      constexpr int k = K_.v, u = k >> 2, i = k & 3;
      if constexpr (j == 0) mfma16z(RW[wb][u], A[abuf][i], fa[i][u]);
      else mfma16a(RW[wb][u], A[abuf][i], fa[i][u]);
      if constexpr (j == 0 && k >= 4 && k < 7) xr[k - 4] = ld8<0>(vo[k - 4], sx, 0);
      if constexpr ((j == 1 || j == 2) && k < 18 && (k & 1)) { if constexpr (k < 8) ld_a(j - 1, j + 3, k >> 1, vl); else ld_w3(j - 1, j + 5, (k >> 1) - 4); }
      if constexpr (j == 3 && k == 6) of = ld4<1>(vfo, sc, 0);
      if constexpr ((j == 3 || j == 4) && k >= 8 && k < 13) RW[j - 1][k - 8] = *(LAS u32x4*)(wl3 + ((j - 3) * 40960 + (k - 8) * 1024));
      if constexpr (j == 4 && k < 8) ld_a(2 + (k >> 2), 6 + (k >> 2), k & 3, vl);
      if constexpr (j == 5 && k < 4) ld_a(0, 8, k, vl8);
      if constexpr (j == 7 && k >= 8 && k < 13) RW[2][k - 8] = *(LAS u32x4*)(wlh + (k - 8) * 1024);
      SB();
    });
  };
  sfor<0, 8>(step);
  asm volatile("s_waitcnt vmcnt(0)" : PINA(0) :: "memory");
  if (wave < 4) step(IC<8>{});
  asm volatile("s_nop 7" ::: "memory");
  SB();
  TS(26);
  {
    LAS char* pw = sm + (R_A + wave * 5120 + t16 * 160 + ((g ^ (t16 >> 2)) * 8));
#pragma unroll
    for (int i = 0; i < 4; i++)
#pragma unroll
      for (int u = 0; u < 5; u++)
        *(LAS u32x2*)(pw + ((i >> 1) * 40960 + (i & 1) * 2560 + u * 32)) = u32x2{pk2(fa[i][u][0], fa[i][u][1]), pk2(fa[i][u][2], fa[i][u][3])};
    WAIT_LGKM0();
    TS(27);
    bar();
    TS(28);
    const i32x4 sout = mk_srd(p.out, X_BYTES);
    unsigned slo = 0x00003f80u, shi = 0x3f800000u;
    asm volatile("" : "+s"(slo), "+s"(shi));
    auto fin = [&](int q) {
      float a0 = blo(xr[q][0]), a1 = bhi(xr[q][0]), a2 = blo(xr[q][1]), a3 = bhi(xr[q][1]);
#pragma unroll
      for (int w = 0; w < 8; w++) {
        const u32x2 v = *(LAS u32x2*)(sm + lo_[q] + w * 5120);
        a0 = dot2(v[0], slo, a0); a1 = dot2(v[0], shi, a1); a2 = dot2(v[1], slo, a2); a3 = dot2(v[1], shi, a3);
      }
      st8<0>(u32x2{pk2(a0, a1), pk2(a2, a3)}, vo[q], sout, 0);
    };
    fin(0); fin(1);
    if (wave < 4) fin(2);
  }
  TSOUT();
}

extern "C" void kernel_launch(void* const* d_in, const int* in_sizes, int n_in, void* d_out, int out_size, void* d_ws,
                              size_t ws_size, hipStream_t stream) {
  Params p;
  p.x = (const char*)d_in[0]; p.wqkv = (const char*)d_in[1]; p.wo = (const char*)d_in[2];
  p.wg = (const char*)d_in[4]; p.wu = (const char*)d_in[5]; p.wd = (const char*)d_in[6];
  p.out = (char*)d_out; p.ws = (char*)d_ws;
  (void)hipFuncSetAttribute((const void*)k_fused, hipFuncAttributeMaxDynamicSharedMemorySize, LDS_BYTES);
  k_fused<<<256, 512, LDS_BYTES, stream>>>(p);
}
```

```cpp
#include <hip/hip_runtime.h>

typedef int i32x4 __attribute__((ext_vector_type(4)));
typedef unsigned u32x4 __attribute__((ext_vector_type(4)));
typedef unsigned u32x2 __attribute__((ext_vector_type(2)));
typedef float f32x4 __attribute__((ext_vector_type(4)));
typedef float f32x2 __attribute__((ext_vector_type(2)));
typedef float f32x16 __attribute__((ext_vector_type(16)));
typedef __bf16 bf16x8 __attribute__((ext_vector_type(8)));
typedef __bf16 bf16x2 __attribute__((ext_vector_type(2)));
typedef short s16x4 __attribute__((ext_vector_type(4)));
#define LAS __attribute__((address_space(3)))
#define DEVI __device__ __forceinline__

constexpr int S_ = 2048, D_ = 640, F_ = 1536;
constexpr unsigned X_BYTES = S_ * D_ * 2, WQ_BYTES = D_ * 3 * D_ * 2, WF_BYTES = D_ * F_ * 2, WO_BYTES = D_ * D_ * 2;
constexpr unsigned QKV_BYTES = S_ * 3 * D_ * 2;
constexpr unsigned AF_BYTES = 128u * 68u * 1024u;
constexpr unsigned W3_BYTES = 68u * 40u * 1024u;
constexpr unsigned WS_QKV = 1u << 20, WS_AF = 16u << 20, WS_W3 = 32u << 20, WS_BYTES = 40u << 20;
constexpr unsigned C_FQ = 0x1000, C_FH = 0x8000, C_FO = 0xC000, C_PF = 0x30000, C_STAT = 0x40000, C_CS = 0x50000;
constexpr unsigned LDS_BYTES = 163840;
constexpr unsigned OOB = 0x80000000u;

DEVI i32x4 mk_srd(const void* p, unsigned n) {
  unsigned long long a = (unsigned long long)p;
  i32x4 r;
  r.x = __builtin_amdgcn_readfirstlane((int)(unsigned)a);
  r.y = __builtin_amdgcn_readfirstlane((int)(unsigned)(a >> 32));
  r.z = (int)n; r.w = 0x00020000;
  return r;
}
DEVI i32x4 sel_srd(bool c, i32x4 a, i32x4 b) { i32x4 r; r.x = c ? a.x : b.x; r.y = c ? a.y : b.y; r.z = c ? a.z : b.z; r.w = a.w; return r; }
template <int SC = 0> DEVI void dma16(unsigned ldsaddr, unsigned voff, i32x4 srd, unsigned soff) {
  if (SC) asm volatile("s_mov_b32 m0, %0\n\ts_nop 0\n\tbuffer_load_dwordx4 %1, %2, %3 offen sc1 lds"
                       :: "s"(ldsaddr), "v"(voff), "s"(srd), "s"(soff) : "memory");
  else    asm volatile("s_mov_b32 m0, %0\n\ts_nop 0\n\tbuffer_load_dwordx4 %1, %2, %3 offen lds"
                       :: "s"(ldsaddr), "v"(voff), "s"(srd), "s"(soff) : "memory");
}
template <int SC, int OFF = 0> DEVI u32x4 ld16(unsigned voff, i32x4 srd, unsigned soff) {
  u32x4 r;
  if (SC) asm volatile("buffer_load_dwordx4 %0, %1, %2, %3 offen offset:%4 sc1" : "=v"(r) : "v"(voff), "s"(srd), "s"(soff), "i"(OFF) : "memory");
  else    asm volatile("buffer_load_dwordx4 %0, %1, %2, %3 offen offset:%4" : "=v"(r) : "v"(voff), "s"(srd), "s"(soff), "i"(OFF) : "memory");
  return r;
}
template <int SC, int OFF = 0> DEVI u32x2 ld8(unsigned voff, i32x4 srd, unsigned soff) {
  u32x2 r;
  if (SC) asm volatile("buffer_load_dwordx2 %0, %1, %2, %3 offen offset:%4 sc1" : "=v"(r) : "v"(voff), "s"(srd), "s"(soff), "i"(OFF) : "memory");
  else    asm volatile("buffer_load_dwordx2 %0, %1, %2, %3 offen offset:%4" : "=v"(r) : "v"(voff), "s"(srd), "s"(soff), "i"(OFF) : "memory");
  return r;
}
template <int SC, int OFF = 0> DEVI unsigned ld4(unsigned voff, i32x4 srd, unsigned soff) {
  unsigned r;
  if (SC) asm volatile("buffer_load_dword %0, %1, %2, %3 offen offset:%4 sc1" : "=v"(r) : "v"(voff), "s"(srd), "s"(soff), "i"(OFF) : "memory");
  else    asm volatile("buffer_load_dword %0, %1, %2, %3 offen offset:%4" : "=v"(r) : "v"(voff), "s"(srd), "s"(soff), "i"(OFF) : "memory");
  return r;
}
template <int SC> DEVI void st16(u32x4 v, unsigned voff, i32x4 srd, unsigned soff) {
  if (SC) asm volatile("buffer_store_dwordx4 %0, %1, %2, %3 offen sc1\n\ts_nop 1" :: "v"(v), "v"(voff), "s"(srd), "s"(soff) : "memory");
  else    asm volatile("buffer_store_dwordx4 %0, %1, %2, %3 offen\n\ts_nop 1" :: "v"(v), "v"(voff), "s"(srd), "s"(soff) : "memory");
}
template <int SC, int OFF = 0> DEVI void st8(u32x2 v, unsigned voff, i32x4 srd, unsigned soff) {
  if (SC) asm volatile("buffer_store_dwordx2 %0, %1, %2, %3 offen offset:%4 sc1" :: "v"(v), "v"(voff), "s"(srd), "s"(soff), "i"(OFF) : "memory");
  else    asm volatile("buffer_store_dwordx2 %0, %1, %2, %3 offen offset:%4" :: "v"(v), "v"(voff), "s"(srd), "s"(soff), "i"(OFF) : "memory");
}
DEVI void st4s(unsigned v, unsigned voff, i32x4 srd) {
  asm volatile("buffer_store_dword %0, %1, %2, 0 offen sc1" :: "v"(v), "v"(voff), "s"(srd) : "memory");
}
template <int N> DEVI void wait_vm() { asm volatile("s_waitcnt vmcnt(%0)" :: "i"(N) : "memory"); }
#define WAIT_LGKM0() asm volatile("s_waitcnt lgkmcnt(0)" ::: "memory")
DEVI void bar() { asm volatile("s_barrier" ::: "memory"); }
#define SB() __builtin_amdgcn_sched_barrier(0)
#define PIN(x) asm volatile("" : "+v"(x))

DEVI unsigned pk2(float a, float b) { bf16x2 v = {(__bf16)a, (__bf16)b}; return __builtin_bit_cast(unsigned, v); }
DEVI float rbf(float a) { return (float)(__bf16)a; }
DEVI bf16x8 asbf(u32x4 v) { return __builtin_bit_cast(bf16x8, v); }
DEVI u32x2 trd(LAS char* p) { return __builtin_bit_cast(u32x2, __builtin_amdgcn_ds_read_tr16_b64_v4i16((LAS s16x4*)p)); }
DEVI u32x4 cat(u32x2 a, u32x2 b) { return __builtin_shufflevector(a, b, 0, 1, 2, 3); }
DEVI f32x4 mfma16(u32x4 a, u32x4 b, f32x4 c) { return __builtin_amdgcn_mfma_f32_16x16x32_bf16(asbf(a), asbf(b), c, 0, 0, 0); }
DEVI void mfma16a(u32x4 a, u32x4 b, f32x4& c) { asm volatile("v_mfma_f32_16x16x32_bf16 %0, %1, %2, %0" : "+v"(c) : "v"(a), "v"(b)); }
DEVI void mfma16z(u32x4 a, u32x4 b, f32x4& c) { asm volatile("v_mfma_f32_16x16x32_bf16 %0, %1, %2, 0" : "=&v"(c) : "v"(a), "v"(b)); }
DEVI void mfma32a(u32x4 a, u32x4 b, f32x16& c) { asm volatile("v_mfma_f32_32x32x16_bf16 %0, %1, %2, %0" : "+v"(c) : "v"(a), "v"(b)); }
DEVI void mfma32z(u32x4 a, u32x4 b, f32x16& c) { asm volatile("v_mfma_f32_32x32x16_bf16 %0, %1, %2, 0" : "=&v"(c) : "v"(a), "v"(b)); }
DEVI void mfma8a(u32x2 a, u32x2 b, f32x16& c) { asm volatile("v_mfma_f32_32x32x8_bf16 %0, %1, %2, %0" : "+v"(c) : "v"(a), "v"(b)); }
DEVI int lane_id() { int l; asm volatile("v_mbcnt_lo_u32_b32 %0, -1, 0\n\tv_mbcnt_hi_u32_b32 %0, -1, %0" : "=v"(l)); return l; }
DEVI f32x16 mfma32(u32x4 a, u32x4 b, f32x16 c) { return __builtin_amdgcn_mfma_f32_32x32x16_bf16(asbf(a), asbf(b), c, 0, 0, 0); }
DEVI float blo(unsigned r) { return __uint_as_float(r << 16); }
DEVI float bhi(unsigned r) { return __uint_as_float(r & 0xffff0000u); }
template <int C> DEVI float dppf(float v) { return __uint_as_float(__builtin_amdgcn_update_dpp(0, __float_as_uint(v), C, 0xf, 0xf, false)); }
DEVI float wave_sum(float v) {
  v += dppf<0x128>(v); v += dppf<0x124>(v); v += dppf<0x122>(v); v += dppf<0x121>(v);
  auto a = __builtin_amdgcn_permlane16_swap(__float_as_uint(v), __float_as_uint(v), false, false);
  v = __uint_as_float(a[0]) + __uint_as_float(a[1]);
  auto b = __builtin_amdgcn_permlane32_swap(__float_as_uint(v), __float_as_uint(v), false, false);
  return __uint_as_float(b[0]) + __uint_as_float(b[1]);
}
DEVI float v_fma(float a, float b, float c) { float r; asm volatile("v_fma_f32 %0, %1, %2, %3" : "=v"(r) : "v"(a), "v"(b), "v"(c)); return r; }
DEVI float v_mul(float a, float b) { float r; asm volatile("v_mul_f32 %0, %1, %2" : "=v"(r) : "v"(a), "v"(b)); return r; }
DEVI float v_exp(float a) { float r; asm volatile("v_exp_f32 %0, %1" : "=v"(r) : "v"(a)); return r; }
DEVI float v_rcp(float a) { float r; asm volatile("v_rcp_f32 %0, %1" : "=v"(r) : "v"(a)); return r; }
DEVI unsigned v_cvt(float a, float b) { unsigned r; asm volatile("v_cvt_pk_bf16_f32 %0, %1, %2" : "=v"(r) : "v"(a), "v"(b)); return r; }
DEVI float dot2(unsigned w, unsigned sel, float c) { return __builtin_amdgcn_fdot2_f32_bf16(__builtin_bit_cast(bf16x2, w), __builtin_bit_cast(bf16x2, sel), c, false); }
DEVI unsigned sel(bool c, unsigned a, unsigned b) { return c ? a : b; }
DEVI bool any_lane(bool c) { return __builtin_amdgcn_ballot_w64(c) != 0; }
constexpr int SPIN = 1 << 15;
DEVI void poll_rest(unsigned v, bool need, unsigned tag, unsigned voff, i32x4 srd) {
  if (any_lane(need & (v != tag))) {
    for (int it = 0; it < SPIN; it++) {
      __builtin_amdgcn_s_sleep(1);
      v = ld4<1>(voff, srd, 0);
      asm volatile("s_waitcnt vmcnt(0)" : "+v"(v) :: "memory");
      if (!any_lane(need & (v != tag))) break;
    }
  }
}

#define TS(i)
#define TSDECL
#define TSOUT()
template <int I> struct IC { static constexpr int v = I; };
template <int B, int E, class F> DEVI void sfor(F&& f) { if constexpr (B < E) { f(IC<B>{}); sfor<B + 1, E>(f); } }

struct Params {
  const char *x, *wqkv, *wo, *wg, *wu, *wd;
  char *out, *ws;
};

extern __shared__ __attribute__((aligned(16))) char smem_[];

constexpr unsigned UN = 8192;
constexpr unsigned LW(int s) { return s % 3 == 0 ? 0 : s % 3 == 1 ? 4 * UN : 7 * UN; }
constexpr unsigned L_PK = 3 * UN, L_X = 10 * UN, L_CSR = 16 * UN;
constexpr unsigned LG(int g) { return (g == 0 || g == 8 ? 16 : g == 1 || g == 9 ? 18 : g == 2 ? 4 : g == 3 ? 12 : g == 4 ? 6 : g == 5 ? 8 : g == 6 ? 0 : 2) * UN; }
constexpr int KS(int g) { return g < 2 ? g : g < 4 ? g + 6 : g - 2; }
constexpr unsigned AT_K = 4 * UN, AT_V = 8 * UN, R_A = 12 * UN;
constexpr int cl(int v, int lo, int hi) { return v < lo ? lo : v > hi ? hi : v; }

__global__ __launch_bounds__(512, 2) void k_fused(Params p) {
  TSDECL;
  LAS char* sm = (LAS char*)smem_;
  const unsigned lb = (unsigned)(size_t)sm;
  const int bid = blockIdx.x, xcd = bid & 7, loc = bid >> 3;
  const int wave = __builtin_amdgcn_readfirstlane(threadIdx.x >> 6);
  int lane = threadIdx.x & 63;
  const int rb = (xcd & 1) * 8 + (loc & 7), jg = (xcd >> 1) * 4 + (loc >> 3), pr = jg >> 1, tyB = jg & 1;
  const int mw = wave & 3, cw = wave >> 2;
  const bool spec = jg == 15, likeA = !tyB || spec;
  const i32x4 sx = mk_srd(p.x, X_BYTES), sq = mk_srd(p.wqkv, WQ_BYTES), sg = mk_srd(p.wg, WF_BYTES), su = mk_srd(p.wu, WF_BYTES);
  const int rot = (xcd & 1) * 5;
  auto sidx = [&](int s) { return (unsigned)(s < 5 ? s + rot : s - rot); };
  const unsigned drow = 8 * wave + (lane >> 3), wcol = ((lane & 7) ^ ((lane >> 2) & 4)) * 16u;
  const unsigned vx = (rb * 128 + drow) * 1280u + (((lane & 7) ^ (((wave & 1) << 2) | (lane >> 4))) * 16u);
  const unsigned vq = drow * 3840u + wcol, vg = drow * 3072u + wcol, vu = vg ^ 32u;
  const unsigned v1 = likeA ? vg : vq, v2 = likeA ? vu : vq;
  const i32x4 s1 = sel_srd(likeA, sg, sq), s2 = sel_srd(likeA, su, sq);
  const unsigned gl1 = spec ? 3 * (rb >> 1) + 1 + (rb & 1) : 3 * pr, gl2 = 3 * pr + 1 + tyB;
  const unsigned c0 = (4 * pr + tyB) * 128u, c1 = likeA ? gl1 * 128u : c0 + 128u, c2 = likeA ? c1 : c0 + 256u;
  const unsigned st1 = likeA ? 196608u : 245760u;
  const unsigned ldw = lb + wave * 1024u;
  auto issue = [&](int s, int b) {
    if (b < 2) dma16(ldw + L_X + (s % 3) * 16384u + b * UN, vx, sx, b * 81920u + sidx(s) * 128u);
    else if (b == 2) dma16(ldw + LW(s), vq, sq, sidx(s) * 245760u + c0);
    else if (b == 3) dma16(ldw + LW(s) + UN, v1, s1, sidx(s) * st1 + c1);
    else dma16(ldw + LW(s) + 2 * UN, v2, s2, sidx(s) * st1 + c2);
  };
  auto issue_g = [&](int g, int b) { dma16(ldw + LG(g) + b * UN, b ? vu : vg, b ? su : sg, sidx(KS(g)) * 196608u + gl2 * 128u); };
#pragma unroll
  for (int b = 0; b < 5; b++) issue(0, b);
  const i32x4 sc = mk_srd(p.ws, WS_BYTES);
  const unsigned srow = rb * 128 + jg * 8 + wave;
  unsigned vl = lane * 16u;
  unsigned ep = ld4<1>(0u, sc, 0);
  u32x4 r0 = ld16<0>(vl, sx, srow * 1280u);
  unsigned r1 = ld4<0, 1024>(lane * 4u, sx, srow * 1280u);
  bar();
#pragma unroll
  for (int b = 0; b < 10; b++) issue(1 + b / 5, b % 5);
  TS(1);
  const i32x4 sd = mk_srd(p.wd, WF_BYTES), so = mk_srd(p.wo, WO_BYTES);
  const unsigned kA = bid * 205u >> 10, cA = bid - kA * 5u, kB = (bid + 256u) * 205u >> 10, cB = bid + 256u - kB * 5u;
  const unsigned offB = bid < 84 ? 0u : OOB;
  const unsigned vpk = (4 * wave + (lane >> 4)) * 1280u + (lane & 15) * 16u;
  dma16(ldw + L_PK, vpk, sel_srd(kA < 48, sd, so), (kA < 48 ? kA : kA - 48) * 40960u + cA * 256u);
  LAS char* pkr = sm + (L_PK + (8 * (lane >> 4) + ((lane & 15) >> 2)) * 256u + wave * 32u + (lane & 3) * 8u);
  int hi = lane >> 5;
  LAS char* xa[4];
#pragma unroll
  for (int ks = 0; ks < 4; ks++) { xa[ks] = sm + (L_X + (32 * mw + (lane & 31)) * 128u + (((2 * ks + hi) ^ ((lane >> 1) & 7)) * 16u)); PIN(xa[ks]); }
  LAS char *wa[3], *wr[2];
  {
    const unsigned ch = (lane >> 4) & 1, r = (lane >> 2) & 3, c = lane & 3;
    const unsigned wl = hi * 1024u + r * 128u + ((cw ^ (r >> 1)) * 64u) + (((c & 1) << 1 | c >> 1) * 8u);
    const unsigned q = wl + ch * 32u, g0 = wl + ch * (UN + 32u), g1 = wl + ch * UN + (ch ^ 1) * 32u;
    wa[0] = sm + q; wa[1] = sm + (UN + (likeA ? g0 : q)); wa[2] = sm + (likeA ? UN + g1 : 2 * UN + q);
    wr[0] = sm + (g0 + LG(0)); wr[1] = sm + (g1 + LG(0));
    PIN(wa[0]); PIN(wa[1]); PIN(wa[2]); PIN(wr[0]); PIN(wr[1]);
  }
  const int cst = loc & 7, cskey = cst < 3 ? mw : -1;
  const unsigned csb = C_CS + (jg * 10 + cw) * 512u;
  const u32x4 ones = {0x3f803f80u, 0x3f803f80u, 0x3f803f80u, 0x3f803f80u};
  const unsigned Rq = rb * 4 + mw;
  const int head = xcd * 4 + (loc & 3), ch = loc >> 2, par = head & 1, o0 = 5 * head >> 1;
  const int rt = xcd * 4 + (loc & 3), cg = loc >> 2;
  const unsigned w3b = WS_W3 + (wave * 40 + cg * 5) * 1024u;
  u32x4 RW[6][5], Q1, Q2;
  unsigned tag = 0;
  auto ld_w3 = [&](int buf, int j, int u) { RW[buf][u] = ld16<1>(vl, sc, w3b + j * 327680u + u * 1024u); };
  auto kv_issue = [&]() {
    const int l = lane_id(), oi = ((l >> 2) & 3) ^ (l >> 4), R = ch * 8 - 8 + 2 * wave;
    const unsigned v = sel(oi < (R >= 0 ? 3 : 0), oi * 512u + ((l >> 4) * 4 + (l & 3)) * 16u, OOB);
    const unsigned b = WS_QKV + (unsigned)((R >= 0 ? R : 0) * 240 + 80 + o0) * 512u;
#pragma unroll
    for (int i = 0; i < 8; i++)
      dma16<1>(lb + AT_K + (i >> 2) * 32768u + wave * 4096u + (i & 3) * 1024u, v, sc, b + (i >> 2) * 40960u + ((i >> 1) & 1) * 122880u + (i & 1) * 256u);
    const unsigned qb = WS_QKV + (unsigned)((ch * 8 + wave) * 240 + o0) * 512u;
    Q1 = ld16<1>(l * 16u, sc, qb);
    Q2 = ld16<1, 1024>(sel(l < 32, l * 16u, OOB), sc, qb);
  };

  auto rest = [&]() __attribute__((always_inline)) {
  lane = lane_id(); hi = lane >> 5; vl = lane * 16u;
  const int q32 = lane & 31;
  const unsigned ab = WS_AF + (rt * 4 * 68 + wave) * 1024u;
  const unsigned r7 = wave < 4 ? R_A + 40960u + wave * 5120u : (wave - 4) * 5120u;
  u32x4 A[4][4];
  auto ld_a = [&](int buf, int j, int i, unsigned v) { A[buf][i] = ld16<1>(v, sc, ab + j * 8192u + i * 69632u); };
  auto dma_w3 = [&](unsigned dst, int j, int u) { dma16<1>(lb + dst + u * 1024u, vl, sc, w3b + j * 327680u + u * 1024u); };
  f32x16 o;
  {
    LAS char* cw_ = sm + (AT_K + wave * 4096 + hi * 2048 + (q32 >> 2) * 256 + ((3 ^ ((q32 >> 2) & 3)) * 64) + (q32 & 3) * 16);
    *(LAS u32x4*)cw_ = u32x4{(ch == 0 && wave < 4) ? 0x3f80fe00u : 0x3f800000u, 0u, 0u, 0u};
    *(LAS u32x4*)(cw_ + 32768) = ones;
    {
      const bool z1 = par & !hi;
      Q1[0] = sel(z1, 0u, Q1[0]); Q1[1] = sel(z1, 0u, Q1[1]);
      Q2[2] = sel(par, Q2[2], 0u); Q2[3] = sel(par, Q2[3], 0u);
      Q2[0] = sel(hi, 0x00003f80u, Q2[0]);
    }
    WAIT_LGKM0();
    bar();
    TS(20);
#pragma unroll
    for (int u = 0; u < 5; u++) ld_w3(3, 3, u);
    LAS char* kp = sm + (AT_K + wave * 2048 + (q32 >> 2) * 256 + ((hi ^ ((q32 >> 2) & 3)) * 64) + (q32 & 3) * 16);
    LAS char* kp1 = sm + (((unsigned)(size_t)kp - lb) ^ 128u);
    LAS char* vp[2];
    {
      const int mq = 4 * ((lane >> 4) & 1) + (lane & 3), iq = sel(mq < 4, 2 * par + ((mq & 1) << 1 | mq >> 1), sel(mq == 4, par ? 1 : 4, 6));
#pragma unroll
      for (int h = 0; h < 2; h++) vp[h] = sm + (AT_V + wave * 2048 + hi * 256 + h * 512 + (((iq >> 1) ^ (2 * h + hi)) * 64) + ((lane >> 2) & 3) * 16 + (iq & 1) * 8);
    }
    f32x16 z;
#pragma unroll
    for (int r = 0; r < 16; r++) z[r] = 0.f;
    auto scq = [&](int off, u32x4 q2) {
      const u32x4 a = *(LAS u32x4*)(kp + off), b = *(LAS u32x4*)(kp1 + off);
      return mfma32(b, q2, mfma32(a, Q1, z));
    };
    auto pv = [&](int off, const unsigned* pk_) {
      const u32x4 V1 = cat(trd(vp[0] + off), trd(vp[1] + off)), V2 = cat(trd(vp[0] + off + 1024), trd(vp[1] + off + 1024));
      o = mfma32(V1, u32x4{pk_[0], pk_[1], pk_[2], pk_[3]}, o);
      o = mfma32(V2, u32x4{pk_[4], pk_[5], pk_[6], pk_[7]}, o);
    };
    const int dl = q32 - 4 * hi;
    auto xmax = [&](float m) {
      auto r = __builtin_amdgcn_permlane32_swap(__float_as_uint(m), __float_as_uint(m), false, false);
      return __builtin_fmaxf(__uint_as_float(r[0]), __uint_as_float(r[1]));
    };
    auto edge = [&](const f32x16& ee, float m) {
      unsigned pa[8], pb[8];
#pragma unroll
      for (int r = 0; r < 16; r += 2) {
        const bool b0 = (r & 3) + 8 * (r >> 2) <= dl, b1 = ((r + 1) & 3) + 8 * (r >> 2) <= dl;
        const unsigned x = pk2(__builtin_amdgcn_exp2f(ee[r] - m), __builtin_amdgcn_exp2f(ee[r + 1] - m));
        const unsigned mk = (b0 ? 0xffffu : 0u) | (b1 ? 0xffff0000u : 0u);
        pb[r >> 1] = x & mk; pa[r >> 1] = x & ~mk;
      }
      pv(0, pa); pv(8 * 2048, pb);
    };
    auto merged = [&]() {
      const f32x16 sa = scq(0, Q2), sb = scq(8 * 2048, Q2);
      f32x16 ee;
#pragma unroll
      for (int r = 0; r < 16; r++) ee[r] = ((r & 3) + 8 * (r >> 2) <= dl) ? sb[r] : sa[r];
      return ee;
    };
    auto vmax = [&](const f32x16& ee, float m) {
#pragma unroll
      for (int r = 0; r < 16; r += 2) m = __builtin_fmaxf(__builtin_fmaxf(m, ee[r]), ee[r + 1]);
      return m;
    };
    const unsigned vfh = sel(lane < 12, C_FH + ((rt * 2 + (lane & 1)) * 64u + wave + 8u * (lane >> 1)) * 4u, OOB);
    unsigned hf;
    o = z;
    {
      const f32x16 ee = merged();
      const unsigned mb = pk2(xmax(vmax(ee, -3.0e38f)), 0.f) << 16;
      const float m = __uint_as_float(mb);
      const u32x4 Q3 = {sel(hi, (mb ^ 0x80000000u) | 0x3f80u, Q2[0]), Q2[1], Q2[2], Q2[3]};
      f32x16 sn = scq(2048, Q3);
      edge(ee, m);
      sfor<1, 8>([&](auto J) {
        constexpr int j = J.v;
        const f32x16 s = sn;
        if constexpr (j < 7) sn = scq((j + 1) * 2048, Q3);
        if constexpr (j == 1) { ld_w3(4, 4, 0); ld_w3(4, 4, 1); ld_w3(4, 4, 2); ld_w3(4, 4, 3); }
        if constexpr (j == 2) {
          wait_vm<9>();
          st4s(tag, sel(lane < (tyB ? 1 : 2), C_FH + (Rq * 64u + 2 * (gl2 - lane) + cw) * 4u, OOB), sc);
          TS(21);
          ld_w3(4, 4, 4); dma_w3(r7, 7, 0); dma_w3(r7, 7, 1); dma_w3(r7, 7, 2);
        }
        if constexpr (j == 3) { dma_w3(r7, 7, 3); dma_w3(r7, 7, 4); hf = ld4<1>(vfh, sc, 0); }
        if constexpr (j == 4) { dma_w3(R_A + wave * 5120u, 6, 0); dma_w3(R_A + wave * 5120u, 6, 1); dma_w3(R_A + wave * 5120u, 6, 2); }
        if constexpr (j == 5) {
          dma_w3(R_A + wave * 5120u, 6, 3); dma_w3(R_A + wave * 5120u, 6, 4);
          asm volatile("s_waitcnt vmcnt(5)" : "+v"(hf) :: "memory");
          TS(22);
          poll_rest(hf, lane < 12, tag, vfh, sc);
        }
        if constexpr (j >= 5) { ld_a(j - 5, j - 5, 0, vl); ld_a(j - 5, j - 5, 1, vl); ld_a(j - 5, j - 5, 2, vl); ld_a(j - 5, j - 5, 3, vl); }
        unsigned pk_[8];
#pragma unroll
        for (int r = 0; r < 16; r += 2) pk_[r >> 1] = pk2(__builtin_amdgcn_exp2f(s[r]), __builtin_amdgcn_exp2f(s[r + 1]));
        pv(j * 2048, pk_);
      });
    }
    {
      float mx = 0.f;
#pragma unroll
      for (int r = 0; r < 12; r += 2) mx = __builtin_fmaxf(__builtin_fmaxf(mx, __builtin_fabsf(o[r])), __builtin_fabsf(o[r + 1]));
      if (any_lane(!((o[12] >= 5.9604645e-8f) & (o[12] <= 16777216.f) & (mx < 3.0e38f)))) {
        float m = vmax(merged(), -3.0e38f);
#pragma nounroll
        for (int j = 1; j < 8; j++) m = vmax(scq(j * 2048, Q2), m);
        m = xmax(m);
        o = z;
        edge(merged(), m);
#pragma nounroll
        for (int j = 1; j < 8; j++) {
          const f32x16 s = scq(j * 2048, Q2);
          unsigned pk_[8];
#pragma unroll
          for (int r = 0; r < 16; r += 2) pk_[r >> 1] = pk2(__builtin_amdgcn_exp2f(s[r] - m), __builtin_amdgcn_exp2f(s[r + 1] - m));
          pv(j * 2048, pk_);
        }
      }
    }
    TS(23);
    const float inv = __builtin_amdgcn_rcpf(o[12]);
    const unsigned tb_ = (unsigned)((ch * 8 + wave) * 2 + (q32 >> 4)) * 68u + 48u;
    const unsigned oa = o0 + par + hi, ob = o0 + (par ? 0 : 2);
    st16<1>(u32x4{pk2(o[0] * inv, o[1] * inv), pk2(o[2] * inv, o[3] * inv), pk2(o[4] * inv, o[5] * inv), pk2(o[6] * inv, o[7] * inv)},
            (tb_ + (oa >> 2)) * 1024u + (oa & 3) * 256u + (q32 & 15) * 16u, sc, WS_AF);
    st8<1>(u32x2{pk2(o[8] * inv, o[9] * inv), pk2(o[10] * inv, o[11] * inv)},
           sel(hi, OOB, (tb_ + (ob >> 2)) * 1024u + (ob & 3) * 256u + (q32 & 15) * 16u + par * 8u), sc, WS_AF);
  }
  const unsigned vl8 = sel(wave < 4, vl, OOB);
  unsigned vfo, vo[3], lo_[3], lo2[3];
  f32x4 fa[4][5];
  unsigned of = 0;
  u32x2 xr[3];
#define PINA(n) "+v"(A[n][0]), "+v"(A[n][1]), "+v"(A[n][2]), "+v"(A[n][3])
#define PINW(n) "+v"(RW[n][0]), "+v"(RW[n][1]), "+v"(RW[n][2]), "+v"(RW[n][3]), "+v"(RW[n][4])
  asm volatile("s_waitcnt vmcnt(10)" : PINA(0), PINW(2), PINW(3), PINW(4) :: "memory");
  TS(24);
  auto step = [&](auto J_) {
    constexpr int j = J_.v, wb = j < 6 ? j : j == 6 ? 1 : j == 7 ? 2 : 0, abuf = j & 3;
    if constexpr (j == 1) asm volatile("s_waitcnt vmcnt(6)" : PINA(1) :: "memory");
    if constexpr (j == 2) {
      asm volatile("s_waitcnt vmcnt(9)" : PINA(2) :: "memory");
      st4s(tag, sel(vl == 0, C_FO + ((ch * 8 + wave) * 32u + head) * 4u, OOB), sc);
      TS(25);
    }
    if constexpr (j == 3) asm volatile("s_waitcnt vmcnt(15)" : PINA(3) :: "memory");
    if constexpr (j == 4) {
      asm volatile("s_waitcnt vmcnt(0)" : PINA(0), PINA(1), "+v"(of), PINW(0), PINW(5) :: "memory");
      TS(26);
      poll_rest(of, vfo != OOB, tag, vfo, sc);
      TS(27);
    }
    if constexpr (j == 6) asm volatile("s_waitcnt vmcnt(8)" : PINA(2) :: "memory");
    if constexpr (j == 7) asm volatile("s_waitcnt vmcnt(7)" : PINA(3) :: "memory");
    SB();
    sfor<0, 20>([&](auto K_) {
      constexpr int k = K_.v, u = k >> 2, i = k & 3;
      if constexpr (j == 0) mfma16z(RW[wb][u], A[abuf][i], fa[i][u]);
      else mfma16a(RW[wb][u], A[abuf][i], fa[i][u]);
      if constexpr (j >= 1 && j <= 3 && k < 4) ld_a((j + 2) & 3, j + 2, k, vl);
      if constexpr (j == 1 && k >= 4 && k < 9) ld_w3(5, 5, k - 4);
      if constexpr (j == 2 && k >= 4 && k < 9) RW[0][k - 4] = ld16<1>(vl8, sc, w3b + 8 * 327680u + (k - 4) * 1024u);
      if constexpr (j == 3 && k == 5) {
        const int l = lane_id(), h5 = 5 * (l & 31);
        auto ov = [&](int q) { return (h5 + 5 > 8 * q) & (h5 < 8 * q + 8); };
        vfo = sel(ov(wave) | ov(wave + 8) | ov(wave + 16), C_FO + (rt * 64u + l) * 4u, OOB);
        PIN(vfo);
      }
      if constexpr (j == 3 && k == 6) of = ld4<1>(vfo, sc, 0);
      if constexpr (j == 4 && k < 8) ld_a(2 + (k >> 2), 6 + (k >> 2), k & 3, vl);
      if constexpr (j == 5 && k < 4) ld_a(0, 8, k, vl8);
      if constexpr (j == 5 && k >= 8 && k < 13) RW[1][k - 8] = *(LAS u32x4*)(sm + (R_A + wave * 5120 + vl) + (k - 8) * 1024);
      if constexpr (j == 5 && k >= 13 && k < 18) RW[2][k - 13] = *(LAS u32x4*)(sm + (r7 + vl) + (k - 13) * 1024);
      if constexpr (j == 6 && k >= 2 && k < 14 && (k & 3) == 2) {
        constexpr int q = k >> 2;
        const unsigned c = ((wave < 4 ? 3 * wave : 4 + 2 * wave) + q) * 64u + lane_id(), row = c * 3277u >> 16, pos = c - row * 20u;
        vo[q] = sel(q == 2 && wave >= 4, OOB, (rt * 64u + row) * 1280u + cg * 160u + pos * 8u);
        const unsigned in = (row & 31) * 160u + ((pos ^ ((row >> 2) & 3)) * 8u);
        lo_[q] = R_A + (row >> 5) * 40960u + in;
        lo2[q] = sel(row >> 5, 0u, R_A + 20480u) + in;
        PIN(lo_[q]); PIN(lo2[q]);
        xr[q] = ld8<0>(vo[q], sx, 0);
      }
      SB();
    });
  };
  sfor<0, 8>(step);
  asm volatile("s_waitcnt vmcnt(0)" : PINA(0), "+v"(xr[0]), "+v"(xr[1]), "+v"(xr[2]) :: "memory");
  if (wave < 4) step(IC<8>{});
  asm volatile("s_nop 7" ::: "memory");
  SB();
  TS(28);
  lane = lane_id();
  const int t16 = lane & 15, g = lane >> 4;
  {
    const unsigned pin = t16 * 160 + ((g ^ (t16 >> 2)) * 8);
    LAS char* pw0 = sm + (R_A + wave * 5120 + pin);
    LAS char* pw1 = sm + (r7 + pin);
#pragma unroll
    for (int i = 0; i < 4; i++)
#pragma unroll
      for (int u = 0; u < 5; u++)
        *(LAS u32x2*)((i >> 1 ? pw1 : pw0) + ((i & 1) * 2560 + u * 32)) = u32x2{pk2(fa[i][u][0], fa[i][u][1]), pk2(fa[i][u][2], fa[i][u][3])};
    WAIT_LGKM0();
    bar();
    const i32x4 sout = mk_srd(p.out, X_BYTES);
    unsigned slo = 0x00003f80u, shi = 0x3f800000u;
    asm volatile("" : "+s"(slo), "+s"(shi));
    auto fin = [&](int q) {
      float a0 = blo(xr[q][0]), a1 = bhi(xr[q][0]), a2 = blo(xr[q][1]), a3 = bhi(xr[q][1]);
#pragma unroll
      for (int w = 0; w < 8; w++) {
        const u32x2 v = *(LAS u32x2*)(sm + (w < 4 ? lo_[q] : lo2[q]) + (w & 3) * 5120);
        a0 = dot2(v[0], slo, a0); a1 = dot2(v[0], shi, a1); a2 = dot2(v[1], slo, a2); a3 = dot2(v[1], shi, a3);
      }
      st8<0>(u32x2{pk2(a0, a1), pk2(a2, a3)}, vo[q], sout, 0);
    };
    fin(0); fin(1);
    if (wave < 4) fin(2);
  }
  TSOUT();
  __builtin_amdgcn_endpgm();
  };

  auto phase1 = [&](auto SP_) {
    constexpr int SP = SP_.v;
    u32x4 X[32], xt[2];
    f32x16 acc[3], cs;
    u32x2 wf[2][3][2], w2[4][2], pk[2], gs, gc[3];
    u32x2 As[3], Bc;
    float sd, rs, rg;
    unsigned pf;
    float e[10], sacc = 0.f, mean = 0.f, cp[4];
    auto stat = [&](int k) {
      if (k < 5) { const unsigned w = k < 4 ? r0[k & 3] : r1; e[2 * k] = blo(w); e[2 * k + 1] = bhi(w); sacc += e[2 * k] + e[2 * k + 1]; }
      else if (k >= 11 && k < 16) {
        const float d0 = e[2 * k - 22] - mean, d1 = e[2 * k - 21] - mean;
        sacc = __builtin_fmaf(d0, d0, sacc); PIN(sacc); sacc = __builtin_fmaf(d1, d1, sacc);
      } else if (k < 22) {
        const int q = k < 11 ? k - 5 : k - 16;
        if (q == 0) sacc += dppf<0x128>(sacc);
        else if (q == 1) sacc += dppf<0x124>(sacc);
        else if (q == 2) sacc += dppf<0x122>(sacc);
        else if (q == 3) sacc += dppf<0x121>(sacc);
        else if (q == 4) { auto a = __builtin_amdgcn_permlane16_swap(__float_as_uint(sacc), __float_as_uint(sacc), false, false); sacc = __uint_as_float(a[0]) + __uint_as_float(a[1]); }
        else {
          auto a = __builtin_amdgcn_permlane32_swap(__float_as_uint(sacc), __float_as_uint(sacc), false, false);
          sacc = (__uint_as_float(a[0]) + __uint_as_float(a[1])) * (1.0f / 640.0f);
          if (k == 10) { mean = sacc; sacc = 0.f; PIN(mean); }
        }
      } else if (k == 22) sacc = __builtin_amdgcn_sqrtf(rbf(rbf(sacc) + 1e-5f));
      PIN(sacc);
    };
    auto rdw = [&](int n, int r) {
      wf[n & 1][r >> 1][r & 1] = trd(wa[r >> 1] + (LW(n >> 2) + (n & 3) * 2048 + (r & 1) * 512));
    };
    auto rdx = [&](int n) {
      const u32x4 v = *(LAS u32x4*)(xa[n & 3] + ((n >> 2) % 3) * 16384);
      if (n < 32) X[n] = v; else xt[n & 1] = v;
    };
    auto wfr = [&](int n, int t) { return cat(wf[n & 1][t][0], wf[n & 1][t][1]); };
    const unsigned vl8 = lane * 8u;
    auto ld_gran = [&](int k) {
      if (k == 0) gs = ld8<1>((lane & 31) * 8u, sc, C_STAT + Rq * 256u);
      else if (k < 4) gc[k - 1] = ld8<1>(vl8, sc, csb + (k - 1) * 1024u);
      else pf = ld4<1>((lane & 31) * 4u, sc, C_PF + wave * 128u);
    };
    auto split3 = [&](unsigned g) {
      const float c = __uint_as_float(g), r1_ = c - rbf(c);
      const unsigned a0 = pk2(c, r1_);
      u32x2 a = {a0, pk2(r1_ - bhi(a0), 0.f)};
      PIN(a);
      return a;
    };
    auto prep = [&](int k) {
      if (k == 0) {
        if (any_lane((gs[1] != tag) | (gc[0][1] != tag) | (gc[1][1] != tag) | (gc[2][1] != tag))) {
          for (int it = 0; it < SPIN; it++) {
            __builtin_amdgcn_s_sleep(1);
#pragma unroll
            for (int i = 0; i < 4; i++) ld_gran(i);
            asm volatile("s_waitcnt vmcnt(0)" : "+v"(gs), "+v"(gc[0]), "+v"(gc[1]), "+v"(gc[2]) :: "memory");
            if (!any_lane((gs[1] != tag) | (gc[0][1] != tag) | (gc[1][1] != tag) | (gc[2][1] != tag))) break;
          }
        }
      } else if (k == 1) {
        const unsigned nm = (gs[0] & 0xffffu) ^ 0x8000u;
        Bc = u32x2{nm | nm << 16, nm}; PIN(Bc);
        sd = bhi(gs[0]); PIN(sd); rs = __builtin_amdgcn_rcpf(sd); PIN(rs);
      } else if (k == 2) { As[0] = split3(gc[0][0]); rg = -1.44269504089f * rs; PIN(rg); }
      else if (k < 5) { if (!SP) As[k - 2] = split3(gc[k - 2][0]); }
    };
#pragma unroll
    for (int r = 0; r < 16; r++) cs[r] = 0.f;
    PIN(cs);
    u32x4 on1 = ones;
    PIN(on1);
    wait_vm<14>();
    bar();
    TS(2);
    rdx(0);
#pragma unroll
    for (int r = 0; r < 6; r++) rdw(0, r);
    SB();
    sfor<0, 40>([&](auto N_) {
      constexpr int n = N_.v, s = n >> 2, ks = n & 3;
      if constexpr (ks == 3) {
        WAIT_LGKM0();
        if constexpr (s == 0) { asm volatile("s_waitcnt vmcnt(6)" : "+v"(ep), "+v"(r0), "+v"(r1) :: "memory"); tag = __builtin_amdgcn_readfirstlane(ep) + 1u; }
        else if constexpr (s == 1) wait_vm<5>();
        else if constexpr (s == 2) wait_vm<7>();
        else if constexpr (s == 3) wait_vm<7>();
        else if constexpr (s == 4) wait_vm<5>();
        else if constexpr (s == 5) wait_vm<6>();
        else if constexpr (s == 6) wait_vm<6>();
        else if constexpr (s == 7) wait_vm<8>();
        else if constexpr (s == 8) wait_vm<8>();
        else asm volatile("s_waitcnt vmcnt(4)" : "+v"(gs), "+v"(gc[0]), "+v"(gc[1]), "+v"(gc[2]) :: "memory");
        bar();
        TS(3 + s);
        SB();
      }
      sfor<0, 3>([&](auto T_) {
        constexpr int t = T_.v, m = ((n + 1) & 3) * 3 + t, sb = ((n + 1) >> 2) - 1;
        const u32x4 xf = n < 32 ? X[n < 32 ? n : 0] : xt[n & 1];
        if constexpr (SP && t > 0) { if constexpr (n == 0) mfma32z(on1, wfr(n, t), acc[t]); else mfma32a(on1, wfr(n, t), acc[t]); }
        else { if constexpr (n == 0) mfma32z(wfr(n, t), xf, acc[t]); else mfma32a(wfr(n, t), xf, acc[t]); }
        if constexpr (n < 39) {
          if constexpr (t < 2) { rdw(n + 1, 3 * t); rdw(n + 1, 3 * t + 1); rdw(n + 1, 3 * t + 2); }
          if constexpr (t == 0) rdx(n + 1);
        } else {
          if constexpr (t < 2) { w2[t][0] = trd(wr[t]); w2[t][1] = trd(wr[t] + 512); }
        }
        if constexpr (sb == 1 && m == 0) st8<1>(u32x2{pk2(mean, sacc), tag}, sel(lane == 0, C_STAT + srow * 8u, OOB), sc, 0);
        if constexpr (sb == 6 && m == 0) st4s(tag, sel(vl8 == 0, wave == 0 ? C_PF + bid * 4u : OOB, OOB), sc);
        if constexpr (sb >= 0 && sb < 7 && (m & 1) && m < 10) issue(sb + 3, m >> 1);
        if constexpr ((sb == 1 || sb == 4) && (m == 2 || m == 4)) pk[m / 2 - 1] = trd(pkr + (m / 2 - 1) * 1024);
        if constexpr (sb == 1 && m == 10) st16<1>(cat(pk[0], pk[1]), vl, sc, WS_W3 + (kA * 40u + cA * 8u + wave) * 1024u);
        if constexpr (sb == 4 && m == 10) st16<1>(cat(pk[0], pk[1]), vl | offB, sc, WS_W3 + (kB * 40u + cB * 8u + wave) * 1024u);
        if constexpr (sb == 2 && m == 10) dma16(ldw + L_PK, vpk | offB, mk_srd(p.wo, WO_BYTES), (kB - 48) * 40960u + cB * 256u);
        if constexpr (sb == 0) { stat(2 * m); stat(2 * m + 1); }
        if constexpr (sb == 4 && m == 6) { PIN(cs); *(LAS float*)(sm + L_CSR + wave * 128 + (lane & 31) * 4) = cs[0]; }
        if constexpr (sb == 5 && m < 4) cp[m] = *(LAS float*)(sm + L_CSR + cw * 512 + m * 128 + (lane & 31) * 4);
        if constexpr (sb == 5 && m == 6)
          st8<1>(u32x2{__float_as_uint((cp[0] + cp[1]) + (cp[2] + cp[3])), tag}, sel(vl8 < (cskey == 0 ? 256u : 0u), vl8, OOB), sc, csb + cst * 1024u + (xcd & 1) * 256u);
        if constexpr (sb == 6 && m == 11) { issue_g(0, 0); issue_g(0, 1); }
        if constexpr (sb == 7 && (m & 1)) issue_g(1 + m / 4, (m >> 1) & 1);
        if constexpr (sb == 8 && m < 4) ld_gran(m);
        if constexpr (sb == 8 && (m & 1) && m >= 5) issue_g(4 + (m - 5) / 4, ((m - 5) >> 1) & 1);
        if constexpr (sb == 9 && m == 0) ld_gran(4);
        if constexpr (sb == 9 && m > 0) { issue_g(5 + m, 0); issue_g(5 + m, 1); }
        if constexpr (sb == 9) prep(m);
        SB();
      });
      if constexpr (s < 5) {
        asm volatile("s_cmp_lg_u32 %5, %6\n\ts_cbranch_scc1 .Lce%=\n\t"
                     "s_cmp_lg_u32 %7, 0\n\ts_cbranch_scc1 .Lc1%=\n\t"
                     "v_mfma_f32_32x32x16_bf16 %0, %1, %2, %0\n\ts_branch .Lce%=\n"
                     ".Lc1%=:\n\ts_cmp_lg_u32 %7, 1\n\ts_cbranch_scc1 .Lc2%=\n\t"
                     "v_mfma_f32_32x32x16_bf16 %0, %1, %3, %0\n\ts_branch .Lce%=\n"
                     ".Lc2%=:\n\tv_mfma_f32_32x32x16_bf16 %0, %1, %4, %0\n"
                     ".Lce%=:"
                     : "+v"(cs) : "v"(on1), "v"(wfr(n, 0)), "v"(wfr(n, 1)), "v"(wfr(n, 2)), "s"(cskey), "i"(ks), "s"(cst) : "scc");
        SB();
      }
    });
    TS(13);
    auto round2 = [&](auto TY_) {
      constexpr int TY = TY_.v;
      f32x16 ac2[2];
      u32x4 qv, hv[4], A4[3];
      u32x2 gc2[2];
      float t[8], rq;
      unsigned fq;
      const unsigned vh = (lane & 15) * 16u + ((lane >> 4) & 1) * 69632u + hi * 256u;
      auto qop = [&](auto U_, auto K_) {
        constexpr int u = U_.v, k = K_.v;
        const unsigned ln = 4 * pr + tyB + u;
        if constexpr (k == 0) {
          const float qs = ln < 10u ? 0.22360679775f * 1.44269504089f : 1.0f;
          asm volatile("v_mul_f32 %0, %1, %2" : "=v"(rq) : "s"(qs), "v"(rs));
        } else {
          constexpr int h = (k - 1) / 13, j = (k - 1) % 13;
          if constexpr (j < 8) t[j] = v_mul(acc[u][8 * h + j], rq);
          else if constexpr (j < 12) qv[j - 8] = v_cvt(t[2 * j - 16], t[2 * j - 15]);
          else st16<1>(qv, vl, sc, WS_QKV + (Rq * 240u + ln * 8u + cw * 4u + h * 2u) * 512u);
        }
      };
      auto gop = [&](f32x16& a, u32x4& o, auto K_) {
        constexpr int k = K_.v, ph = k >> 3, j = k & 7;
        if constexpr (ph == 0) t[j] = v_mul(a[j], rg);
        else if constexpr (ph == 1) t[j] = v_exp(t[j]);
        else if constexpr (ph == 2) t[j] = v_fma(t[j], sd, sd);
        else if constexpr (ph == 3) t[j] = v_rcp(t[j]);
        else if constexpr (ph == 4) t[j] = v_mul(t[j], a[8 + j]);
        else o[j] = v_cvt(t[2 * j], t[2 * j + 1]);
      };
      auto st_h = [&](u32x4 v, unsigned gl, int a) { st16<1>(v, vh, sc, WS_AF + ((rb * 8 + 2 * mw) * 68u + 2 * gl + cw) * 1024u + a * 512u); };
      auto epi = [&](auto K_) {
        constexpr int k = K_.v;
        if constexpr (k < 27) qop(IC<0>{}, K_);
        else if constexpr (TY == 1) { if constexpr (k < 81) qop(IC<k / 27>{}, IC<k % 27>{}); }
        else if constexpr (TY == 0) { if constexpr (k < 115) gop(acc[1 + (k - 27) / 44], hv[(k - 27) / 44], IC<(k - 27) % 44>{}); }
        else if constexpr (k < 29) {
          PIN(acc[k - 26]);
          st8<1>(u32x2{__float_as_uint(acc[k - 26][0] * 0.5f), tag}, sel(mw == 0, vl8, OOB), sc, C_CS + (rb * 10 + cw) * 512u + (k - 24) * 1024u);
        }
      };
      unsigned vf;
      sfor<0, 80>([&](auto M_) {
        constexpr int m = M_.v;
        constexpr auto KK = [](int q) { return q < 64 ? q >> 1 : 32 + (q & 7); };
        constexpr auto AA = [](int q) { return q < 64 ? q & 1 : (q >> 3) & 1; };
        constexpr int kk = KK(m), a = AA(m), g = kk >> 2;
        if constexpr (m == 14) {
          WAIT_LGKM0();
          asm volatile("s_waitcnt vmcnt(%1)" : "+v"(pf) : "i"(TY == 1 ? 7 : TY == 0 ? 6 : 8) : "memory");
          poll_rest(pf, true, tag, C_PF + wave * 128u + (lane & 31) * 4u, sc);
          bar();
          TS(14);
          st4s(tag, sel(vl8 == 0, (bid | wave) == 0 ? 0u : OOB, OOB), sc);
          SB();
        }
        if constexpr (m == 46) {
          WAIT_LGKM0();
          wait_vm<10>();
          st4s(tag, sel(vl8 < (TY == 1 ? 24u : 8u), C_FQ + (Rq * 64u + (4 * pr + tyB) * 2u + cw) * 4u + vl8, OOB), sc);
          bar();
          TS(15);
          {
            const int l = lane_id(), sec = (l + 2) >> 2, fr = sel(l < 2, ch * 8 + wave, ch * 8 - 8 + 2 * wave + ((l >> 1) & 1));
            vf = sel((l < 10) & (fr >= 0), C_FQ + (fr * 64u + ((sec * 80u + o0 + (l & 1) * 2u) >> 2)) * 4u, OOB);
          }
          fq = ld4<1>(vf, sc, 0);
          gc2[0] = ld8<1>(vl8, sc, csb + 3072u); gc2[1] = ld8<1>(vl8, sc, csb + 4096u);
          SB();
        }
        if constexpr (m == 60) {
          asm volatile("s_waitcnt vmcnt(0)" : "+v"(fq), "+v"(gc2[0]), "+v"(gc2[1]) :: "memory");
          TS(16);
          poll_rest(fq, vf != OOB, tag, vf, sc);
          kv_issue();
          if (any_lane((gc2[0][1] != tag) | (gc2[1][1] != tag))) {
            for (int it = 0; it < SPIN; it++) {
              __builtin_amdgcn_s_sleep(1);
              gc2[0] = ld8<1>(vl8, sc, csb + 3072u); gc2[1] = ld8<1>(vl8, sc, csb + 4096u);
              asm volatile("s_waitcnt vmcnt(0)" : "+v"(gc2[0]), "+v"(gc2[1]), "+v"(Q1), "+v"(Q2) :: "memory");
              if (!any_lane((gc2[0][1] != tag) | (gc2[1][1] != tag))) break;
            }
          }
          TS(17);
          SB();
        }
        const u32x4 xf = (g == 2 || g == 3) ? xt[kk & 1] : X[(g == 2 || g == 3) ? 0 : KS(g) * 4 + (kk & 3)];
        if constexpr (m < 2) mfma32z(cat(w2[m & 3][0], w2[m & 3][1]), xf, ac2[a]);
        else mfma32a(cat(w2[m & 3][0], w2[m & 3][1]), xf, ac2[a]);
        if constexpr (m + 2 < 80) {
          constexpr int k2 = KK(m + 2), a2 = AA(m + 2), g2 = k2 >> 2;
          constexpr int gp = m < 62 ? (k2 - 1) >> 2 : m == 70 ? 7 : (k2 - 1) >> 2;
          if constexpr ((LG(g2) >> 16) != (LG(gp) >> 16)) { wr[a2] += (int)(LG(g2) & ~65535u) - (int)(LG(gp) & ~65535u); PIN(wr[a2]); }
          w2[(m + 2) & 3][0] = trd(wr[a2] + ((LG(g2) & 65535u) + (k2 & 3) * 2048));
          w2[(m + 2) & 3][1] = trd(wr[a2] + ((LG(g2) & 65535u) + (k2 & 3) * 2048 + 512));
          if constexpr (a2 == 0 && (g2 == 2 || g2 == 3)) xt[k2 & 1] = *(LAS u32x4*)(xa[k2 & 3] + (KS(g2) % 3) * 16384);
        }
        if constexpr (m < 2) prep(3 + m);
        if constexpr (m >= 2 && m < (SP ? 3 : 5)) mfma8a(As[m - 2], Bc, acc[m - 2]);
        if constexpr (m >= 5) sfor<(m - 5) * 5, cl((m - 4) * 5, 0, 115)>(epi);
        if constexpr (m == 14 || m == 15) { issue_g(m - 6, 0); issue_g(m - 6, 1); }
        if constexpr (m >= 24 && m < 44 && !(m & 1)) ld_w3((m - 24) / 10, (m - 24) / 10, ((m - 24) >> 1) % 5);
        if constexpr (m == 61 || m == 62) { const u32x2 a_ = split3(gc2[m - 61][0]); A4[m - 61] = u32x4{a_[0], a_[1], 0u, 0u}; PIN(A4[m - 61]); }
        if constexpr (m == 63) { A4[2] = u32x4{Bc[0], Bc[1], 0u, 0u}; PIN(A4[2]); }
        if constexpr (m == 64 || m == 65) mfma32a(A4[m - 64], A4[2], ac2[m - 64]);
        if constexpr (TY == 0 && (m == 63 || m == 64)) st_h(hv[m - 63], gl1, m - 63);
        if constexpr (m >= 66 && m < 76 && !(m & 1)) ld_w3(2, 2, (m - 66) >> 1);
        if constexpr (m >= 73) sfor<(m - 73) * 7, cl((m - 72) * 7, 0, 44)>([&](auto K_) { gop(ac2[0], hv[2], K_); });
        SB();
      });
      TS(18);
      st_h(hv[2], gl2, 0);
      asm volatile("s_nop 7" ::: "memory");
      sfor<0, 44>([&](auto K_) { gop(ac2[1], hv[3], K_); });
      st_h(hv[3], gl2, 1);
      u32x4 (&rw)[6][5] = RW; u32x4 &q1 = Q1, &q2 = Q2;
      asm volatile("s_waitcnt vmcnt(%12)" : "+v"(q1), "+v"(q2), "+v"(rw[0][0]), "+v"(rw[0][1]), "+v"(rw[0][2]), "+v"(rw[0][3]), "+v"(rw[0][4]),
                   "+v"(rw[1][0]), "+v"(rw[1][1]), "+v"(rw[1][2]), "+v"(rw[1][3]), "+v"(rw[1][4]) : "i"(TY == 0 ? 9 : 7) : "memory");
      TS(19);
      rest();
    };
    if constexpr (SP) round2(IC<2>{});
    else { if (tyB) round2(IC<1>{}); else round2(IC<0>{}); }
  };
  if (spec) phase1(IC<1>{}); else phase1(IC<0>{});
}

extern "C" void kernel_launch(void* const* d_in, const int* in_sizes, int n_in, void* d_out, int out_size, void* d_ws,
                              size_t ws_size, hipStream_t stream) {
  Params p;
  p.x = (const char*)d_in[0]; p.wqkv = (const char*)d_in[1]; p.wo = (const char*)d_in[2];
  p.wg = (const char*)d_in[4]; p.wu = (const char*)d_in[5]; p.wd = (const char*)d_in[6];
  p.out = (char*)d_out; p.ws = (char*)d_ws;
  (void)hipFuncSetAttribute((const void*)k_fused, hipFuncAttributeMaxDynamicSharedMemorySize, LDS_BYTES);
  k_fused<<<256, 512, LDS_BYTES, stream>>>(p);
}
```

```cpp
#include <hip/hip_runtime.h>

typedef int i32x4 __attribute__((ext_vector_type(4)));
typedef unsigned u32x4 __attribute__((ext_vector_type(4)));
typedef unsigned u32x2 __attribute__((ext_vector_type(2)));
typedef float f32x4 __attribute__((ext_vector_type(4)));
typedef float f32x2 __attribute__((ext_vector_type(2)));
typedef float f32x16 __attribute__((ext_vector_type(16)));
typedef __bf16 bf16x8 __attribute__((ext_vector_type(8)));
typedef __bf16 bf16x2 __attribute__((ext_vector_type(2)));
typedef short s16x4 __attribute__((ext_vector_type(4)));
#define LAS __attribute__((address_space(3)))
#define DEVI __device__ __forceinline__

constexpr int S_ = 2048, D_ = 640, F_ = 1536;
constexpr unsigned X_BYTES = S_ * D_ * 2, WQ_BYTES = D_ * 3 * D_ * 2, WF_BYTES = D_ * F_ * 2, WO_BYTES = D_ * D_ * 2;
constexpr unsigned QKV_BYTES = S_ * 3 * D_ * 2;
constexpr unsigned AF_BYTES = 128u * 68u * 1024u;
constexpr unsigned W3_BYTES = 68u * 40u * 1024u;
constexpr unsigned WS_QKV = 1u << 20, WS_AF = 16u << 20, WS_W3 = 32u << 20, WS_BYTES = 40u << 20;
constexpr unsigned C_PF = 0x8000, C_STAT = 0x10000, C_CS = 0x20000, C_FQ = 0x40000, C_FH = 0x80000, C_FO = 0xC0000;
constexpr unsigned LDS_BYTES = 163840;
constexpr unsigned OOB = 0x80000000u;

DEVI i32x4 mk_srd(const void* p, unsigned n) {
  unsigned long long a = (unsigned long long)p;
  i32x4 r;
  r.x = __builtin_amdgcn_readfirstlane((int)(unsigned)a);
  r.y = __builtin_amdgcn_readfirstlane((int)(unsigned)(a >> 32));
  r.z = (int)n; r.w = 0x00020000;
  return r;
}
DEVI i32x4 sel_srd(bool c, i32x4 a, i32x4 b) { i32x4 r; r.x = c ? a.x : b.x; r.y = c ? a.y : b.y; r.z = c ? a.z : b.z; r.w = a.w; return r; }
template <int SC = 0> DEVI void dma16(unsigned ldsaddr, unsigned voff, i32x4 srd, unsigned soff) {
  if (SC) asm volatile("s_mov_b32 m0, %0\n\ts_nop 0\n\tbuffer_load_dwordx4 %1, %2, %3 offen sc1 lds"
                       :: "s"(ldsaddr), "v"(voff), "s"(srd), "s"(soff) : "memory");
  else    asm volatile("s_mov_b32 m0, %0\n\ts_nop 0\n\tbuffer_load_dwordx4 %1, %2, %3 offen lds"
                       :: "s"(ldsaddr), "v"(voff), "s"(srd), "s"(soff) : "memory");
}
template <int SC, int OFF = 0> DEVI u32x4 ld16(unsigned voff, i32x4 srd, unsigned soff) {
  u32x4 r;
  if (SC) asm volatile("buffer_load_dwordx4 %0, %1, %2, %3 offen offset:%4 sc1" : "=v"(r) : "v"(voff), "s"(srd), "s"(soff), "i"(OFF) : "memory");
  else    asm volatile("buffer_load_dwordx4 %0, %1, %2, %3 offen offset:%4" : "=v"(r) : "v"(voff), "s"(srd), "s"(soff), "i"(OFF) : "memory");
  return r;
}
template <int SC, int OFF = 0> DEVI u32x2 ld8(unsigned voff, i32x4 srd, unsigned soff) {
  u32x2 r;
  if (SC) asm volatile("buffer_load_dwordx2 %0, %1, %2, %3 offen offset:%4 sc1" : "=v"(r) : "v"(voff), "s"(srd), "s"(soff), "i"(OFF) : "memory");
  else    asm volatile("buffer_load_dwordx2 %0, %1, %2, %3 offen offset:%4" : "=v"(r) : "v"(voff), "s"(srd), "s"(soff), "i"(OFF) : "memory");
  return r;
}
template <int SC, int OFF = 0> DEVI unsigned ld4(unsigned voff, i32x4 srd, unsigned soff) {
  unsigned r;
  if (SC) asm volatile("buffer_load_dword %0, %1, %2, %3 offen offset:%4 sc1" : "=v"(r) : "v"(voff), "s"(srd), "s"(soff), "i"(OFF) : "memory");
  else    asm volatile("buffer_load_dword %0, %1, %2, %3 offen offset:%4" : "=v"(r) : "v"(voff), "s"(srd), "s"(soff), "i"(OFF) : "memory");
  return r;
}
template <int SC> DEVI void st16(u32x4 v, unsigned voff, i32x4 srd, unsigned soff) {
  if (SC) asm volatile("buffer_store_dwordx4 %0, %1, %2, %3 offen sc1\n\ts_nop 1" :: "v"(v), "v"(voff), "s"(srd), "s"(soff) : "memory");
  else    asm volatile("buffer_store_dwordx4 %0, %1, %2, %3 offen\n\ts_nop 1" :: "v"(v), "v"(voff), "s"(srd), "s"(soff) : "memory");
}
template <int SC, int OFF = 0> DEVI void st8(u32x2 v, unsigned voff, i32x4 srd, unsigned soff) {
  if (SC) asm volatile("buffer_store_dwordx2 %0, %1, %2, %3 offen offset:%4 sc1" :: "v"(v), "v"(voff), "s"(srd), "s"(soff), "i"(OFF) : "memory");
  else    asm volatile("buffer_store_dwordx2 %0, %1, %2, %3 offen offset:%4" :: "v"(v), "v"(voff), "s"(srd), "s"(soff), "i"(OFF) : "memory");
}
DEVI void st4s(unsigned v, unsigned voff, i32x4 srd) {
  asm volatile("buffer_store_dword %0, %1, %2, 0 offen sc1" :: "v"(v), "v"(voff), "s"(srd) : "memory");
}
template <int N> DEVI void wait_vm() { asm volatile("s_waitcnt vmcnt(%0)" :: "i"(N) : "memory"); }
#define WAIT_LGKM0() asm volatile("s_waitcnt lgkmcnt(0)" ::: "memory")
DEVI void bar() { asm volatile("s_barrier" ::: "memory"); }
#define SB() __builtin_amdgcn_sched_barrier(0)
#define PIN(x) asm volatile("" : "+v"(x))

DEVI unsigned pk2(float a, float b) { bf16x2 v = {(__bf16)a, (__bf16)b}; return __builtin_bit_cast(unsigned, v); }
DEVI float rbf(float a) { return (float)(__bf16)a; }
DEVI bf16x8 asbf(u32x4 v) { return __builtin_bit_cast(bf16x8, v); }
DEVI u32x2 trd(LAS char* p) { return __builtin_bit_cast(u32x2, __builtin_amdgcn_ds_read_tr16_b64_v4i16((LAS s16x4*)p)); }
DEVI u32x4 cat(u32x2 a, u32x2 b) { return __builtin_shufflevector(a, b, 0, 1, 2, 3); }
DEVI f32x4 mfma16(u32x4 a, u32x4 b, f32x4 c) { return __builtin_amdgcn_mfma_f32_16x16x32_bf16(asbf(a), asbf(b), c, 0, 0, 0); }
DEVI void mfma16a(u32x4 a, u32x4 b, f32x4& c) { asm volatile("v_mfma_f32_16x16x32_bf16 %0, %1, %2, %0" : "+v"(c) : "v"(a), "v"(b)); }
DEVI void mfma16z(u32x4 a, u32x4 b, f32x4& c) { asm volatile("v_mfma_f32_16x16x32_bf16 %0, %1, %2, 0" : "=&v"(c) : "v"(a), "v"(b)); }
DEVI void mfma32a(u32x4 a, u32x4 b, f32x16& c) { asm volatile("v_mfma_f32_32x32x16_bf16 %0, %1, %2, %0" : "+v"(c) : "v"(a), "v"(b)); }
DEVI void mfma32z(u32x4 a, u32x4 b, f32x16& c) { asm volatile("v_mfma_f32_32x32x16_bf16 %0, %1, %2, 0" : "=&v"(c) : "v"(a), "v"(b)); }
DEVI void mfma8a(u32x2 a, u32x2 b, f32x16& c) { asm volatile("v_mfma_f32_32x32x8_bf16 %0, %1, %2, %0" : "+v"(c) : "v"(a), "v"(b)); }
DEVI int lane_id() { int l; asm volatile("v_mbcnt_lo_u32_b32 %0, -1, 0\n\tv_mbcnt_hi_u32_b32 %0, -1, %0" : "=v"(l)); return l; }
DEVI f32x16 mfma32(u32x4 a, u32x4 b, f32x16 c) { return __builtin_amdgcn_mfma_f32_32x32x16_bf16(asbf(a), asbf(b), c, 0, 0, 0); }
DEVI float blo(unsigned r) { return __uint_as_float(r << 16); }
DEVI float bhi(unsigned r) { return __uint_as_float(r & 0xffff0000u); }
template <int C> DEVI float dppf(float v) { return __uint_as_float(__builtin_amdgcn_update_dpp(0, __float_as_uint(v), C, 0xf, 0xf, false)); }
DEVI float wave_sum(float v) {
  v += dppf<0x128>(v); v += dppf<0x124>(v); v += dppf<0x122>(v); v += dppf<0x121>(v);
  auto a = __builtin_amdgcn_permlane16_swap(__float_as_uint(v), __float_as_uint(v), false, false);
  v = __uint_as_float(a[0]) + __uint_as_float(a[1]);
  auto b = __builtin_amdgcn_permlane32_swap(__float_as_uint(v), __float_as_uint(v), false, false);
  return __uint_as_float(b[0]) + __uint_as_float(b[1]);
}
DEVI float v_fma(float a, float b, float c) { float r; asm volatile("v_fma_f32 %0, %1, %2, %3" : "=v"(r) : "v"(a), "v"(b), "v"(c)); return r; }
DEVI float v_mul(float a, float b) { float r; asm volatile("v_mul_f32 %0, %1, %2" : "=v"(r) : "v"(a), "v"(b)); return r; }
DEVI float v_exp(float a) { float r; asm volatile("v_exp_f32 %0, %1" : "=v"(r) : "v"(a)); return r; }
DEVI float v_rcp(float a) { float r; asm volatile("v_rcp_f32 %0, %1" : "=v"(r) : "v"(a)); return r; }
DEVI unsigned v_cvt(float a, float b) { unsigned r; asm volatile("v_cvt_pk_bf16_f32 %0, %1, %2" : "=v"(r) : "v"(a), "v"(b)); return r; }
DEVI float dot2(unsigned w, unsigned sel, float c) { return __builtin_amdgcn_fdot2_f32_bf16(__builtin_bit_cast(bf16x2, w), __builtin_bit_cast(bf16x2, sel), c, false); }
DEVI unsigned sel(bool c, unsigned a, unsigned b) { return c ? a : b; }
DEVI bool any_lane(bool c) { return __builtin_amdgcn_ballot_w64(c) != 0; }
constexpr int SPIN = 1 << 15;
DEVI void poll_rest(unsigned v, bool need, unsigned tag, unsigned voff, i32x4 srd) {
  if (any_lane(need & (v != tag))) {
    for (int it = 0; it < SPIN; it++) {
      __builtin_amdgcn_s_sleep(1);
      v = ld4<1>(voff, srd, 0);
      asm volatile("s_waitcnt vmcnt(0)" : "+v"(v) :: "memory");
      if (!any_lane(need & (v != tag))) break;
    }
  }
}

#define TS(i)
#define TSDECL
#define TSOUT()
template <int I> struct IC { static constexpr int v = I; };
template <int B, int E, class F> DEVI void sfor(F&& f) { if constexpr (B < E) { f(IC<B>{}); sfor<B + 1, E>(f); } }

struct Params {
  const char *x, *wqkv, *wo, *wg, *wu, *wd;
  char *out, *ws;
};

extern __shared__ __attribute__((aligned(16))) char smem_[];

constexpr unsigned UN = 8192;
constexpr unsigned LW(int s) { return s % 3 == 0 ? 0 : s % 3 == 1 ? 4 * UN : 7 * UN; }
constexpr unsigned L_PK = 3 * UN, L_X = 10 * UN, L_CSR = 16 * UN;
constexpr unsigned LG(int g) { return (g == 0 || g == 8 ? 16 : g == 1 || g == 9 ? 18 : g == 2 ? 4 : g == 3 ? 12 : g == 4 ? 6 : g == 5 ? 8 : g == 6 ? 0 : 2) * UN; }
constexpr int KS(int g) { return g < 2 ? g : g < 4 ? g + 6 : g - 2; }
constexpr unsigned AT_K = 4 * UN, AT_V = 8 * UN, R_A = 12 * UN;
constexpr int cl(int v, int lo, int hi) { return v < lo ? lo : v > hi ? hi : v; }

__global__ __launch_bounds__(512, 2) void k_fused(Params p) {
  TSDECL;
  LAS char* sm = (LAS char*)smem_;
  const unsigned lb = (unsigned)(size_t)sm;
  const int bid = blockIdx.x, xcd = bid & 7, loc = bid >> 3;
  const int wave = __builtin_amdgcn_readfirstlane(threadIdx.x >> 6);
  int lane = threadIdx.x & 63;
  const int rb = (xcd & 1) * 8 + (loc & 7), jg = (xcd >> 1) * 4 + (loc >> 3), pr = jg >> 1, tyB = jg & 1;
  const int mw = wave & 3, cw = wave >> 2;
  const bool spec = jg == 15, likeA = !tyB || spec;
  const i32x4 sx = mk_srd(p.x, X_BYTES), sq = mk_srd(p.wqkv, WQ_BYTES), sg = mk_srd(p.wg, WF_BYTES), su = mk_srd(p.wu, WF_BYTES);
  const int rot = (xcd & 1) * 5;
  auto sidx = [&](int s) { return (unsigned)(s < 5 ? s + rot : s - rot); };
  const unsigned drow = 8 * wave + (lane >> 3), wcol = ((lane & 7) ^ ((lane >> 2) & 4)) * 16u;
  const unsigned vx = (rb * 128 + drow) * 1280u + (((lane & 7) ^ (((wave & 1) << 2) | (lane >> 4))) * 16u);
  const unsigned vq = drow * 3840u + wcol, vg = drow * 3072u + wcol, vu = vg ^ 32u;
  const unsigned v1 = likeA ? vg : vq, v2 = likeA ? vu : vq;
  const i32x4 s1 = sel_srd(likeA, sg, sq), s2 = sel_srd(likeA, su, sq);
  const unsigned gl1 = spec ? 3 * (rb >> 1) + 1 + (rb & 1) : 3 * pr, gl2 = 3 * pr + 1 + tyB;
  const unsigned c0 = (4 * pr + tyB) * 128u, c1 = likeA ? gl1 * 128u : c0 + 128u, c2 = likeA ? c1 : c0 + 256u;
  const unsigned st1 = likeA ? 196608u : 245760u;
  const unsigned ldw = lb + wave * 1024u;
  auto issue = [&](int s, int b) {
    if (b < 2) dma16(ldw + L_X + (s % 3) * 16384u + b * UN, vx, sx, b * 81920u + sidx(s) * 128u);
    else if (b == 2) dma16(ldw + LW(s), vq, sq, sidx(s) * 245760u + c0);
    else if (b == 3) dma16(ldw + LW(s) + UN, v1, s1, sidx(s) * st1 + c1);
    else dma16(ldw + LW(s) + 2 * UN, v2, s2, sidx(s) * st1 + c2);
  };
  auto issue_g = [&](int g, int b) { dma16(ldw + LG(g) + b * UN, b ? vu : vg, b ? su : sg, sidx(KS(g)) * 196608u + gl2 * 128u); };
#pragma unroll
  for (int b = 0; b < 5; b++) issue(0, b);
  const i32x4 sc = mk_srd(p.ws, WS_BYTES);
  const unsigned srow = rb * 128 + jg * 8 + wave;
  unsigned vl = lane * 16u;
  unsigned ep = ld4<1>(0u, sc, 0);
  u32x4 r0 = ld16<0>(vl, sx, srow * 1280u);
  unsigned r1 = ld4<0, 1024>(lane * 4u, sx, srow * 1280u);
  bar();
#pragma unroll
  for (int b = 0; b < 10; b++) issue(1 + b / 5, b % 5);
  TS(1);
  const i32x4 sd = mk_srd(p.wd, WF_BYTES), so = mk_srd(p.wo, WO_BYTES);
  const unsigned kA = bid * 205u >> 10, cA = bid - kA * 5u, kB = (bid + 256u) * 205u >> 10, cB = bid + 256u - kB * 5u;
  const unsigned offB = bid < 84 ? 0u : OOB;
  const unsigned vpk = (4 * wave + (lane >> 4)) * 1280u + (lane & 15) * 16u;
  dma16(ldw + L_PK, vpk, sel_srd(kA < 48, sd, so), (kA < 48 ? kA : kA - 48) * 40960u + cA * 256u);
  LAS char* pkr = sm + (L_PK + (8 * (lane >> 4) + ((lane & 15) >> 2)) * 256u + wave * 32u + (lane & 3) * 8u);
  int hi = lane >> 5;
  LAS char* xa[4];
#pragma unroll
  for (int ks = 0; ks < 4; ks++) { xa[ks] = sm + (L_X + (32 * mw + (lane & 31)) * 128u + (((2 * ks + hi) ^ ((lane >> 1) & 7)) * 16u)); PIN(xa[ks]); }
  LAS char *wa[3], *wr[2];
  {
    const unsigned ch = (lane >> 4) & 1, r = (lane >> 2) & 3, c = lane & 3;
    const unsigned wl = hi * 1024u + r * 128u + ((cw ^ (r >> 1)) * 64u) + (((c & 1) << 1 | c >> 1) * 8u);
    const unsigned q = wl + ch * 32u, g0 = wl + ch * (UN + 32u), g1 = wl + ch * UN + (ch ^ 1) * 32u;
    wa[0] = sm + q; wa[1] = sm + (UN + (likeA ? g0 : q)); wa[2] = sm + (likeA ? UN + g1 : 2 * UN + q);
    wr[0] = sm + (g0 + LG(0)); wr[1] = sm + (g1 + LG(0));
    PIN(wa[0]); PIN(wa[1]); PIN(wa[2]); PIN(wr[0]); PIN(wr[1]);
  }
  const int cst = loc & 7, cskey = cst < 3 ? mw : -1;
  const unsigned csb = C_CS + (jg * 10 + cw) * 512u;
  const u32x4 ones = {0x3f803f80u, 0x3f803f80u, 0x3f803f80u, 0x3f803f80u};
  const unsigned Rq = rb * 4 + mw;
  const unsigned fme = ((jg * 16 + rb) * 8 + wave) * 128u;
  const int head = xcd * 4 + (loc & 3), ch = loc >> 2, par = head & 1, o0 = 5 * head >> 1;
  const int rt = xcd * 4 + (loc & 3), cg = loc >> 2;
  const unsigned w3b = WS_W3 + (wave * 40 + cg * 5) * 1024u;
  u32x4 RW[6][5], Q1, Q2;
  unsigned tag = 0;
  auto ld_w3 = [&](int buf, int j, int u) { RW[buf][u] = ld16<1>(vl, sc, w3b + j * 327680u + u * 1024u); };
  auto kv_issue = [&]() {
    const int l = lane_id(), oi = ((l >> 2) & 3) ^ (l >> 4), R = ch * 8 - 8 + 2 * wave;
    const unsigned v = sel(oi < (R >= 0 ? 3 : 0), oi * 512u + ((l >> 4) * 4 + (l & 3)) * 16u, OOB);
    const unsigned b = WS_QKV + (unsigned)((R >= 0 ? R : 0) * 240 + 80 + o0) * 512u;
#pragma unroll
    for (int i = 0; i < 8; i++)
      dma16<1>(lb + AT_K + (i >> 2) * 32768u + wave * 4096u + (i & 3) * 1024u, v, sc, b + (i >> 2) * 40960u + ((i >> 1) & 1) * 122880u + (i & 1) * 256u);
    const unsigned qb = WS_QKV + (unsigned)((ch * 8 + wave) * 240 + o0) * 512u;
    Q1 = ld16<1>(l * 16u, sc, qb);
    Q2 = ld16<1, 1024>(sel(l < 32, l * 16u, OOB), sc, qb);
  };

  auto rest = [&]() __attribute__((always_inline)) {
  lane = lane_id(); hi = lane >> 5; vl = lane * 16u;
  const int q32 = lane & 31;
  const unsigned ab = WS_AF + (rt * 4 * 68 + wave) * 1024u;
  const unsigned r7 = wave < 4 ? R_A + 40960u + wave * 5120u : (wave - 4) * 5120u;
  u32x4 A[4][4];
  auto ld_a = [&](int buf, int j, int i, unsigned v) { A[buf][i] = ld16<1>(v, sc, ab + j * 8192u + i * 69632u); };
  auto dma_w3 = [&](unsigned dst, int j, int u) { dma16<1>(lb + dst + u * 1024u, vl, sc, w3b + j * 327680u + u * 1024u); };
  f32x16 o;
  {
    LAS char* cw_ = sm + (AT_K + wave * 4096 + hi * 2048 + (q32 >> 2) * 256 + ((3 ^ ((q32 >> 2) & 3)) * 64) + (q32 & 3) * 16);
    *(LAS u32x4*)cw_ = u32x4{(ch == 0 && wave < 4) ? 0x3f80fe00u : 0x3f800000u, 0u, 0u, 0u};
    *(LAS u32x4*)(cw_ + 32768) = ones;
    {
      const bool z1 = par & !hi;
      Q1[0] = sel(z1, 0u, Q1[0]); Q1[1] = sel(z1, 0u, Q1[1]);
      Q2[2] = sel(par, Q2[2], 0u); Q2[3] = sel(par, Q2[3], 0u);
      Q2[0] = sel(hi, 0x00003f80u, Q2[0]);
    }
    WAIT_LGKM0();
    bar();
    TS(20);
#pragma unroll
    for (int u = 0; u < 5; u++) ld_w3(3, 3, u);
    LAS char* kp = sm + (AT_K + wave * 2048 + (q32 >> 2) * 256 + ((hi ^ ((q32 >> 2) & 3)) * 64) + (q32 & 3) * 16);
    LAS char* kp1 = sm + (((unsigned)(size_t)kp - lb) ^ 128u);
    LAS char* vp[2];
    {
      const int mq = 4 * ((lane >> 4) & 1) + (lane & 3), iq = sel(mq < 4, 2 * par + ((mq & 1) << 1 | mq >> 1), sel(mq == 4, par ? 1 : 4, 6));
#pragma unroll
      for (int h = 0; h < 2; h++) vp[h] = sm + (AT_V + wave * 2048 + hi * 256 + h * 512 + (((iq >> 1) ^ (2 * h + hi)) * 64) + ((lane >> 2) & 3) * 16 + (iq & 1) * 8);
    }
    f32x16 z;
#pragma unroll
    for (int r = 0; r < 16; r++) z[r] = 0.f;
    auto scq = [&](int off, u32x4 q2) {
      const u32x4 a = *(LAS u32x4*)(kp + off), b = *(LAS u32x4*)(kp1 + off);
      return mfma32(b, q2, mfma32(a, Q1, z));
    };
    auto pv = [&](int off, const unsigned* pk_) {
      const u32x4 V1 = cat(trd(vp[0] + off), trd(vp[1] + off)), V2 = cat(trd(vp[0] + off + 1024), trd(vp[1] + off + 1024));
      o = mfma32(V1, u32x4{pk_[0], pk_[1], pk_[2], pk_[3]}, o);
      o = mfma32(V2, u32x4{pk_[4], pk_[5], pk_[6], pk_[7]}, o);
    };
    const int dl = q32 - 4 * hi;
    auto xmax = [&](float m) {
      auto r = __builtin_amdgcn_permlane32_swap(__float_as_uint(m), __float_as_uint(m), false, false);
      return __builtin_fmaxf(__uint_as_float(r[0]), __uint_as_float(r[1]));
    };
    auto edge = [&](const f32x16& ee, float m) {
      unsigned pa[8], pb[8];
#pragma unroll
      for (int r = 0; r < 16; r += 2) {
        const bool b0 = (r & 3) + 8 * (r >> 2) <= dl, b1 = ((r + 1) & 3) + 8 * (r >> 2) <= dl;
        const unsigned x = pk2(__builtin_amdgcn_exp2f(ee[r] - m), __builtin_amdgcn_exp2f(ee[r + 1] - m));
        const unsigned mk = (b0 ? 0xffffu : 0u) | (b1 ? 0xffff0000u : 0u);
        pb[r >> 1] = x & mk; pa[r >> 1] = x & ~mk;
      }
      pv(0, pa); pv(8 * 2048, pb);
    };
    auto merged = [&]() {
      const f32x16 sa = scq(0, Q2), sb = scq(8 * 2048, Q2);
      f32x16 ee;
#pragma unroll
      for (int r = 0; r < 16; r++) ee[r] = ((r & 3) + 8 * (r >> 2) <= dl) ? sb[r] : sa[r];
      return ee;
    };
    auto vmax = [&](const f32x16& ee, float m) {
#pragma unroll
      for (int r = 0; r < 16; r += 2) m = __builtin_fmaxf(__builtin_fmaxf(m, ee[r]), ee[r + 1]);
      return m;
    };
    unsigned vfh;
    {
      const unsigned gl = (wave >> 1) + 4 * (lane >> 1), pq = gl * 43u >> 7, jp = 2 * pq + (gl - 3 * pq == 2), R = rt * 2 + (lane & 1);
      vfh = sel(lane < 12, C_FH + ((jp * 16 + (R >> 2)) * 8 + (wave & 1) * 4 + (R & 3)) * 128u, OOB);
    }
    unsigned hf;
    o = z;
    {
      const f32x16 ee = merged();
      const unsigned mb = pk2(xmax(vmax(ee, -3.0e38f)), 0.f) << 16;
      const float m = __uint_as_float(mb);
      const u32x4 Q3 = {sel(hi, (mb ^ 0x80000000u) | 0x3f80u, Q2[0]), Q2[1], Q2[2], Q2[3]};
      f32x16 sn = scq(2048, Q3);
      edge(ee, m);
      sfor<1, 8>([&](auto J) {
        constexpr int j = J.v;
        const f32x16 s = sn;
        if constexpr (j < 7) sn = scq((j + 1) * 2048, Q3);
        if constexpr (j == 1) { ld_w3(4, 4, 0); ld_w3(4, 4, 1); ld_w3(4, 4, 2); ld_w3(4, 4, 3); }
        if constexpr (j == 2) {
          wait_vm<9>();
          st4s(tag, sel(lane == 0, C_FH + fme, OOB), sc);
          TS(21);
          ld_w3(4, 4, 4); dma_w3(r7, 7, 0); dma_w3(r7, 7, 1); dma_w3(r7, 7, 2);
        }
        if constexpr (j == 3) { dma_w3(r7, 7, 3); dma_w3(r7, 7, 4); hf = ld4<1>(vfh, sc, 0); }
        if constexpr (j == 4) { dma_w3(R_A + wave * 5120u, 6, 0); dma_w3(R_A + wave * 5120u, 6, 1); dma_w3(R_A + wave * 5120u, 6, 2); }
        if constexpr (j == 5) {
          dma_w3(R_A + wave * 5120u, 6, 3); dma_w3(R_A + wave * 5120u, 6, 4);
          asm volatile("s_waitcnt vmcnt(5)" : "+v"(hf) :: "memory");
          TS(22);
          poll_rest(hf, lane < 12, tag, vfh, sc);
        }
        if constexpr (j >= 5) { ld_a(j - 5, j - 5, 0, vl); ld_a(j - 5, j - 5, 1, vl); ld_a(j - 5, j - 5, 2, vl); ld_a(j - 5, j - 5, 3, vl); }
        unsigned pk_[8];
#pragma unroll
        for (int r = 0; r < 16; r += 2) pk_[r >> 1] = pk2(__builtin_amdgcn_exp2f(s[r]), __builtin_amdgcn_exp2f(s[r + 1]));
        pv(j * 2048, pk_);
      });
    }
    {
      float mx = 0.f;
#pragma unroll
      for (int r = 0; r < 12; r += 2) mx = __builtin_fmaxf(__builtin_fmaxf(mx, __builtin_fabsf(o[r])), __builtin_fabsf(o[r + 1]));
      if (any_lane(!((o[12] >= 5.9604645e-8f) & (o[12] <= 16777216.f) & (mx < 3.0e38f)))) {
        float m = vmax(merged(), -3.0e38f);
#pragma nounroll
        for (int j = 1; j < 8; j++) m = vmax(scq(j * 2048, Q2), m);
        m = xmax(m);
        o = z;
        edge(merged(), m);
#pragma nounroll
        for (int j = 1; j < 8; j++) {
          const f32x16 s = scq(j * 2048, Q2);
          unsigned pk_[8];
#pragma unroll
          for (int r = 0; r < 16; r += 2) pk_[r >> 1] = pk2(__builtin_amdgcn_exp2f(s[r] - m), __builtin_amdgcn_exp2f(s[r + 1] - m));
          pv(j * 2048, pk_);
        }
      }
    }
    TS(23);
    const float inv = __builtin_amdgcn_rcpf(o[12]);
    const unsigned tb_ = (unsigned)((ch * 8 + wave) * 2 + (q32 >> 4)) * 68u + 48u;
    const unsigned oa = o0 + par + hi, ob = o0 + (par ? 0 : 2);
    st16<1>(u32x4{pk2(o[0] * inv, o[1] * inv), pk2(o[2] * inv, o[3] * inv), pk2(o[4] * inv, o[5] * inv), pk2(o[6] * inv, o[7] * inv)},
            (tb_ + (oa >> 2)) * 1024u + (oa & 3) * 256u + (q32 & 15) * 16u, sc, WS_AF);
    st8<1>(u32x2{pk2(o[8] * inv, o[9] * inv), pk2(o[10] * inv, o[11] * inv)},
           sel(hi, OOB, (tb_ + (ob >> 2)) * 1024u + (ob & 3) * 256u + (q32 & 15) * 16u + par * 8u), sc, WS_AF);
  }
  const unsigned vl8 = sel(wave < 4, vl, OOB);
  unsigned vfo, vo[3], lo_[3], lo2[3];
  f32x4 fa[4][5];
  unsigned of = 0;
  u32x2 xr[3];
#define PINA(n) "+v"(A[n][0]), "+v"(A[n][1]), "+v"(A[n][2]), "+v"(A[n][3])
#define PINW(n) "+v"(RW[n][0]), "+v"(RW[n][1]), "+v"(RW[n][2]), "+v"(RW[n][3]), "+v"(RW[n][4])
  asm volatile("s_waitcnt vmcnt(10)" : PINA(0), PINW(2), PINW(3), PINW(4) :: "memory");
  TS(24);
  auto step = [&](auto J_) {
    constexpr int j = J_.v, wb = j < 6 ? j : j == 6 ? 1 : j == 7 ? 2 : 0, abuf = j & 3;
    if constexpr (j == 1) asm volatile("s_waitcnt vmcnt(6)" : PINA(1) :: "memory");
    if constexpr (j == 2) {
      asm volatile("s_waitcnt vmcnt(9)" : PINA(2) :: "memory");
      st4s(tag, sel(vl == 0, C_FO + ((ch * 8 + wave) * 32u + head) * 128u, OOB), sc);
      TS(25);
    }
    if constexpr (j == 3) asm volatile("s_waitcnt vmcnt(15)" : PINA(3) :: "memory");
    if constexpr (j == 4) {
      asm volatile("s_waitcnt vmcnt(0)" : PINA(0), PINA(1), "+v"(of), PINW(0), PINW(5) :: "memory");
      TS(26);
      poll_rest(of, vfo != OOB, tag, vfo, sc);
      TS(27);
    }
    if constexpr (j == 6) asm volatile("s_waitcnt vmcnt(8)" : PINA(2) :: "memory");
    if constexpr (j == 7) asm volatile("s_waitcnt vmcnt(7)" : PINA(3) :: "memory");
    SB();
    sfor<0, 20>([&](auto K_) {
      constexpr int k = K_.v, u = k >> 2, i = k & 3;
      if constexpr (j == 0) mfma16z(RW[wb][u], A[abuf][i], fa[i][u]);
      else mfma16a(RW[wb][u], A[abuf][i], fa[i][u]);
      if constexpr (j >= 1 && j <= 3 && k < 4) ld_a((j + 2) & 3, j + 2, k, vl);
      if constexpr (j == 1 && k >= 4 && k < 9) ld_w3(5, 5, k - 4);
      if constexpr (j == 2 && k >= 4 && k < 9) RW[0][k - 4] = ld16<1>(vl8, sc, w3b + 8 * 327680u + (k - 4) * 1024u);
      if constexpr (j == 3 && k == 5) {
        const int l = lane_id(), h5 = 5 * (l & 31);
        auto ov = [&](int q) { return (h5 + 5 > 8 * q) & (h5 < 8 * q + 8); };
        vfo = sel(ov(wave) | ov(wave + 8) | ov(wave + 16), C_FO + (rt * 64u + l) * 128u, OOB);
        PIN(vfo);
      }
      if constexpr (j == 3 && k == 6) of = ld4<1>(vfo, sc, 0);
      if constexpr (j == 4 && k < 8) ld_a(2 + (k >> 2), 6 + (k >> 2), k & 3, vl);
      if constexpr (j == 5 && k < 4) ld_a(0, 8, k, vl8);
      if constexpr (j == 5 && k >= 8 && k < 13) RW[1][k - 8] = *(LAS u32x4*)(sm + (R_A + wave * 5120 + vl) + (k - 8) * 1024);
      if constexpr (j == 5 && k >= 13 && k < 18) RW[2][k - 13] = *(LAS u32x4*)(sm + (r7 + vl) + (k - 13) * 1024);
      if constexpr (j == 6 && k >= 2 && k < 14 && (k & 3) == 2) {
        constexpr int q = k >> 2;
        const unsigned c = ((wave < 4 ? 3 * wave : 4 + 2 * wave) + q) * 64u + lane_id(), row = c * 3277u >> 16, pos = c - row * 20u;
        vo[q] = sel(q == 2 && wave >= 4, OOB, (rt * 64u + row) * 1280u + cg * 160u + pos * 8u);
        const unsigned in = (row & 31) * 160u + ((pos ^ ((row >> 2) & 3)) * 8u);
        lo_[q] = R_A + (row >> 5) * 40960u + in;
        lo2[q] = sel(row >> 5, 0u, R_A + 20480u) + in;
        PIN(lo_[q]); PIN(lo2[q]);
        xr[q] = ld8<0>(vo[q], sx, 0);
      }
      SB();
    });
  };
  sfor<0, 8>(step);
  asm volatile("s_waitcnt vmcnt(0)" : PINA(0), "+v"(xr[0]), "+v"(xr[1]), "+v"(xr[2]) :: "memory");
  if (wave < 4) step(IC<8>{});
  asm volatile("s_nop 7" ::: "memory");
  SB();
  TS(28);
  lane = lane_id();
  const int t16 = lane & 15, g = lane >> 4;
  {
    const unsigned pin = t16 * 160 + ((g ^ (t16 >> 2)) * 8);
    LAS char* pw0 = sm + (R_A + wave * 5120 + pin);
    LAS char* pw1 = sm + (r7 + pin);
#pragma unroll
    for (int i = 0; i < 4; i++)
#pragma unroll
      for (int u = 0; u < 5; u++)
        *(LAS u32x2*)((i >> 1 ? pw1 : pw0) + ((i & 1) * 2560 + u * 32)) = u32x2{pk2(fa[i][u][0], fa[i][u][1]), pk2(fa[i][u][2], fa[i][u][3])};
    WAIT_LGKM0();
    bar();
    const i32x4 sout = mk_srd(p.out, X_BYTES);
    unsigned slo = 0x00003f80u, shi = 0x3f800000u;
    asm volatile("" : "+s"(slo), "+s"(shi));
    auto fin = [&](int q) {
      float a0 = blo(xr[q][0]), a1 = bhi(xr[q][0]), a2 = blo(xr[q][1]), a3 = bhi(xr[q][1]);
#pragma unroll
      for (int w = 0; w < 8; w++) {
        const u32x2 v = *(LAS u32x2*)(sm + (w < 4 ? lo_[q] : lo2[q]) + (w & 3) * 5120);
        a0 = dot2(v[0], slo, a0); a1 = dot2(v[0], shi, a1); a2 = dot2(v[1], slo, a2); a3 = dot2(v[1], shi, a3);
      }
      st8<0>(u32x2{pk2(a0, a1), pk2(a2, a3)}, vo[q], sout, 0);
    };
    fin(0); fin(1);
    if (wave < 4) fin(2);
  }
  TSOUT();
  __builtin_amdgcn_endpgm();
  };

  auto phase1 = [&](auto SP_) {
    constexpr int SP = SP_.v;
    u32x4 X[32], xt[2];
    f32x16 acc[3], cs;
    u32x2 wf[2][3][2], w2[4][2], pk[2], gs, gc[3];
    u32x2 As[3], Bc;
    float sd, rs, rg;
    unsigned pf;
    float e[10], sacc = 0.f, mean = 0.f, cp[4];
    auto stat = [&](int k) {
      if (k < 5) { const unsigned w = k < 4 ? r0[k & 3] : r1; e[2 * k] = blo(w); e[2 * k + 1] = bhi(w); sacc += e[2 * k] + e[2 * k + 1]; }
      else if (k >= 11 && k < 16) {
        const float d0 = e[2 * k - 22] - mean, d1 = e[2 * k - 21] - mean;
        sacc = __builtin_fmaf(d0, d0, sacc); PIN(sacc); sacc = __builtin_fmaf(d1, d1, sacc);
      } else if (k < 22) {
        const int q = k < 11 ? k - 5 : k - 16;
        if (q == 0) sacc += dppf<0x128>(sacc);
        else if (q == 1) sacc += dppf<0x124>(sacc);
        else if (q == 2) sacc += dppf<0x122>(sacc);
        else if (q == 3) sacc += dppf<0x121>(sacc);
        else if (q == 4) { auto a = __builtin_amdgcn_permlane16_swap(__float_as_uint(sacc), __float_as_uint(sacc), false, false); sacc = __uint_as_float(a[0]) + __uint_as_float(a[1]); }
        else {
          auto a = __builtin_amdgcn_permlane32_swap(__float_as_uint(sacc), __float_as_uint(sacc), false, false);
          sacc = (__uint_as_float(a[0]) + __uint_as_float(a[1])) * (1.0f / 640.0f);
          if (k == 10) { mean = sacc; sacc = 0.f; PIN(mean); }
        }
      } else if (k == 22) sacc = __builtin_amdgcn_sqrtf(rbf(rbf(sacc) + 1e-5f));
      PIN(sacc);
    };
    auto rdw = [&](int n, int r) {
      wf[n & 1][r >> 1][r & 1] = trd(wa[r >> 1] + (LW(n >> 2) + (n & 3) * 2048 + (r & 1) * 512));
    };
    auto rdx = [&](int n) {
      const u32x4 v = *(LAS u32x4*)(xa[n & 3] + ((n >> 2) % 3) * 16384);
      if (n < 32) X[n] = v; else xt[n & 1] = v;
    };
    auto wfr = [&](int n, int t) { return cat(wf[n & 1][t][0], wf[n & 1][t][1]); };
    const unsigned vl8 = lane * 8u;
    auto ld_gran = [&](int k) {
      if (k == 0) gs = ld8<1>((vl8 & 56u) | (vl8 & 192u) << 1, sc, C_STAT + Rq * 512u);
      else if (k < 4) gc[k - 1] = ld8<1>(vl8, sc, csb + (k - 1) * 1024u);
      else pf = ld4<1>((vl8 & 248u) << 4, sc, C_PF + wave * 4096u);
    };
    auto split3 = [&](unsigned g) {
      const float c = __uint_as_float(g), r1_ = c - rbf(c);
      const unsigned a0 = pk2(c, r1_);
      u32x2 a = {a0, pk2(r1_ - bhi(a0), 0.f)};
      PIN(a);
      return a;
    };
    auto prep = [&](int k) {
      if (k == 0) {
        if (any_lane((gs[1] != tag) | (gc[0][1] != tag) | (gc[1][1] != tag) | (gc[2][1] != tag))) {
          for (int it = 0; it < SPIN; it++) {
            __builtin_amdgcn_s_sleep(1);
#pragma unroll
            for (int i = 0; i < 4; i++) ld_gran(i);
            asm volatile("s_waitcnt vmcnt(0)" : "+v"(gs), "+v"(gc[0]), "+v"(gc[1]), "+v"(gc[2]) :: "memory");
            if (!any_lane((gs[1] != tag) | (gc[0][1] != tag) | (gc[1][1] != tag) | (gc[2][1] != tag))) break;
          }
        }
      } else if (k == 1) {
        const unsigned nm = (gs[0] & 0xffffu) ^ 0x8000u;
        Bc = u32x2{nm | nm << 16, nm}; PIN(Bc);
        sd = bhi(gs[0]); PIN(sd); rs = __builtin_amdgcn_rcpf(sd); PIN(rs);
      } else if (k == 2) { As[0] = split3(gc[0][0]); rg = -1.44269504089f * rs; PIN(rg); }
      else if (k < 5) { if (!SP) As[k - 2] = split3(gc[k - 2][0]); }
    };
#pragma unroll
    for (int r = 0; r < 16; r++) cs[r] = 0.f;
    PIN(cs);
    u32x4 on1 = ones;
    PIN(on1);
    wait_vm<14>();
    bar();
    TS(2);
    rdx(0);
#pragma unroll
    for (int r = 0; r < 6; r++) rdw(0, r);
    SB();
    sfor<0, 40>([&](auto N_) {
      constexpr int n = N_.v, s = n >> 2, ks = n & 3;
      if constexpr (ks == 3) {
        WAIT_LGKM0();
        if constexpr (s == 0) { asm volatile("s_waitcnt vmcnt(6)" : "+v"(ep), "+v"(r0), "+v"(r1) :: "memory"); tag = __builtin_amdgcn_readfirstlane(ep) + 1u; }
        else if constexpr (s == 1) wait_vm<5>();
        else if constexpr (s == 2) wait_vm<6>();
        else if constexpr (s == 3) wait_vm<8>();
        else if constexpr (s == 4) wait_vm<5>();
        else if constexpr (s == 5) wait_vm<6>();
        else if constexpr (s == 6) wait_vm<6>();
        else if constexpr (s == 7) wait_vm<8>();
        else if constexpr (s == 8) wait_vm<8>();
        else asm volatile("s_waitcnt vmcnt(4)" : "+v"(gs), "+v"(gc[0]), "+v"(gc[1]), "+v"(gc[2]) :: "memory");
        bar();
        TS(3 + s);
        SB();
      }
      sfor<0, 3>([&](auto T_) {
        constexpr int t = T_.v, m = ((n + 1) & 3) * 3 + t, sb = ((n + 1) >> 2) - 1;
        const u32x4 xf = n < 32 ? X[n < 32 ? n : 0] : xt[n & 1];
        if constexpr (SP && t > 0) { if constexpr (n == 0) mfma32z(on1, wfr(n, t), acc[t]); else mfma32a(on1, wfr(n, t), acc[t]); }
        else { if constexpr (n == 0) mfma32z(wfr(n, t), xf, acc[t]); else mfma32a(wfr(n, t), xf, acc[t]); }
        if constexpr (n < 39) {
          if constexpr (t < 2) { rdw(n + 1, 3 * t); rdw(n + 1, 3 * t + 1); rdw(n + 1, 3 * t + 2); }
          if constexpr (t == 0) rdx(n + 1);
        } else {
          if constexpr (t < 2) { w2[t][0] = trd(wr[t]); w2[t][1] = trd(wr[t] + 512); }
        }
        if constexpr (sb == 1 && m == 0) *(LAS unsigned*)(sm + L_CSR + 2048 + wave * 256 + lane * 4) = pk2(mean, sacc);
        if constexpr (sb == 2 && m == 0) pf = *(LAS unsigned*)(sm + L_CSR + 2048 + (lane & 7) * 256);
        if constexpr (sb == 2 && m == 4) st8<1>(u32x2{pf, tag}, sel(vl8 < (wave == 0 ? 64u : 0u), vl8, OOB), sc, C_STAT + (rb * 16 + jg) * 128u);
        if constexpr (sb == 6 && m == 0) st4s(tag, sel(vl8 == 0, wave == 0 ? C_PF + bid * 128u : OOB, OOB), sc);
        if constexpr (sb >= 0 && sb < 7 && (m & 1) && m < 10) issue(sb + 3, m >> 1);
        if constexpr ((sb == 1 || sb == 4) && (m == 2 || m == 4)) pk[m / 2 - 1] = trd(pkr + (m / 2 - 1) * 1024);
        if constexpr (sb == 1 && m == 10) st16<1>(cat(pk[0], pk[1]), vl, sc, WS_W3 + (kA * 40u + cA * 8u + wave) * 1024u);
        if constexpr (sb == 4 && m == 10) st16<1>(cat(pk[0], pk[1]), vl | offB, sc, WS_W3 + (kB * 40u + cB * 8u + wave) * 1024u);
        if constexpr (sb == 2 && m == 10) dma16(ldw + L_PK, vpk | offB, mk_srd(p.wo, WO_BYTES), (kB - 48) * 40960u + cB * 256u);
        if constexpr (sb == 0) { stat(2 * m); stat(2 * m + 1); }
        if constexpr (sb == 4 && m == 6) { PIN(cs); *(LAS float*)(sm + L_CSR + wave * 128 + (lane & 31) * 4) = cs[0]; }
        if constexpr (sb == 5 && m < 4) cp[m] = *(LAS float*)(sm + L_CSR + cw * 512 + m * 128 + (lane & 31) * 4);
        if constexpr (sb == 5 && m == 6)
          st8<1>(u32x2{__float_as_uint((cp[0] + cp[1]) + (cp[2] + cp[3])), tag}, sel(vl8 < (cskey == 0 ? 256u : 0u), vl8, OOB), sc, csb + cst * 1024u + (xcd & 1) * 256u);
        if constexpr (sb == 6 && m == 11) { issue_g(0, 0); issue_g(0, 1); }
        if constexpr (sb == 7 && (m & 1)) issue_g(1 + m / 4, (m >> 1) & 1);
        if constexpr (sb == 8 && m < 4) ld_gran(m);
        if constexpr (sb == 8 && (m & 1) && m >= 5) issue_g(4 + (m - 5) / 4, ((m - 5) >> 1) & 1);
        if constexpr (sb == 9 && m == 0) ld_gran(4);
        if constexpr (sb == 9 && m > 0) { issue_g(5 + m, 0); issue_g(5 + m, 1); }
        if constexpr (sb == 9) prep(m);
        SB();
      });
      if constexpr (s < 5) {
        asm volatile("s_cmp_lg_u32 %5, %6\n\ts_cbranch_scc1 .Lce%=\n\t"
                     "s_cmp_lg_u32 %7, 0\n\ts_cbranch_scc1 .Lc1%=\n\t"
                     "v_mfma_f32_32x32x16_bf16 %0, %1, %2, %0\n\ts_branch .Lce%=\n"
                     ".Lc1%=:\n\ts_cmp_lg_u32 %7, 1\n\ts_cbranch_scc1 .Lc2%=\n\t"
                     "v_mfma_f32_32x32x16_bf16 %0, %1, %3, %0\n\ts_branch .Lce%=\n"
                     ".Lc2%=:\n\tv_mfma_f32_32x32x16_bf16 %0, %1, %4, %0\n"
                     ".Lce%=:"
                     : "+v"(cs) : "v"(on1), "v"(wfr(n, 0)), "v"(wfr(n, 1)), "v"(wfr(n, 2)), "s"(cskey), "i"(ks), "s"(cst) : "scc");
        SB();
      }
    });
    TS(13);
    auto round2 = [&](auto TY_) {
      constexpr int TY = TY_.v;
      f32x16 ac2[2];
      u32x4 qv, hv[4], A4[3];
      u32x2 gc2[2];
      float t[8], rq;
      unsigned fq;
      const unsigned vh = (lane & 15) * 16u + ((lane >> 4) & 1) * 69632u + hi * 256u;
      auto qop = [&](auto U_, auto K_) {
        constexpr int u = U_.v, k = K_.v;
        const unsigned ln = 4 * pr + tyB + u;
        if constexpr (k == 0) {
          const float qs = ln < 10u ? 0.22360679775f * 1.44269504089f : 1.0f;
          asm volatile("v_mul_f32 %0, %1, %2" : "=v"(rq) : "s"(qs), "v"(rs));
        } else {
          constexpr int h = (k - 1) / 13, j = (k - 1) % 13;
          if constexpr (j < 8) t[j] = v_mul(acc[u][8 * h + j], rq);
          else if constexpr (j < 12) qv[j - 8] = v_cvt(t[2 * j - 16], t[2 * j - 15]);
          else st16<1>(qv, vl, sc, WS_QKV + (Rq * 240u + ln * 8u + cw * 4u + h * 2u) * 512u);
        }
      };
      auto gop = [&](f32x16& a, u32x4& o, auto K_) {
        constexpr int k = K_.v, ph = k >> 3, j = k & 7;
        if constexpr (ph == 0) t[j] = v_mul(a[j], rg);
        else if constexpr (ph == 1) t[j] = v_exp(t[j]);
        else if constexpr (ph == 2) t[j] = v_fma(t[j], sd, sd);
        else if constexpr (ph == 3) t[j] = v_rcp(t[j]);
        else if constexpr (ph == 4) t[j] = v_mul(t[j], a[8 + j]);
        else o[j] = v_cvt(t[2 * j], t[2 * j + 1]);
      };
      auto st_h = [&](u32x4 v, unsigned gl, int a) { st16<1>(v, vh, sc, WS_AF + ((rb * 8 + 2 * mw) * 68u + 2 * gl + cw) * 1024u + a * 512u); };
      auto epi = [&](auto K_) {
        constexpr int k = K_.v;
        if constexpr (k < 27) qop(IC<0>{}, K_);
        else if constexpr (TY == 1) { if constexpr (k < 81) qop(IC<k / 27>{}, IC<k % 27>{}); }
        else if constexpr (TY == 0) { if constexpr (k < 115) gop(acc[1 + (k - 27) / 44], hv[(k - 27) / 44], IC<(k - 27) % 44>{}); }
        else if constexpr (k < 29) {
          PIN(acc[k - 26]);
          st8<1>(u32x2{__float_as_uint(acc[k - 26][0] * 0.5f), tag}, sel(mw == 0, vl8, OOB), sc, C_CS + (rb * 10 + cw) * 512u + (k - 24) * 1024u);
        }
      };
      unsigned vf;
      sfor<0, 80>([&](auto M_) {
        constexpr int m = M_.v;
        constexpr auto KK = [](int q) { return q < 64 ? q >> 1 : 32 + (q & 7); };
        constexpr auto AA = [](int q) { return q < 64 ? q & 1 : (q >> 3) & 1; };
        constexpr int kk = KK(m), a = AA(m), g = kk >> 2;
        if constexpr (m == 14) {
          WAIT_LGKM0();
          asm volatile("s_waitcnt vmcnt(%1)" : "+v"(pf) : "i"(TY == 1 ? 7 : TY == 0 ? 6 : 8) : "memory");
          poll_rest(pf, true, tag, C_PF + wave * 4096u + ((vl8 & 248u) << 4), sc);
          bar();
          TS(14);
          st4s(tag, sel(vl8 == 0, (bid | wave) == 0 ? 0u : OOB, OOB), sc);
          SB();
        }
        if constexpr (m == 46) {
          WAIT_LGKM0();
          wait_vm<10>();
          st4s(tag, sel(vl8 == 0, C_FQ + fme, OOB), sc);
          bar();
          TS(15);
          {
            const int l = lane_id(), sec = (l + 2) >> 2, fr = sel(l < 2, ch * 8 + wave, ch * 8 - 8 + 2 * wave + ((l >> 1) & 1));
            const unsigned oc = sec * 80u + o0 + (l & 1) * 2u, jp = (oc >> 5) * 2 + ((oc & 24u) != 0);
            vf = sel((l < 10) & (fr >= 0), C_FQ + ((jp * 16 + (fr >> 2)) * 8 + (oc & 4u) + (fr & 3)) * 128u, OOB);
          }
          fq = ld4<1>(vf, sc, 0);
          gc2[0] = ld8<1>(vl8, sc, csb + 3072u); gc2[1] = ld8<1>(vl8, sc, csb + 4096u);
          SB();
        }
        if constexpr (m == 60) {
          asm volatile("s_waitcnt vmcnt(0)" : "+v"(fq), "+v"(gc2[0]), "+v"(gc2[1]) :: "memory");
          TS(16);
          poll_rest(fq, vf != OOB, tag, vf, sc);
          kv_issue();
          if (any_lane((gc2[0][1] != tag) | (gc2[1][1] != tag))) {
            for (int it = 0; it < SPIN; it++) {
              __builtin_amdgcn_s_sleep(1);
              gc2[0] = ld8<1>(vl8, sc, csb + 3072u); gc2[1] = ld8<1>(vl8, sc, csb + 4096u);
              asm volatile("s_waitcnt vmcnt(0)" : "+v"(gc2[0]), "+v"(gc2[1]), "+v"(Q1), "+v"(Q2) :: "memory");
              if (!any_lane((gc2[0][1] != tag) | (gc2[1][1] != tag))) break;
            }
          }
          TS(17);
          SB();
        }
        const u32x4 xf = (g == 2 || g == 3) ? xt[kk & 1] : X[(g == 2 || g == 3) ? 0 : KS(g) * 4 + (kk & 3)];
        if constexpr (m < 2) mfma32z(cat(w2[m & 3][0], w2[m & 3][1]), xf, ac2[a]);
        else mfma32a(cat(w2[m & 3][0], w2[m & 3][1]), xf, ac2[a]);
        if constexpr (m + 2 < 80) {
          constexpr int k2 = KK(m + 2), a2 = AA(m + 2), g2 = k2 >> 2;
          constexpr int gp = m < 62 ? (k2 - 1) >> 2 : m == 70 ? 7 : (k2 - 1) >> 2;
          if constexpr ((LG(g2) >> 16) != (LG(gp) >> 16)) { wr[a2] += (int)(LG(g2) & ~65535u) - (int)(LG(gp) & ~65535u); PIN(wr[a2]); }
          w2[(m + 2) & 3][0] = trd(wr[a2] + ((LG(g2) & 65535u) + (k2 & 3) * 2048));
          w2[(m + 2) & 3][1] = trd(wr[a2] + ((LG(g2) & 65535u) + (k2 & 3) * 2048 + 512));
          if constexpr (a2 == 0 && (g2 == 2 || g2 == 3)) xt[k2 & 1] = *(LAS u32x4*)(xa[k2 & 3] + (KS(g2) % 3) * 16384);
        }
        if constexpr (m < 2) prep(3 + m);
        if constexpr (m >= 2 && m < (SP ? 3 : 5)) mfma8a(As[m - 2], Bc, acc[m - 2]);
        if constexpr (m >= 5) sfor<(m - 5) * 5, cl((m - 4) * 5, 0, 115)>(epi);
        if constexpr (m == 14 || m == 15) { issue_g(m - 6, 0); issue_g(m - 6, 1); }
        if constexpr (m >= 24 && m < 44 && !(m & 1)) ld_w3((m - 24) / 10, (m - 24) / 10, ((m - 24) >> 1) % 5);
        if constexpr (m == 61 || m == 62) { const u32x2 a_ = split3(gc2[m - 61][0]); A4[m - 61] = u32x4{a_[0], a_[1], 0u, 0u}; PIN(A4[m - 61]); }
        if constexpr (m == 63) { A4[2] = u32x4{Bc[0], Bc[1], 0u, 0u}; PIN(A4[2]); }
        if constexpr (m == 64 || m == 65) mfma32a(A4[m - 64], A4[2], ac2[m - 64]);
        if constexpr (TY == 0 && (m == 63 || m == 64)) st_h(hv[m - 63], gl1, m - 63);
        if constexpr (m >= 66 && m < 76 && !(m & 1)) ld_w3(2, 2, (m - 66) >> 1);
        if constexpr (m >= 73) sfor<(m - 73) * 7, cl((m - 72) * 7, 0, 44)>([&](auto K_) { gop(ac2[0], hv[2], K_); });
        SB();
      });
      TS(18);
      st_h(hv[2], gl2, 0);
      asm volatile("s_nop 7" ::: "memory");
      sfor<0, 44>([&](auto K_) { gop(ac2[1], hv[3], K_); });
      st_h(hv[3], gl2, 1);
      u32x4 (&rw)[6][5] = RW; u32x4 &q1 = Q1, &q2 = Q2;
      asm volatile("s_waitcnt vmcnt(%12)" : "+v"(q1), "+v"(q2), "+v"(rw[0][0]), "+v"(rw[0][1]), "+v"(rw[0][2]), "+v"(rw[0][3]), "+v"(rw[0][4]),
                   "+v"(rw[1][0]), "+v"(rw[1][1]), "+v"(rw[1][2]), "+v"(rw[1][3]), "+v"(rw[1][4]) : "i"(TY == 0 ? 9 : 7) : "memory");
      TS(19);
      rest();
    };
    if constexpr (SP) round2(IC<2>{});
    else { if (tyB) round2(IC<1>{}); else round2(IC<0>{}); }
  };
  if (spec) phase1(IC<1>{}); else phase1(IC<0>{});
}

extern "C" void kernel_launch(void* const* d_in, const int* in_sizes, int n_in, void* d_out, int out_size, void* d_ws,
                              size_t ws_size, hipStream_t stream) {
  Params p;
  p.x = (const char*)d_in[0]; p.wqkv = (const char*)d_in[1]; p.wo = (const char*)d_in[2];
  p.wg = (const char*)d_in[4]; p.wu = (const char*)d_in[5]; p.wd = (const char*)d_in[6];
  p.out = (char*)d_out; p.ws = (char*)d_ws;
  (void)hipFuncSetAttribute((const void*)k_fused, hipFuncAttributeMaxDynamicSharedMemorySize, LDS_BYTES);
  k_fused<<<256, 512, LDS_BYTES, stream>>>(p);
}
```

```cpp
#include <hip/hip_runtime.h>

typedef int i32x4 __attribute__((ext_vector_type(4)));
typedef unsigned u32x4 __attribute__((ext_vector_type(4)));
typedef unsigned u32x2 __attribute__((ext_vector_type(2)));
typedef float f32x4 __attribute__((ext_vector_type(4)));
typedef float f32x2 __attribute__((ext_vector_type(2)));
typedef float f32x16 __attribute__((ext_vector_type(16)));
typedef __bf16 bf16x8 __attribute__((ext_vector_type(8)));
typedef __bf16 bf16x2 __attribute__((ext_vector_type(2)));
typedef short s16x4 __attribute__((ext_vector_type(4)));
#define LAS __attribute__((address_space(3)))
#define DEVI __device__ __forceinline__

constexpr int S_ = 2048, D_ = 640, F_ = 1536;
constexpr unsigned X_BYTES = S_ * D_ * 2, WQ_BYTES = D_ * 3 * D_ * 2, WF_BYTES = D_ * F_ * 2, WO_BYTES = D_ * D_ * 2;
constexpr unsigned QKV_BYTES = S_ * 3 * D_ * 2;
constexpr unsigned AF_BYTES = 128u * 68u * 1024u;
constexpr unsigned W3_BYTES = 68u * 40u * 1024u;
constexpr unsigned WS_QKV = 2u << 20, WS_AF = 16u << 20, WS_W3 = 32u << 20, WS_BYTES = 40u << 20;
constexpr unsigned C_PF = 0x8000, C_STAT = 0x10000, C_CS = 0x20000, C_FQ = 0x40000, C_FH = 0x80000, C_FO = 0xC0000, C_FE = 0x100000;
constexpr unsigned LDS_BYTES = 163840;
constexpr unsigned OOB = 0x80000000u;

DEVI i32x4 mk_srd(const void* p, unsigned n) {
  unsigned long long a = (unsigned long long)p;
  i32x4 r;
  r.x = __builtin_amdgcn_readfirstlane((int)(unsigned)a);
  r.y = __builtin_amdgcn_readfirstlane((int)(unsigned)(a >> 32));
  r.z = (int)n; r.w = 0x00020000;
  return r;
}
DEVI i32x4 sel_srd(bool c, i32x4 a, i32x4 b) { i32x4 r; r.x = c ? a.x : b.x; r.y = c ? a.y : b.y; r.z = c ? a.z : b.z; r.w = a.w; return r; }
template <int SC = 0> DEVI void dma16(unsigned ldsaddr, unsigned voff, i32x4 srd, unsigned soff) {
  if (SC) asm volatile("s_mov_b32 m0, %0\n\ts_nop 0\n\tbuffer_load_dwordx4 %1, %2, %3 offen sc1 lds"
                       :: "s"(ldsaddr), "v"(voff), "s"(srd), "s"(soff) : "memory");
  else    asm volatile("s_mov_b32 m0, %0\n\ts_nop 0\n\tbuffer_load_dwordx4 %1, %2, %3 offen lds"
                       :: "s"(ldsaddr), "v"(voff), "s"(srd), "s"(soff) : "memory");
}
template <int SC, int OFF = 0> DEVI u32x4 ld16(unsigned voff, i32x4 srd, unsigned soff) {
  u32x4 r;
  if (SC) asm volatile("buffer_load_dwordx4 %0, %1, %2, %3 offen offset:%4 sc1" : "=v"(r) : "v"(voff), "s"(srd), "s"(soff), "i"(OFF) : "memory");
  else    asm volatile("buffer_load_dwordx4 %0, %1, %2, %3 offen offset:%4" : "=v"(r) : "v"(voff), "s"(srd), "s"(soff), "i"(OFF) : "memory");
  return r;
}
template <int SC, int OFF = 0> DEVI u32x2 ld8(unsigned voff, i32x4 srd, unsigned soff) {
  u32x2 r;
  if (SC) asm volatile("buffer_load_dwordx2 %0, %1, %2, %3 offen offset:%4 sc1" : "=v"(r) : "v"(voff), "s"(srd), "s"(soff), "i"(OFF) : "memory");
  else    asm volatile("buffer_load_dwordx2 %0, %1, %2, %3 offen offset:%4" : "=v"(r) : "v"(voff), "s"(srd), "s"(soff), "i"(OFF) : "memory");
  return r;
}
template <int SC, int OFF = 0> DEVI unsigned ld4(unsigned voff, i32x4 srd, unsigned soff) {
  unsigned r;
  if (SC) asm volatile("buffer_load_dword %0, %1, %2, %3 offen offset:%4 sc1" : "=v"(r) : "v"(voff), "s"(srd), "s"(soff), "i"(OFF) : "memory");
  else    asm volatile("buffer_load_dword %0, %1, %2, %3 offen offset:%4" : "=v"(r) : "v"(voff), "s"(srd), "s"(soff), "i"(OFF) : "memory");
  return r;
}
template <int SC> DEVI void st16(u32x4 v, unsigned voff, i32x4 srd, unsigned soff) {
  if (SC) asm volatile("buffer_store_dwordx4 %0, %1, %2, %3 offen sc1\n\ts_nop 1" :: "v"(v), "v"(voff), "s"(srd), "s"(soff) : "memory");
  else    asm volatile("buffer_store_dwordx4 %0, %1, %2, %3 offen\n\ts_nop 1" :: "v"(v), "v"(voff), "s"(srd), "s"(soff) : "memory");
}
template <int SC, int OFF = 0> DEVI void st8(u32x2 v, unsigned voff, i32x4 srd, unsigned soff) {
  if (SC) asm volatile("buffer_store_dwordx2 %0, %1, %2, %3 offen offset:%4 sc1" :: "v"(v), "v"(voff), "s"(srd), "s"(soff), "i"(OFF) : "memory");
  else    asm volatile("buffer_store_dwordx2 %0, %1, %2, %3 offen offset:%4" :: "v"(v), "v"(voff), "s"(srd), "s"(soff), "i"(OFF) : "memory");
}
DEVI void st4s(unsigned v, unsigned voff, i32x4 srd) {
  asm volatile("buffer_store_dword %0, %1, %2, 0 offen sc1" :: "v"(v), "v"(voff), "s"(srd) : "memory");
}
template <int N> DEVI void wait_vm() { asm volatile("s_waitcnt vmcnt(%0)" :: "i"(N) : "memory"); }
#define WAIT_LGKM0() asm volatile("s_waitcnt lgkmcnt(0)" ::: "memory")
DEVI void bar() { asm volatile("s_barrier" ::: "memory"); }
#define SB() __builtin_amdgcn_sched_barrier(0)
#define PIN(x) asm volatile("" : "+v"(x))

DEVI unsigned pk2(float a, float b) { bf16x2 v = {(__bf16)a, (__bf16)b}; return __builtin_bit_cast(unsigned, v); }
DEVI float rbf(float a) { return (float)(__bf16)a; }
DEVI bf16x8 asbf(u32x4 v) { return __builtin_bit_cast(bf16x8, v); }
DEVI u32x2 trd(LAS char* p) { return __builtin_bit_cast(u32x2, __builtin_amdgcn_ds_read_tr16_b64_v4i16((LAS s16x4*)p)); }
DEVI u32x4 cat(u32x2 a, u32x2 b) { return __builtin_shufflevector(a, b, 0, 1, 2, 3); }
DEVI f32x4 mfma16(u32x4 a, u32x4 b, f32x4 c) { return __builtin_amdgcn_mfma_f32_16x16x32_bf16(asbf(a), asbf(b), c, 0, 0, 0); }
DEVI void mfma16a(u32x4 a, u32x4 b, f32x4& c) { asm volatile("v_mfma_f32_16x16x32_bf16 %0, %1, %2, %0" : "+v"(c) : "v"(a), "v"(b)); }
DEVI void mfma16z(u32x4 a, u32x4 b, f32x4& c) { asm volatile("v_mfma_f32_16x16x32_bf16 %0, %1, %2, 0" : "=&v"(c) : "v"(a), "v"(b)); }
DEVI void mfma32a(u32x4 a, u32x4 b, f32x16& c) { asm volatile("v_mfma_f32_32x32x16_bf16 %0, %1, %2, %0" : "+v"(c) : "v"(a), "v"(b)); }
DEVI void mfma32z(u32x4 a, u32x4 b, f32x16& c) { asm volatile("v_mfma_f32_32x32x16_bf16 %0, %1, %2, 0" : "=&v"(c) : "v"(a), "v"(b)); }
DEVI int lane_id() { int l; asm volatile("v_mbcnt_lo_u32_b32 %0, -1, 0\n\tv_mbcnt_hi_u32_b32 %0, -1, %0" : "=v"(l)); return l; }
DEVI f32x16 mfma32(u32x4 a, u32x4 b, f32x16 c) { return __builtin_amdgcn_mfma_f32_32x32x16_bf16(asbf(a), asbf(b), c, 0, 0, 0); }
DEVI float blo(unsigned r) { return __uint_as_float(r << 16); }
DEVI float bhi(unsigned r) { return __uint_as_float(r & 0xffff0000u); }
template <int C> DEVI float dppf(float v) { return __uint_as_float(__builtin_amdgcn_update_dpp(0, __float_as_uint(v), C, 0xf, 0xf, false)); }
DEVI float wave_sum(float v) {
  v += dppf<0x128>(v); v += dppf<0x124>(v); v += dppf<0x122>(v); v += dppf<0x121>(v);
  auto a = __builtin_amdgcn_permlane16_swap(__float_as_uint(v), __float_as_uint(v), false, false);
  v = __uint_as_float(a[0]) + __uint_as_float(a[1]);
  auto b = __builtin_amdgcn_permlane32_swap(__float_as_uint(v), __float_as_uint(v), false, false);
  return __uint_as_float(b[0]) + __uint_as_float(b[1]);
}
DEVI float v_fma(float a, float b, float c) { float r; asm volatile("v_fma_f32 %0, %1, %2, %3" : "=v"(r) : "v"(a), "v"(b), "v"(c)); return r; }
DEVI float v_mul(float a, float b) { float r; asm volatile("v_mul_f32 %0, %1, %2" : "=v"(r) : "v"(a), "v"(b)); return r; }
DEVI float v_exp(float a) { float r; asm volatile("v_exp_f32 %0, %1" : "=v"(r) : "v"(a)); return r; }
DEVI float v_rcp(float a) { float r; asm volatile("v_rcp_f32 %0, %1" : "=v"(r) : "v"(a)); return r; }
DEVI unsigned v_cvt(float a, float b) { unsigned r; asm volatile("v_cvt_pk_bf16_f32 %0, %1, %2" : "=v"(r) : "v"(a), "v"(b)); return r; }
DEVI float dot2(unsigned w, unsigned sel, float c) { return __builtin_amdgcn_fdot2_f32_bf16(__builtin_bit_cast(bf16x2, w), __builtin_bit_cast(bf16x2, sel), c, false); }
DEVI unsigned sel(bool c, unsigned a, unsigned b) { return c ? a : b; }
DEVI bool any_lane(bool c) { return __builtin_amdgcn_ballot_w64(c) != 0; }
constexpr int SPIN = 1 << 15;
DEVI void poll_rest(unsigned v, bool need, unsigned tag, unsigned voff, i32x4 srd) {
  if (any_lane(need & (v != tag))) {
    for (int it = 0; it < SPIN; it++) {
      __builtin_amdgcn_s_sleep(1);
      v = ld4<1>(voff, srd, 0);
      asm volatile("s_waitcnt vmcnt(0)" : "+v"(v) :: "memory");
      if (!any_lane(need & (v != tag))) break;
    }
  }
}

#define TS(i)
#define TSDECL
#define TSOUT()
template <int I> struct IC { static constexpr int v = I; };
template <int B, int E, class F> DEVI void sfor(F&& f) { if constexpr (B < E) { f(IC<B>{}); sfor<B + 1, E>(f); } }

struct Params {
  const char *x, *wqkv, *wo, *wg, *wu, *wd;
  char *out, *ws;
};

extern __shared__ __attribute__((aligned(16))) char smem_[];

constexpr unsigned UN = 8192;
constexpr unsigned LW(int s) { return s % 3 == 0 ? 0 : s % 3 == 1 ? 4 * UN : 7 * UN; }
constexpr unsigned L_PK = 3 * UN, L_X = 10 * UN, L_CSR = 16 * UN;
constexpr unsigned LG(int g) { return (g == 0 || g == 8 ? 16 : g == 1 || g == 9 ? 18 : g == 2 ? 4 : g == 3 ? 12 : g == 4 ? 6 : g == 5 ? 8 : g == 6 ? 0 : 2) * UN; }
constexpr int KS(int g) { return g < 2 ? g : g < 4 ? g + 6 : g - 2; }
constexpr unsigned AT_K = 4 * UN, AT_V = 8 * UN, R_A = 12 * UN;
constexpr int cl(int v, int lo, int hi) { return v < lo ? lo : v > hi ? hi : v; }

__global__ __launch_bounds__(512, 2) void k_fused(Params p) {
  TSDECL;
  LAS char* sm = (LAS char*)smem_;
  const unsigned lb = (unsigned)(size_t)sm;
  const int bid = blockIdx.x, xcd = bid & 7, loc = bid >> 3;
  const int wave = __builtin_amdgcn_readfirstlane(threadIdx.x >> 6);
  int lane = threadIdx.x & 63;
  const int rb = (xcd & 1) * 8 + (loc & 7), jg = (xcd >> 1) * 4 + (loc >> 3), pr = jg >> 1, tyB = jg & 1;
  const int mw = wave & 3, cw = wave >> 2;
  const bool spec = jg == 15, likeA = !tyB || spec;
  const i32x4 sx = mk_srd(p.x, X_BYTES), sq = mk_srd(p.wqkv, WQ_BYTES), sg = mk_srd(p.wg, WF_BYTES), su = mk_srd(p.wu, WF_BYTES);
  const int rot = (xcd & 1) * 5;
  auto sidx = [&](int s) { return (unsigned)(s < 5 ? s + rot : s - rot); };
  const unsigned drow = 8 * wave + (lane >> 3), wcol = ((lane & 7) ^ ((lane >> 2) & 4)) * 16u;
  const unsigned vx = (rb * 128 + drow) * 1280u + (((lane & 7) ^ (((wave & 1) << 2) | (lane >> 4))) * 16u);
  const unsigned vq = drow * 3840u + wcol, vg = drow * 3072u + wcol, vu = vg ^ 32u;
  const unsigned v1 = likeA ? vg : vq, v2 = likeA ? vu : vq;
  const i32x4 s1 = sel_srd(likeA, sg, sq), s2 = sel_srd(likeA, su, sq);
  const unsigned gl1 = spec ? 3 * (rb >> 1) + 1 + (rb & 1) : 3 * pr, gl2 = 3 * pr + 1 + tyB;
  const unsigned c0 = (4 * pr + tyB) * 128u, c1 = likeA ? gl1 * 128u : c0 + 128u, c2 = likeA ? c1 : c0 + 256u;
  const unsigned st1 = likeA ? 196608u : 245760u;
  const unsigned ldw = lb + wave * 1024u;
  auto issue = [&](int s, int b) {
    if (b < 2) dma16(ldw + L_X + (s % 3) * 16384u + b * UN, vx, sx, b * 81920u + sidx(s) * 128u);
    else if (b == 2) dma16(ldw + LW(s), vq, sq, sidx(s) * 245760u + c0);
    else if (b == 3) dma16(ldw + LW(s) + UN, v1, s1, sidx(s) * st1 + c1);
    else dma16(ldw + LW(s) + 2 * UN, v2, s2, sidx(s) * st1 + c2);
  };
  auto issue_g = [&](int g, int b) { dma16(ldw + LG(g) + b * UN, b ? vu : vg, b ? su : sg, sidx(KS(g)) * 196608u + gl2 * 128u); };
#pragma unroll
  for (int b = 0; b < 5; b++) issue(0, b);
  const i32x4 sc = mk_srd(p.ws, WS_BYTES);
  const unsigned srow = rb * 128 + jg * 8 + wave;
  unsigned vl = lane * 16u;
  unsigned ep = ld4<1>(0u, sc, 0);
  u32x4 r0 = ld16<0>(vl, sx, srow * 1280u);
  unsigned r1 = ld4<0, 1024>(lane * 4u, sx, srow * 1280u);
  bar();
#pragma unroll
  for (int b = 0; b < 10; b++) issue(1 + b / 5, b % 5);
  TS(1);
  const i32x4 sd = mk_srd(p.wd, WF_BYTES), so = mk_srd(p.wo, WO_BYTES);
  const unsigned kA = bid * 205u >> 10, cA = bid - kA * 5u, kB = (bid + 256u) * 205u >> 10, cB = bid + 256u - kB * 5u;
  const unsigned offB = bid < 84 ? 0u : OOB;
  const unsigned vpk = (4 * wave + (lane >> 4)) * 1280u + (lane & 15) * 16u;
  dma16(ldw + L_PK, vpk, sel_srd(kA < 48, sd, so), (kA < 48 ? kA : kA - 48) * 40960u + cA * 256u);
  LAS char* pkr = sm + (L_PK + (8 * (lane >> 4) + ((lane & 15) >> 2)) * 256u + wave * 32u + (lane & 3) * 8u);
  int hi = lane >> 5;
  LAS char* xa[4];
#pragma unroll
  for (int ks = 0; ks < 4; ks++) { xa[ks] = sm + (L_X + (32 * mw + (lane & 31)) * 128u + (((2 * ks + hi) ^ ((lane >> 1) & 7)) * 16u)); PIN(xa[ks]); }
  LAS char *wa[3], *wr[2];
  {
    const unsigned ch = (lane >> 4) & 1, r = (lane >> 2) & 3, c = lane & 3;
    const unsigned wl = hi * 1024u + r * 128u + ((cw ^ (r >> 1)) * 64u) + (((c & 1) << 1 | c >> 1) * 8u);
    const unsigned q = wl + ch * 32u, g0 = wl + ch * (UN + 32u), g1 = wl + ch * UN + (ch ^ 1) * 32u;
    wa[0] = sm + q; wa[1] = sm + (UN + (likeA ? g0 : q)); wa[2] = sm + (likeA ? UN + g1 : 2 * UN + q);
    wr[0] = sm + (g0 + LG(0)); wr[1] = sm + (g1 + LG(0));
    PIN(wa[0]); PIN(wa[1]); PIN(wa[2]); PIN(wr[0]); PIN(wr[1]);
  }
  const int cst = loc & 7;
  LAS char* wc = (cst == 1 ? wa[1] : cst == 2 ? wa[2] : wa[0]) + mw * 2048;
  PIN(wc);
  const unsigned csb = C_CS + (jg * 10 + cw) * 512u;
  const u32x4 ones = {0x3f803f80u, 0x3f803f80u, 0x3f803f80u, 0x3f803f80u};
  const unsigned Rq = rb * 4 + mw;
  const unsigned fme = ((jg * 16 + rb) * 8 + wave) * 128u;
  const int head = xcd * 4 + (loc & 3), ch = loc >> 2, par = head & 1, o0 = 5 * head >> 1;
  const unsigned w3b = WS_W3 + (wave * 240 + ch * 5) * 1024u;
  unsigned w3c = 0;
  auto w3a = [&](int j, int u) { return j < 6 ? w3b + j * 40960u + u * 1024u : w3c + (j - 6) * 327680u + u * 1024u; };
  u32x4 RW[6][5], Q1, Q2;
  unsigned tag = 0;
  auto ld_w3 = [&](int buf, int j, int u) { RW[buf][u] = ld16<1>(vl, sc, w3a(j, u)); };
  auto kv_issue = [&]() {
    const int l = lane_id(), oi = ((l >> 2) & 3) ^ (l >> 4), R = ch * 8 - 8 + 2 * wave;
    const unsigned v = sel(oi < (R >= 0 ? 3 : 0), oi * 512u + ((l >> 4) * 4 + (l & 3)) * 16u, OOB);
    const unsigned b = WS_QKV + (unsigned)((R >= 0 ? R : 0) * 240 + 80 + o0) * 512u;
#pragma unroll
    for (int i = 0; i < 8; i++)
      dma16<1>(lb + AT_K + (i >> 2) * 32768u + wave * 4096u + (i & 3) * 1024u, v, sc, b + (i >> 2) * 40960u + ((i >> 1) & 1) * 122880u + (i & 1) * 256u);
  };
  auto q_issue = [&]() {
    const unsigned qb = WS_QKV + (unsigned)((ch * 8 + wave) * 240 + o0) * 512u;
    Q1 = ld16<1>(vl, sc, qb);
    Q2 = ld16<1, 1024>(sel(vl < 512u, vl, OOB), sc, qb);
  };

#define PINA(n) "+v"(A[n][0]), "+v"(A[n][1]), "+v"(A[n][2]), "+v"(A[n][3])
#define PINW(n) "+v"(RW[n][0]), "+v"(RW[n][1]), "+v"(RW[n][2]), "+v"(RW[n][3]), "+v"(RW[n][4])
  auto rest = [&]() __attribute__((always_inline)) {
  lane = lane_id(); hi = lane >> 5; vl = lane * 16u;
  const int q32 = lane & 31;
  int bid2 = bid;
  asm volatile("" : "+s"(bid2));
  const int rt = (bid2 & 7) * 4 + ((bid2 >> 3) & 3), cg = bid2 >> 5;
  w3c = WS_W3 + ((48 + wave) * 40 + cg * 5) * 1024u;
  const unsigned ab = WS_AF + (rt * 4 * 68 + wave * 6) * 1024u, ac = WS_AF + (rt * 4 * 68 + 48 + wave) * 1024u;
  const unsigned r7 = wave < 4 ? R_A + 40960u + wave * 5120u : (wave - 4) * 5120u;
  u32x4 A[4][4];
  auto ld_a = [&](int buf, int j, int i, unsigned v) { A[buf][i] = ld16<1>(v, sc, (j < 6 ? ab + j * 1024u : ac + (j - 6) * 8192u) + i * 69632u); };
  auto dma_w3 = [&](unsigned dst, int j, int u) { dma16<1>(lb + dst + u * 1024u, vl, sc, w3a(j, u)); };
  f32x16 o;
  {
    LAS char* cw_ = sm + (AT_K + wave * 4096 + hi * 2048 + (q32 >> 2) * 256 + ((3 ^ ((q32 >> 2) & 3)) * 64) + (q32 & 3) * 16);
    *(LAS u32x4*)cw_ = u32x4{(ch == 0 && wave < 4) ? 0x3f80fe00u : 0x3f800000u, 0u, 0u, 0u};
    *(LAS u32x4*)(cw_ + 32768) = ones;
    {
      const bool z1 = par & !hi;
      Q1[0] = sel(z1, 0u, Q1[0]); Q1[1] = sel(z1, 0u, Q1[1]);
      Q2[2] = sel(par, Q2[2], 0u); Q2[3] = sel(par, Q2[3], 0u);
      Q2[0] = sel(hi, 0x00003f80u, Q2[0]);
    }
    WAIT_LGKM0();
    bar();
    TS(20);
#pragma unroll
    for (int u = 0; u < 5; u++) ld_w3(3, 3, u);
    LAS char* kp = sm + (AT_K + wave * 2048 + (q32 >> 2) * 256 + ((hi ^ ((q32 >> 2) & 3)) * 64) + (q32 & 3) * 16);
    LAS char* kp1 = sm + (((unsigned)(size_t)kp - lb) ^ 128u);
    LAS char* vp[2];
    {
      const int mq = 4 * ((lane >> 4) & 1) + (lane & 3), iq = sel(mq < 4, 2 * par + ((mq & 1) << 1 | mq >> 1), sel(mq == 4, par ? 1 : 4, 6));
#pragma unroll
      for (int h = 0; h < 2; h++) vp[h] = sm + (AT_V + wave * 2048 + hi * 256 + h * 512 + (((iq >> 1) ^ (2 * h + hi)) * 64) + ((lane >> 2) & 3) * 16 + (iq & 1) * 8);
    }
    f32x16 z;
#pragma unroll
    for (int r = 0; r < 16; r++) z[r] = 0.f;
    auto scq = [&](int off, u32x4 q2) {
      const u32x4 a = *(LAS u32x4*)(kp + off), b = *(LAS u32x4*)(kp1 + off);
      return mfma32(b, q2, mfma32(a, Q1, z));
    };
    auto pv = [&](int off, const unsigned* pk_) {
      const u32x4 V1 = cat(trd(vp[0] + off), trd(vp[1] + off)), V2 = cat(trd(vp[0] + off + 1024), trd(vp[1] + off + 1024));
      o = mfma32(V1, u32x4{pk_[0], pk_[1], pk_[2], pk_[3]}, o);
      o = mfma32(V2, u32x4{pk_[4], pk_[5], pk_[6], pk_[7]}, o);
    };
    const int dl = q32 - 4 * hi;
    auto xmax = [&](float m) {
      auto r = __builtin_amdgcn_permlane32_swap(__float_as_uint(m), __float_as_uint(m), false, false);
      return __builtin_fmaxf(__uint_as_float(r[0]), __uint_as_float(r[1]));
    };
    auto edge = [&](const f32x16& ee, float m) {
      unsigned pa[8], pb[8];
#pragma unroll
      for (int r = 0; r < 16; r += 2) {
        const bool b0 = (r & 3) + 8 * (r >> 2) <= dl, b1 = ((r + 1) & 3) + 8 * (r >> 2) <= dl;
        const unsigned x = pk2(__builtin_amdgcn_exp2f(ee[r] - m), __builtin_amdgcn_exp2f(ee[r + 1] - m));
        const unsigned mk = (b0 ? 0xffffu : 0u) | (b1 ? 0xffff0000u : 0u);
        pb[r >> 1] = x & mk; pa[r >> 1] = x & ~mk;
      }
      pv(0, pa); pv(8 * 2048, pb);
    };
    auto merged = [&]() {
      const f32x16 sa = scq(0, Q2), sb = scq(8 * 2048, Q2);
      f32x16 ee;
#pragma unroll
      for (int r = 0; r < 16; r++) ee[r] = ((r & 3) + 8 * (r >> 2) <= dl) ? sb[r] : sa[r];
      return ee;
    };
    auto vmax = [&](const f32x16& ee, float m) {
#pragma unroll
      for (int r = 0; r < 16; r += 2) m = __builtin_fmaxf(__builtin_fmaxf(m, ee[r]), ee[r + 1]);
      return m;
    };
    const unsigned fpo = (((2 * wave + (lane >> 2)) * 16 + (rt >> 1)) * 8 + (lane & 2) * 2 + (rt & 1) * 2 + (lane & 1)) * 128u;
    const unsigned vfe = sel(lane < 4, C_FE + fpo, OOB), vfh = sel(lane < 8, C_FH + fpo, OOB);
    unsigned hf;
    o = z;
    {
      const f32x16 ee = merged();
      const unsigned mb = pk2(xmax(vmax(ee, -3.0e38f)), 0.f) << 16;
      const float m = __uint_as_float(mb);
      const u32x4 Q3 = {sel(hi, (mb ^ 0x80000000u) | 0x3f80u, Q2[0]), Q2[1], Q2[2], Q2[3]};
      f32x16 sn = scq(2048, Q3);
      edge(ee, m);
      sfor<1, 8>([&](auto J) {
        constexpr int j = J.v;
        const f32x16 s = sn;
        if constexpr (j < 7) sn = scq((j + 1) * 2048, Q3);
        if constexpr (j == 1) { hf = ld4<1>(vfe, sc, 0); ld_w3(4, 4, 0); ld_w3(4, 4, 1); ld_w3(4, 4, 2); ld_w3(4, 4, 3); }
        if constexpr (j == 2) { ld_w3(4, 4, 4); dma_w3(r7, 7, 0); dma_w3(r7, 7, 1); dma_w3(r7, 7, 2); }
        if constexpr (j == 3) {
          dma_w3(r7, 7, 3); dma_w3(r7, 7, 4);
          asm volatile("s_waitcnt vmcnt(10)" : "+v"(hf) :: "memory");
          st4s(tag, sel(lane == 0, C_FH + fme, OOB), sc);
          TS(21);
          poll_rest(hf, lane < 4, tag, vfe, sc);
          TS(22);
        }
        if constexpr (j == 3 || j == 4) { ld_a(j - 3, j - 3, 0, vl); ld_a(j - 3, j - 3, 1, vl); ld_a(j - 3, j - 3, 2, vl); ld_a(j - 3, j - 3, 3, vl); }
        if constexpr (j == 5) { dma_w3(R_A + wave * 5120u, 6, 0); dma_w3(R_A + wave * 5120u, 6, 1); dma_w3(R_A + wave * 5120u, 6, 2); }
        if constexpr (j == 6) { dma_w3(R_A + wave * 5120u, 6, 3); dma_w3(R_A + wave * 5120u, 6, 4); }
        if constexpr (j == 7) hf = ld4<1>(vfh, sc, 0);
        unsigned pk_[8];
#pragma unroll
        for (int r = 0; r < 16; r += 2) pk_[r >> 1] = pk2(__builtin_amdgcn_exp2f(s[r]), __builtin_amdgcn_exp2f(s[r + 1]));
        pv(j * 2048, pk_);
      });
    }
    {
      float mx = 0.f;
#pragma unroll
      for (int r = 0; r < 12; r += 2) mx = __builtin_fmaxf(__builtin_fmaxf(mx, __builtin_fabsf(o[r])), __builtin_fabsf(o[r + 1]));
      if (any_lane(!((o[12] >= 5.9604645e-8f) & (o[12] <= 16777216.f) & (mx < 3.0e38f)))) {
        float m = vmax(merged(), -3.0e38f);
#pragma nounroll
        for (int j = 1; j < 8; j++) m = vmax(scq(j * 2048, Q2), m);
        m = xmax(m);
        o = z;
        edge(merged(), m);
#pragma nounroll
        for (int j = 1; j < 8; j++) {
          const f32x16 s = scq(j * 2048, Q2);
          unsigned pk_[8];
#pragma unroll
          for (int r = 0; r < 16; r += 2) pk_[r >> 1] = pk2(__builtin_amdgcn_exp2f(s[r] - m), __builtin_amdgcn_exp2f(s[r + 1] - m));
          pv(j * 2048, pk_);
        }
      }
    }
    TS(23);
    const float inv = __builtin_amdgcn_rcpf(o[12]);
    const unsigned tb_ = (unsigned)((cg * 8 + wave) * 2 + (q32 >> 4)) * 68u + 48u;
    const unsigned oa = o0 + par + hi, ob = o0 + (par ? 0 : 2);
    st16<1>(u32x4{pk2(o[0] * inv, o[1] * inv), pk2(o[2] * inv, o[3] * inv), pk2(o[4] * inv, o[5] * inv), pk2(o[6] * inv, o[7] * inv)},
            (tb_ + (oa >> 2)) * 1024u + (oa & 3) * 256u + (q32 & 15) * 16u, sc, WS_AF);
    st8<1>(u32x2{pk2(o[8] * inv, o[9] * inv), pk2(o[10] * inv, o[11] * inv)},
           sel(hi, OOB, (tb_ + (ob >> 2)) * 1024u + (ob & 3) * 256u + (q32 & 15) * 16u + par * 8u), sc, WS_AF);
    asm volatile("s_waitcnt vmcnt(2)" : "+v"(hf), PINA(0), PINA(1), PINW(2), PINW(3), PINW(4) :: "memory");
    poll_rest(hf, lane < 8, tag, vfh, sc);
#pragma unroll
    for (int i = 0; i < 4; i++) ld_a(2, 2, i, vl);
  }
  const unsigned vl8 = sel(wave < 4, vl, OOB);
  unsigned vfo, vo[3], lo_[3], lo2[3];
  f32x4 fa[4][5];
  unsigned of = 0;
  u32x2 xr[3];
  TS(24);
  auto step = [&](auto J_) {
    constexpr int j = J_.v, wb = j < 6 ? j : j == 6 ? 1 : j == 7 ? 2 : 0, abuf = j & 3;
    if constexpr (j == 2) {
      asm volatile("s_waitcnt vmcnt(9)" : PINA(2) :: "memory");
      st4s(tag, sel(vl == 0, C_FO + ((cg * 8 + wave) * 32u + rt) * 128u, OOB), sc);
      TS(25);
    }
    if constexpr (j == 3) asm volatile("s_waitcnt vmcnt(15)" : PINA(3) :: "memory");
    if constexpr (j == 4) {
      asm volatile("s_waitcnt vmcnt(0)" : PINA(0), PINA(1), "+v"(of), PINW(0), PINW(5) :: "memory");
      TS(26);
      poll_rest(of, vfo != OOB, tag, vfo, sc);
      TS(27);
    }
    if constexpr (j == 6) asm volatile("s_waitcnt vmcnt(8)" : PINA(2) :: "memory");
    if constexpr (j == 7) asm volatile("s_waitcnt vmcnt(7)" : PINA(3) :: "memory");
    SB();
    sfor<0, 20>([&](auto K_) {
      constexpr int k = K_.v, u = k >> 2, i = k & 3;
      if constexpr (j == 0) mfma16z(RW[wb][u], A[abuf][i], fa[i][u]);
      else mfma16a(RW[wb][u], A[abuf][i], fa[i][u]);
      if constexpr (j >= 1 && j <= 3 && k < 4) ld_a((j + 2) & 3, j + 2, k, vl);
      if constexpr (j == 1 && k >= 4 && k < 9) ld_w3(5, 5, k - 4);
      if constexpr (j == 2 && k >= 4 && k < 9) RW[0][k - 4] = ld16<1>(vl8, sc, w3a(8, k - 4));
      if constexpr (j == 3 && k == 5) {
        const int l = lane_id(), h5 = 5 * (l & 31);
        auto ov = [&](int q) { return (h5 + 5 > 8 * q) & (h5 < 8 * q + 8); };
        vfo = sel(ov(wave) | ov(wave + 8) | ov(wave + 16), C_FO + (rt * 64u + l) * 128u, OOB);
        PIN(vfo);
      }
      if constexpr (j == 3 && k == 6) of = ld4<1>(vfo, sc, 0);
      if constexpr (j == 4 && k < 8) ld_a(2 + (k >> 2), 6 + (k >> 2), k & 3, vl);
      if constexpr (j == 5 && k < 4) ld_a(0, 8, k, vl8);
      if constexpr (j == 5 && k >= 8 && k < 13) RW[1][k - 8] = *(LAS u32x4*)(sm + (R_A + wave * 5120 + vl) + (k - 8) * 1024);
      if constexpr (j == 5 && k >= 13 && k < 18) RW[2][k - 13] = *(LAS u32x4*)(sm + (r7 + vl) + (k - 13) * 1024);
      if constexpr (j == 6 && k >= 2 && k < 14 && (k & 3) == 2) {
        constexpr int q = k >> 2;
        const unsigned c = ((wave < 4 ? 3 * wave : 4 + 2 * wave) + q) * 64u + lane_id(), row = c * 3277u >> 16, pos = c - row * 20u;
        vo[q] = sel(q == 2 && wave >= 4, OOB, (rt * 64u + row) * 1280u + cg * 160u + pos * 8u);
        const unsigned in = (row & 31) * 160u + ((pos ^ ((row >> 2) & 3)) * 8u);
        lo_[q] = R_A + (row >> 5) * 40960u + in;
        lo2[q] = sel(row >> 5, 0u, R_A + 20480u) + in;
        PIN(lo_[q]); PIN(lo2[q]);
        xr[q] = ld8<0>(vo[q], sx, 0);
      }
      SB();
    });
  };
  sfor<0, 8>(step);
  asm volatile("s_waitcnt vmcnt(0)" : PINA(0), "+v"(xr[0]), "+v"(xr[1]), "+v"(xr[2]) :: "memory");
  if (wave < 4) step(IC<8>{});
  asm volatile("s_nop 7" ::: "memory");
  SB();
  TS(28);
  lane = lane_id();
  const int t16 = lane & 15, g = lane >> 4;
  {
    const unsigned pin = t16 * 160 + ((g ^ (t16 >> 2)) * 8);
    LAS char* pw0 = sm + (R_A + wave * 5120 + pin);
    LAS char* pw1 = sm + (r7 + pin);
#pragma unroll
    for (int i = 0; i < 4; i++)
#pragma unroll
      for (int u = 0; u < 5; u++)
        *(LAS u32x2*)((i >> 1 ? pw1 : pw0) + ((i & 1) * 2560 + u * 32)) = u32x2{pk2(fa[i][u][0], fa[i][u][1]), pk2(fa[i][u][2], fa[i][u][3])};
    WAIT_LGKM0();
    bar();
    const i32x4 sout = mk_srd(p.out, X_BYTES);
    unsigned slo = 0x00003f80u, shi = 0x3f800000u;
    asm volatile("" : "+s"(slo), "+s"(shi));
    auto fin = [&](int q) {
      float a0 = blo(xr[q][0]), a1 = bhi(xr[q][0]), a2 = blo(xr[q][1]), a3 = bhi(xr[q][1]);
#pragma unroll
      for (int w = 0; w < 8; w++) {
        const u32x2 v = *(LAS u32x2*)(sm + (w < 4 ? lo_[q] : lo2[q]) + (w & 3) * 5120);
        a0 = dot2(v[0], slo, a0); a1 = dot2(v[0], shi, a1); a2 = dot2(v[1], slo, a2); a3 = dot2(v[1], shi, a3);
      }
      st8<0>(u32x2{pk2(a0, a1), pk2(a2, a3)}, vo[q], sout, 0);
    };
    fin(0); fin(1);
    if (wave < 4) fin(2);
  }
  TSOUT();
  __builtin_amdgcn_endpgm();
  };

  auto phase1 = [&](auto SP_) {
    constexpr int SP = SP_.v;
    u32x4 X[32], xt[2];
    f32x16 acc[3], cs;
    u32x2 wf[2][3][2], w2[4][2], pk[2], gs, gc[3];
    u32x4 As, Bc;
    float sd, rs, rg;
    unsigned pf, nm;
    float e[10], sacc = 0.f, mean = 0.f, cp[4];
    auto stat = [&](int k) {
      if (k < 5) { const unsigned w = k < 4 ? r0[k & 3] : r1; e[2 * k] = blo(w); e[2 * k + 1] = bhi(w); sacc += e[2 * k] + e[2 * k + 1]; }
      else if (k >= 11 && k < 16) {
        const float d0 = e[2 * k - 22] - mean, d1 = e[2 * k - 21] - mean;
        sacc = __builtin_fmaf(d0, d0, sacc); PIN(sacc); sacc = __builtin_fmaf(d1, d1, sacc);
      } else if (k < 22) {
        const int q = k < 11 ? k - 5 : k - 16;
        if (q == 0) sacc += dppf<0x128>(sacc);
        else if (q == 1) sacc += dppf<0x124>(sacc);
        else if (q == 2) sacc += dppf<0x122>(sacc);
        else if (q == 3) sacc += dppf<0x121>(sacc);
        else if (q == 4) { auto a = __builtin_amdgcn_permlane16_swap(__float_as_uint(sacc), __float_as_uint(sacc), false, false); sacc = __uint_as_float(a[0]) + __uint_as_float(a[1]); }
        else {
          auto a = __builtin_amdgcn_permlane32_swap(__float_as_uint(sacc), __float_as_uint(sacc), false, false);
          sacc = (__uint_as_float(a[0]) + __uint_as_float(a[1])) * (1.0f / 640.0f);
          if (k == 10) { mean = sacc; sacc = 0.f; PIN(mean); }
        }
      } else if (k == 22) sacc = __builtin_amdgcn_sqrtf(rbf(rbf(sacc) + 1e-5f));
      PIN(sacc);
    };
    auto rdw = [&](int n, int t) {
      wf[n & 1][t][0] = trd(wa[t] + (LW(n >> 2) + (n & 3) * 2048));
      wf[n & 1][t][1] = trd(wa[t] + (LW(n >> 2) + (n & 3) * 2048 + 512));
    };
    auto rdx = [&](int n) {
      const u32x4 v = *(LAS u32x4*)(xa[n & 3] + ((n >> 2) % 3) * 16384);
      if (n < 32) X[n] = v; else xt[n & 1] = v;
    };
    auto rdc = [&](int s) { pk[0] = trd(wc + LW(s)); pk[1] = trd(wc + (LW(s) + 512)); };
    auto wfr = [&](int n, int t) { return cat(wf[n & 1][t][0], wf[n & 1][t][1]); };
    const unsigned vl8 = lane * 8u;
    auto ld_gran = [&](int k) {
      if (k == 0) gs = ld8<1>((vl8 & 56u) | (vl8 & 192u) << 1, sc, C_STAT + Rq * 512u);
      else if (k < 4) gc[k - 1] = ld8<1>(vl8, sc, csb + (k - 1) * 1024u);
      else pf = ld4<1>((vl8 & 248u) << 4, sc, C_PF + wave * 4096u);
    };
    auto split3 = [&](unsigned g) {
      const float c = __uint_as_float(g), r1_ = c - rbf(c);
      const unsigned a0 = pk2(c, r1_);
      u32x4 a = {a0, pk2(r1_ - bhi(a0), 0.f), 0u, 0u};
      PIN(a);
      return a;
    };
    auto mk_bc = [&]() { u32x4 b = {nm | nm << 16, nm, 0u, 0u}; PIN(b); return b; };
    auto prep = [&](int k) {
      if (k == 0) {
        if (any_lane((gs[1] != tag) | (gc[0][1] != tag) | (gc[1][1] != tag) | (gc[2][1] != tag))) {
          for (int it = 0; it < SPIN; it++) {
            __builtin_amdgcn_s_sleep(1);
#pragma unroll
            for (int i = 0; i < 4; i++) ld_gran(i);
            asm volatile("s_waitcnt vmcnt(0)" : "+v"(gs), "+v"(gc[0]), "+v"(gc[1]), "+v"(gc[2]) :: "memory");
            if (!any_lane((gs[1] != tag) | (gc[0][1] != tag) | (gc[1][1] != tag) | (gc[2][1] != tag))) break;
          }
        }
      } else if (k == 1) {
        nm = (gs[0] & 0xffffu) ^ 0x8000u; PIN(nm);
        Bc = mk_bc();
        sd = bhi(gs[0]); PIN(sd); rs = __builtin_amdgcn_rcpf(sd); PIN(rs);
      } else if (k == 8) { rg = -1.44269504089f * rs; PIN(rg); }
      else if (k < (SP ? 4 : 8)) { if (k & 1) mfma32a(As, Bc, acc[(k - 3) >> 1]); else As = split3(gc[(k - 2) >> 1][0]); }
    };
#pragma unroll
    for (int r = 0; r < 16; r++) cs[r] = 0.f;
    PIN(cs);
    u32x4 on1 = ones;
    PIN(on1);
    wait_vm<14>();
    bar();
    TS(2);
    rdx(0);
#pragma unroll
    for (int t = 0; t < 3; t++) rdw(0, t);
    rdx(1);
#pragma unroll
    for (int t = 0; t < 3; t++) rdw(1, t);
    rdc(0);
    SB();
    sfor<0, 120>([&](auto I_) {
      constexpr int n = I_.v / 3, t = I_.v % 3, s = n >> 2, ks = n & 3;
      constexpr int sb = (I_.v + 4) / 12 - 1, m = (I_.v + 4) % 12;
      const u32x4 xf = n < 32 ? X[n < 32 ? n : 0] : xt[n & 1];
      if constexpr (SP && t > 0) { if constexpr (n == 0) mfma32z(on1, wfr(n, t), acc[t]); else mfma32a(on1, wfr(n, t), acc[t]); }
      else { if constexpr (n == 0) mfma32z(wfr(n, t), xf, acc[t]); else mfma32a(wfr(n, t), xf, acc[t]); }
      if constexpr (m == 11) {
        SB();
        WAIT_LGKM0();
        if constexpr (s == 0) { asm volatile("s_waitcnt vmcnt(6)" : "+v"(ep), "+v"(r0), "+v"(r1) :: "memory"); tag = __builtin_amdgcn_readfirstlane(ep) + 1u; }
        else if constexpr (s == 1) wait_vm<5>();
        else if constexpr (s == 2) wait_vm<6>();
        else if constexpr (s == 3) wait_vm<7>();
        else if constexpr (s == 4) wait_vm<6>();
        else if constexpr (s == 5) wait_vm<7>();
        else if constexpr (s == 6) wait_vm<5>();
        else if constexpr (s == 7) wait_vm<12>();
        else if constexpr (s == 8) asm volatile("s_waitcnt vmcnt(6)" : "+v"(gs), "+v"(gc[0]), "+v"(gc[1]), "+v"(gc[2]) :: "memory");
        else wait_vm<4>();
        bar();
        TS(3 + s);
        SB();
        if constexpr (n < 38) { rdw(n + 2, 0); rdw(n + 2, 1); }
      } else if constexpr (n < 38) {
        if constexpr (m != 10) rdw(n + 2, t);
        if constexpr (t == 2) rdx(n + 2);
      } else if constexpr (n == 39) { w2[t][0] = trd(wr[t & 1] + (t >> 1) * 2048); w2[t][1] = trd(wr[t & 1] + ((t >> 1) * 2048 + 512)); }
      if constexpr (sb >= 0 && sb < 4 && m == 0) rdc(sb + 1);
      if constexpr ((sb == -1 && m == 6) || (sb >= 0 && sb < 4 && m == 3))
        asm volatile("s_cmp_gt_u32 %3, 2\n\ts_cbranch_scc1 .Lce%=\n\tv_mfma_f32_32x32x16_bf16 %0, %1, %2, %0\n.Lce%=:"
                     : "+v"(cs) : "v"(on1), "v"(cat(pk[0], pk[1])), "s"(cst) : "scc");
      if constexpr (sb == 3 && m == 8) { PIN(cs); *(LAS float*)(sm + L_CSR + wave * 128 + (lane & 31) * 4) = cs[0]; }
      if constexpr (sb == 4 && m < 4) cp[m] = *(LAS float*)(sm + L_CSR + cw * 512 + m * 128 + (lane & 31) * 4);
      if constexpr (sb == 4 && m == 4)
        st8<1>(u32x2{__float_as_uint((cp[0] + cp[1]) + (cp[2] + cp[3])), tag}, sel(vl8 < (cst < 3 && mw == 0 ? 256u : 0u), vl8, OOB), sc, csb + cst * 1024u + (xcd & 1) * 256u);
      if constexpr (sb == 0 || sb == 1) stat(sb * 12 + m);
      if constexpr (sb == 2 && m == 1) *(LAS unsigned*)(sm + L_CSR + 2048 + wave * 256 + lane * 4) = pk2(mean, sacc);
      if constexpr (sb == 3 && m == 1) pf = *(LAS unsigned*)(sm + L_CSR + 2048 + (lane & 7) * 256);
      if constexpr (sb == 3 && m == 4) st8<1>(u32x2{pf, tag}, sel(vl8 < (wave == 0 ? 64u : 0u), vl8, OOB), sc, C_STAT + (rb * 16 + jg) * 128u);
      if constexpr ((sb == 1 || sb == 4) && (m == 5 || m == 6)) pk[m - 5] = trd(pkr + (m - 5) * 1024);
      if constexpr (sb == 1 && m == 10) st16<1>(cat(pk[0], pk[1]), vl, sc, WS_W3 + (kA * 40u + cA * 8u + wave) * 1024u);
      if constexpr (sb == 4 && m == 10) st16<1>(cat(pk[0], pk[1]), vl | offB, sc, WS_W3 + (kB * 40u + cB * 8u + wave) * 1024u);
      if constexpr (sb == 2 && m == 10) dma16(ldw + L_PK, vpk | offB, mk_srd(p.wo, WO_BYTES), (kB - 48) * 40960u + cB * 256u);
      if constexpr (sb == 6 && m == 0) st4s(tag, sel(vl8 == 0, wave == 0 ? C_PF + bid * 128u : OOB, OOB), sc);
      if constexpr (sb >= 0 && sb < 7 && (m & 1) && m < 10) issue(sb + 3, m >> 1);
      if constexpr (sb == 6 && m == 10) { issue_g(0, 0); issue_g(0, 1); ld_gran(0); ld_gran(1); ld_gran(2); ld_gran(3); }
      if constexpr (sb == 7 && !(m & 1)) issue_g(1 + m / 4, (m >> 1) & 1);
      if constexpr (sb == 8 && (m & 1) && m < 9) issue_g(4 + m / 4, (m >> 1) & 1);
      if constexpr (sb == 9 && m == 0) ld_gran(4);
      if constexpr (sb == 9 && m > 0 && m < 3) { issue_g(5 + m, 0); issue_g(5 + m, 1); }
      if constexpr (sb == 8 && m < 9) prep(m);
      SB();
    });
    TS(13);
    auto round2 = [&](auto TY_) {
      constexpr int TY = TY_.v;
      f32x16 ac2[2];
      u32x4 qv, hv[2], A4[2];
      u32x2 gc2[2];
      float t[8], rq;
      unsigned fq, vf, vh;
      bool late;
      auto qop = [&](auto U_, auto K_) {
        constexpr int u = U_.v, k = K_.v;
        const unsigned ln = 4 * pr + tyB + u;
        if constexpr (k == 0) {
          const float qs = ln < 10u ? 0.22360679775f * 1.44269504089f : 1.0f;
          asm volatile("v_mul_f32 %0, %1, %2" : "=v"(rq) : "s"(qs), "v"(rs));
        } else {
          constexpr int h = (k - 1) / 13, j = (k - 1) % 13;
          if constexpr (j < 8) t[j] = v_mul(acc[u][8 * h + j], rq);
          else if constexpr (j < 12) qv[j - 8] = v_cvt(t[2 * j - 16], t[2 * j - 15]);
          else st16<1>(qv, vl, sc, WS_QKV + (Rq * 240u + ln * 8u + cw * 4u + h * 2u) * 512u);
        }
      };
      auto gop = [&](f32x16& a, u32x4& o, auto K_) {
        constexpr int k = K_.v, ph = k >> 3, j = k & 7;
        if constexpr (ph == 0) t[j] = v_mul(a[j], rg);
        else if constexpr (ph == 1) t[j] = v_exp(t[j]);
        else if constexpr (ph == 2) t[j] = v_fma(t[j], sd, sd);
        else if constexpr (ph == 3) t[j] = v_rcp(t[j]);
        else if constexpr (ph == 4) t[j] = v_mul(t[j], a[8 + j]);
        else o[j] = v_cvt(t[2 * j], t[2 * j + 1]);
      };
      auto st_h = [&](u32x4 v, unsigned gl, int a) { st16<1>(v, vh, sc, WS_AF + ((rb * 8 + 2 * mw) * 68u + 2 * gl + cw) * 1024u + a * 512u); };
      auto epi = [&](auto K_) {
        constexpr int k = K_.v;
        if constexpr (k < 27) qop(IC<0>{}, K_);
        else if constexpr (TY == 1) { if constexpr (k < 81) qop(IC<k / 27>{}, IC<k % 27>{}); }
        else if constexpr (TY == 0) {
          if constexpr (k < 117) {
            constexpr int u = (k - 27) / 45, q = (k - 27) % 45;
            if constexpr (q < 44) gop(acc[1 + u], hv[u], IC<q>{}); else st_h(hv[u], gl1, u);
          }
        } else if constexpr (k < 29) {
          PIN(acc[k - 26]);
          st8<1>(u32x2{__float_as_uint(acc[k - 26][0] * 0.5f), tag}, sel(mw == 0, vl8, OOB), sc, C_CS + (rb * 10 + cw) * 512u + (k - 24) * 1024u);
        }
      };
      constexpr auto EK = [](int q) { return q < 1 ? 0 : (TY == 0 && q > 7) ? cl(30 + (q - 7) * 3, 0, 117) : cl((q - 1) * 5, 0, 117); };
      sfor<0, 80>([&](auto M_) {
        constexpr int m = M_.v;
        constexpr auto KK = [](int q) { return q < 64 ? q >> 1 : 32 + (q & 7); };
        constexpr auto AA = [](int q) { return q < 64 ? q & 1 : (q >> 3) & 1; };
        constexpr int kk = KK(m), a = AA(m), g = kk >> 2;
        if constexpr (m == 16) {
          asm volatile("s_waitcnt vmcnt(%1)" : "+v"(pf) : "i"(TY == 1 ? 5 : TY == 0 ? 2 : 4) : "memory");
          poll_rest(pf, true, tag, C_PF + wave * 4096u + ((vl8 & 248u) << 4), sc);
          bar();
          TS(14);
          st4s(tag, sel(vl8 == 0, (bid | wave) == 0 ? 0u : OOB, OOB), sc);
          SB();
        }
        if constexpr (m == 44) {
          wait_vm<(TY == 0 ? 11 : 10)>();
          st4s(tag, sel(vl8 == 0, C_FQ + fme, OOB), sc);
          TS(15);
          SB();
        }
        if constexpr (m == 48) { bar(); SB(); }
        if constexpr (m == 50) {
          {
            const int l = lane_id(), sec = (l + 2) >> 2, fr = sel(l < 2, ch * 8 + wave, ch * 8 - 8 + 2 * wave + ((l >> 1) & 1));
            const unsigned oc = sec * 80u + o0 + (l & 1) * 2u, jp = (oc >> 5) * 2 + ((oc & 24u) != 0);
            vf = sel((l < 10) & (fr >= 0), C_FQ + ((jp * 16 + (fr >> 2)) * 8 + (oc & 4u) + (fr & 3)) * 128u, OOB);
          }
          gc2[0] = ld8<1>(vl8, sc, csb + 3072u); gc2[1] = ld8<1>(vl8, sc, csb + 4096u);
          fq = ld4<1>(vf, sc, 0);
          SB();
        }
        if constexpr (m == 59) {
          asm volatile("s_waitcnt vmcnt(0)" : "+v"(fq), "+v"(gc2[0]), "+v"(gc2[1]) :: "memory");
          TS(16);
          if (any_lane((gc2[0][1] != tag) | (gc2[1][1] != tag))) {
            for (int it = 0; it < SPIN; it++) {
              __builtin_amdgcn_s_sleep(1);
              gc2[0] = ld8<1>(vl8, sc, csb + 3072u); gc2[1] = ld8<1>(vl8, sc, csb + 4096u);
              asm volatile("s_waitcnt vmcnt(0)" : "+v"(gc2[0]), "+v"(gc2[1]) :: "memory");
              if (!any_lane((gc2[0][1] != tag) | (gc2[1][1] != tag))) break;
            }
          }
          late = any_lane((vf != OOB) & (fq != tag));
          fq = ld4<1>(vf, sc, 0);
          if (!late) kv_issue();
          SB();
        }
        if constexpr (m == 68) {
          if (late) {
            asm volatile("s_waitcnt vmcnt(1)" : "+v"(fq) :: "memory");
            poll_rest(fq, vf != OOB, tag, vf, sc);
            kv_issue();
          }
          q_issue();
          TS(17);
          SB();
        }
        const u32x4 xf = (g == 2 || g == 3) ? xt[kk & 1] : X[(g == 2 || g == 3) ? 0 : KS(g) * 4 + (kk & 3)];
        if constexpr (m < 2) mfma32z(cat(w2[m & 3][0], w2[m & 3][1]), xf, ac2[a]);
        else mfma32a(cat(w2[m & 3][0], w2[m & 3][1]), xf, ac2[a]);
        if constexpr (m + 3 < 80) {
          constexpr int k2 = KK(m + 3), a2 = AA(m + 3), g2 = k2 >> 2, gp = (k2 - 1) >> 2;
          if constexpr ((LG(g2) >> 16) != (LG(gp) >> 16)) { wr[a2] += (int)(LG(g2) & ~65535u) - (int)(LG(gp) & ~65535u); PIN(wr[a2]); }
          w2[(m + 3) & 3][0] = trd(wr[a2] + ((LG(g2) & 65535u) + (k2 & 3) * 2048));
          w2[(m + 3) & 3][1] = trd(wr[a2] + ((LG(g2) & 65535u) + (k2 & 3) * 2048 + 512));
          if constexpr (a2 == 0 && (g2 == 2 || g2 == 3)) xt[k2 & 1] = *(LAS u32x4*)(xa[k2 & 3] + (KS(g2) % 3) * 16384);
        }
        if constexpr (m == 8) { const unsigned l = lane_id(); vh = (l & 15) * 16u + ((l >> 4) & 1) * 69632u + (l >> 5) * 256u; PIN(vh); }
        sfor<EK(m), EK(m + 1)>(epi);
        if constexpr (m == 16 || m == 17) { issue_g(m - 8, 0); issue_g(m - 8, 1); }
        if constexpr (m >= 24 && m < 44 && !(m & 1)) ld_w3((m - 24) / 10, (m - 24) / 10, ((m - 24) >> 1) % 5);
        if constexpr (m == 60 || m == 61) A4[m - 60] = split3(gc2[m - 60][0]);
        if constexpr (m == 62) Bc = mk_bc();
        if constexpr (m == 64 || m == 65) mfma32a(A4[m - 64], Bc, ac2[m - 64]);
        if constexpr (m == 66 || (m >= 70 && m < 78 && !(m & 1))) ld_w3(2, 2, m == 66 ? 0 : (m - 68) >> 1);
        if constexpr (m >= 73) sfor<(m - 73) * 7, cl((m - 72) * 7, 0, 44)>([&](auto K_) { gop(ac2[0], hv[0], K_); });
        SB();
      });
      TS(18);
      st_h(hv[0], gl2, 0);
      asm volatile("s_nop 7" ::: "memory");
      sfor<0, 44>([&](auto K_) { gop(ac2[1], hv[1], K_); });
      st_h(hv[1], gl2, 1);
      u32x4 (&rw)[6][5] = RW; u32x4 &q1 = Q1, &q2 = Q2;
      asm volatile("s_waitcnt vmcnt(6)" : "+v"(q1), "+v"(q2), "+v"(fq), "+v"(rw[0][0]), "+v"(rw[0][1]), "+v"(rw[0][2]), "+v"(rw[0][3]), "+v"(rw[0][4]),
                   "+v"(rw[1][0]), "+v"(rw[1][1]), "+v"(rw[1][2]), "+v"(rw[1][3]), "+v"(rw[1][4]) :: "memory");
      if constexpr (TY == 0) st4s(tag, sel(vl8 == 0, C_FE + fme, OOB), sc);
      TS(19);
      rest();
    };
    if constexpr (SP) round2(IC<2>{});
    else { if (tyB) round2(IC<1>{}); else round2(IC<0>{}); }
  };
  if (spec) phase1(IC<1>{}); else phase1(IC<0>{});
}

extern "C" void kernel_launch(void* const* d_in, const int* in_sizes, int n_in, void* d_out, int out_size, void* d_ws,
                              size_t ws_size, hipStream_t stream) {
  Params p;
  p.x = (const char*)d_in[0]; p.wqkv = (const char*)d_in[1]; p.wo = (const char*)d_in[2];
  p.wg = (const char*)d_in[4]; p.wu = (const char*)d_in[5]; p.wd = (const char*)d_in[6];
  p.out = (char*)d_out; p.ws = (char*)d_ws;
  (void)hipFuncSetAttribute((const void*)k_fused, hipFuncAttributeMaxDynamicSharedMemorySize, LDS_BYTES);
  k_fused<<<256, 512, LDS_BYTES, stream>>>(p);
}
```

```cpp
#include <hip/hip_runtime.h>

typedef int i32x4 __attribute__((ext_vector_type(4)));
typedef unsigned u32x4 __attribute__((ext_vector_type(4)));
typedef unsigned u32x2 __attribute__((ext_vector_type(2)));
typedef float f32x4 __attribute__((ext_vector_type(4)));
typedef float f32x2 __attribute__((ext_vector_type(2)));
typedef float f32x16 __attribute__((ext_vector_type(16)));
typedef __bf16 bf16x8 __attribute__((ext_vector_type(8)));
typedef __bf16 bf16x2 __attribute__((ext_vector_type(2)));
typedef short s16x4 __attribute__((ext_vector_type(4)));
#define LAS __attribute__((address_space(3)))
#define DEVI __device__ __forceinline__

constexpr int S_ = 2048, D_ = 640, F_ = 1536;
constexpr unsigned X_BYTES = S_ * D_ * 2, WQ_BYTES = D_ * 3 * D_ * 2, WF_BYTES = D_ * F_ * 2, WO_BYTES = D_ * D_ * 2;
constexpr unsigned QKV_BYTES = S_ * 3 * D_ * 2;
constexpr unsigned AF_BYTES = 128u * 68u * 1024u;
constexpr unsigned W3_BYTES = 68u * 40u * 1024u;
constexpr unsigned WS_QKV = 2u << 20, WS_AF = 16u << 20, WS_W3 = 32u << 20, WS_BYTES = 40u << 20;
constexpr unsigned C_PF = 0x8000, C_STAT = 0x10000, C_CS = 0x20000, C_FQ = 0x40000, C_FH = 0x80000, C_FO = 0xC0000, C_FE = 0x100000;
constexpr unsigned LDS_BYTES = 163840;
constexpr unsigned OOB = 0x80000000u;

DEVI i32x4 mk_srd(const void* p, unsigned n) {
  unsigned long long a = (unsigned long long)p;
  i32x4 r;
  r.x = __builtin_amdgcn_readfirstlane((int)(unsigned)a);
  r.y = __builtin_amdgcn_readfirstlane((int)(unsigned)(a >> 32));
  r.z = (int)n; r.w = 0x00020000;
  return r;
}
DEVI i32x4 sel_srd(bool c, i32x4 a, i32x4 b) { i32x4 r; r.x = c ? a.x : b.x; r.y = c ? a.y : b.y; r.z = c ? a.z : b.z; r.w = a.w; return r; }
template <int SC = 0> DEVI void dma16(unsigned ldsaddr, unsigned voff, i32x4 srd, unsigned soff) {
  if (SC) asm volatile("s_mov_b32 m0, %0\n\ts_nop 0\n\tbuffer_load_dwordx4 %1, %2, %3 offen sc1 lds"
                       :: "s"(ldsaddr), "v"(voff), "s"(srd), "s"(soff) : "memory");
  else    asm volatile("s_mov_b32 m0, %0\n\ts_nop 0\n\tbuffer_load_dwordx4 %1, %2, %3 offen lds"
                       :: "s"(ldsaddr), "v"(voff), "s"(srd), "s"(soff) : "memory");
}
template <int SC, int OFF = 0> DEVI u32x4 ld16(unsigned voff, i32x4 srd, unsigned soff) {
  u32x4 r;
  if (SC) asm volatile("buffer_load_dwordx4 %0, %1, %2, %3 offen offset:%4 sc1" : "=v"(r) : "v"(voff), "s"(srd), "s"(soff), "i"(OFF) : "memory");
  else    asm volatile("buffer_load_dwordx4 %0, %1, %2, %3 offen offset:%4" : "=v"(r) : "v"(voff), "s"(srd), "s"(soff), "i"(OFF) : "memory");
  return r;
}
template <int SC, int OFF = 0> DEVI u32x2 ld8(unsigned voff, i32x4 srd, unsigned soff) {
  u32x2 r;
  if (SC) asm volatile("buffer_load_dwordx2 %0, %1, %2, %3 offen offset:%4 sc1" : "=v"(r) : "v"(voff), "s"(srd), "s"(soff), "i"(OFF) : "memory");
  else    asm volatile("buffer_load_dwordx2 %0, %1, %2, %3 offen offset:%4" : "=v"(r) : "v"(voff), "s"(srd), "s"(soff), "i"(OFF) : "memory");
  return r;
}
template <int SC, int OFF = 0> DEVI unsigned ld4(unsigned voff, i32x4 srd, unsigned soff) {
  unsigned r;
  if (SC) asm volatile("buffer_load_dword %0, %1, %2, %3 offen offset:%4 sc1" : "=v"(r) : "v"(voff), "s"(srd), "s"(soff), "i"(OFF) : "memory");
  else    asm volatile("buffer_load_dword %0, %1, %2, %3 offen offset:%4" : "=v"(r) : "v"(voff), "s"(srd), "s"(soff), "i"(OFF) : "memory");
  return r;
}
template <int SC> DEVI void st16(u32x4 v, unsigned voff, i32x4 srd, unsigned soff) {
  if (SC) asm volatile("buffer_store_dwordx4 %0, %1, %2, %3 offen sc1\n\ts_nop 1" :: "v"(v), "v"(voff), "s"(srd), "s"(soff) : "memory");
  else    asm volatile("buffer_store_dwordx4 %0, %1, %2, %3 offen\n\ts_nop 1" :: "v"(v), "v"(voff), "s"(srd), "s"(soff) : "memory");
}
template <int SC, int OFF = 0> DEVI void st8(u32x2 v, unsigned voff, i32x4 srd, unsigned soff) {
  if (SC) asm volatile("buffer_store_dwordx2 %0, %1, %2, %3 offen offset:%4 sc1" :: "v"(v), "v"(voff), "s"(srd), "s"(soff), "i"(OFF) : "memory");
  else    asm volatile("buffer_store_dwordx2 %0, %1, %2, %3 offen offset:%4" :: "v"(v), "v"(voff), "s"(srd), "s"(soff), "i"(OFF) : "memory");
}
DEVI void st4s(unsigned v, unsigned voff, i32x4 srd) {
  asm volatile("buffer_store_dword %0, %1, %2, 0 offen sc1" :: "v"(v), "v"(voff), "s"(srd) : "memory");
}
template <int N> DEVI void wait_vm() { asm volatile("s_waitcnt vmcnt(%0)" :: "i"(N) : "memory"); }
#define WAIT_LGKM0() asm volatile("s_waitcnt lgkmcnt(0)" ::: "memory")
DEVI void bar() { asm volatile("s_barrier" ::: "memory"); }
#define SB() __builtin_amdgcn_sched_barrier(0)
#define PIN(x) asm volatile("" : "+v"(x))

DEVI unsigned pk2(float a, float b) { bf16x2 v = {(__bf16)a, (__bf16)b}; return __builtin_bit_cast(unsigned, v); }
DEVI float rbf(float a) { return (float)(__bf16)a; }
DEVI bf16x8 asbf(u32x4 v) { return __builtin_bit_cast(bf16x8, v); }
DEVI u32x2 trd(LAS char* p) { return __builtin_bit_cast(u32x2, __builtin_amdgcn_ds_read_tr16_b64_v4i16((LAS s16x4*)p)); }
DEVI u32x4 cat(u32x2 a, u32x2 b) { return __builtin_shufflevector(a, b, 0, 1, 2, 3); }
DEVI f32x4 mfma16(u32x4 a, u32x4 b, f32x4 c) { return __builtin_amdgcn_mfma_f32_16x16x32_bf16(asbf(a), asbf(b), c, 0, 0, 0); }
DEVI void mfma16a(u32x4 a, u32x4 b, f32x4& c) { asm volatile("v_mfma_f32_16x16x32_bf16 %0, %1, %2, %0" : "+v"(c) : "v"(a), "v"(b)); }
DEVI void mfma16z(u32x4 a, u32x4 b, f32x4& c) { asm volatile("v_mfma_f32_16x16x32_bf16 %0, %1, %2, 0" : "=&v"(c) : "v"(a), "v"(b)); }
DEVI void mfma32a(u32x4 a, u32x4 b, f32x16& c) { asm volatile("v_mfma_f32_32x32x16_bf16 %0, %1, %2, %0" : "+v"(c) : "v"(a), "v"(b)); }
DEVI void mfma32z(u32x4 a, u32x4 b, f32x16& c) { asm volatile("v_mfma_f32_32x32x16_bf16 %0, %1, %2, 0" : "=&v"(c) : "v"(a), "v"(b)); }
DEVI int lane_id() { int l; asm volatile("v_mbcnt_lo_u32_b32 %0, -1, 0\n\tv_mbcnt_hi_u32_b32 %0, -1, %0" : "=v"(l)); return l; }
DEVI f32x16 mfma32(u32x4 a, u32x4 b, f32x16 c) { return __builtin_amdgcn_mfma_f32_32x32x16_bf16(asbf(a), asbf(b), c, 0, 0, 0); }
DEVI float blo(unsigned r) { return __uint_as_float(r << 16); }
DEVI float bhi(unsigned r) { return __uint_as_float(r & 0xffff0000u); }
template <int C> DEVI float dppf(float v) { return __uint_as_float(__builtin_amdgcn_update_dpp(0, __float_as_uint(v), C, 0xf, 0xf, false)); }
DEVI float wave_sum(float v) {
  v += dppf<0x128>(v); v += dppf<0x124>(v); v += dppf<0x122>(v); v += dppf<0x121>(v);
  auto a = __builtin_amdgcn_permlane16_swap(__float_as_uint(v), __float_as_uint(v), false, false);
  v = __uint_as_float(a[0]) + __uint_as_float(a[1]);
  auto b = __builtin_amdgcn_permlane32_swap(__float_as_uint(v), __float_as_uint(v), false, false);
  return __uint_as_float(b[0]) + __uint_as_float(b[1]);
}
DEVI float v_fma(float a, float b, float c) { float r; asm volatile("v_fma_f32 %0, %1, %2, %3" : "=v"(r) : "v"(a), "v"(b), "v"(c)); return r; }
DEVI float v_mul(float a, float b) { float r; asm volatile("v_mul_f32 %0, %1, %2" : "=v"(r) : "v"(a), "v"(b)); return r; }
DEVI float v_exp(float a) { float r; asm volatile("v_exp_f32 %0, %1" : "=v"(r) : "v"(a)); return r; }
DEVI float v_rcp(float a) { float r; asm volatile("v_rcp_f32 %0, %1" : "=v"(r) : "v"(a)); return r; }
DEVI unsigned v_cvt(float a, float b) { unsigned r; asm volatile("v_cvt_pk_bf16_f32 %0, %1, %2" : "=v"(r) : "v"(a), "v"(b)); return r; }
DEVI float dot2(unsigned w, unsigned sel, float c) { return __builtin_amdgcn_fdot2_f32_bf16(__builtin_bit_cast(bf16x2, w), __builtin_bit_cast(bf16x2, sel), c, false); }
DEVI unsigned sel(bool c, unsigned a, unsigned b) { return c ? a : b; }
DEVI bool any_lane(bool c) { return __builtin_amdgcn_ballot_w64(c) != 0; }
constexpr int SPIN = 1 << 15;
DEVI void poll_rest(unsigned v, bool need, unsigned tag, unsigned voff, i32x4 srd) {
  if (any_lane(need & (v != tag))) {
    for (int it = 0; it < SPIN; it++) {
      __builtin_amdgcn_s_sleep(1);
      v = ld4<1>(voff, srd, 0);
      asm volatile("s_waitcnt vmcnt(0)" : "+v"(v) :: "memory");
      if (!any_lane(need & (v != tag))) break;
    }
  }
}

#define TS(i)
#define TSDECL
#define TSOUT()
template <int I> struct IC { static constexpr int v = I; };
template <int B, int E, class F> DEVI void sfor(F&& f) { if constexpr (B < E) { f(IC<B>{}); sfor<B + 1, E>(f); } }

struct Params {
  const char *x, *wqkv, *wo, *wg, *wu, *wd;
  char *out, *ws;
};

extern __shared__ __attribute__((aligned(16))) char smem_[];

constexpr unsigned UN = 8192;
constexpr unsigned LW(int s) { return s % 3 == 0 ? 0 : s % 3 == 1 ? 4 * UN : 7 * UN; }
constexpr unsigned L_PK = 3 * UN, L_X = 10 * UN, L_CSR = 16 * UN;
constexpr unsigned LG(int g) { return (g == 0 || g == 8 ? 16 : g == 1 || g == 9 ? 18 : g == 2 ? 4 : g == 3 ? 12 : g == 4 ? 6 : g == 5 ? 8 : g == 6 ? 0 : 2) * UN; }
constexpr int KS(int g) { return g < 2 ? g : g < 4 ? g + 6 : g - 2; }
constexpr unsigned AT_K = 4 * UN, AT_V = 8 * UN, R_A = 12 * UN;
constexpr int cl(int v, int lo, int hi) { return v < lo ? lo : v > hi ? hi : v; }

__global__ __launch_bounds__(512, 2) void k_fused(Params p) {
  TSDECL;
  LAS char* sm = (LAS char*)smem_;
  const unsigned lb = (unsigned)(size_t)sm;
  const int bid = blockIdx.x, xcd = bid & 7, loc = bid >> 3;
  const int wave = __builtin_amdgcn_readfirstlane(threadIdx.x >> 6);
  int lane = threadIdx.x & 63;
  const int rb = (xcd & 1) * 8 + (loc & 7), jg = (xcd >> 1) * 4 + (loc >> 3), pr = jg >> 1, tyB = jg & 1;
  const int mw = wave & 3, cw = wave >> 2;
  const bool spec = jg == 15, likeA = !tyB || spec;
  const i32x4 sx = mk_srd(p.x, X_BYTES), sq = mk_srd(p.wqkv, WQ_BYTES), sg = mk_srd(p.wg, WF_BYTES), su = mk_srd(p.wu, WF_BYTES);
  const int rot = (xcd & 1) * 5;
#define FHQ 3
#define ROT 0
  const int rr = ROT ? ((loc & 7) + (loc >> 3)) % 5 : 0;
  auto sidx = [&](int s) {
    int r2 = rr;
    if (ROT) asm volatile("" : "+s"(r2));
    const int v = s % 5 + r2;
    return (unsigned)((v >= 5 ? v - 5 : v) + (s < 5 ? rot : 5 - rot));
  };
  const unsigned drow = 8 * wave + (lane >> 3), wcol = ((lane & 7) ^ ((lane >> 2) & 4)) * 16u;
  const unsigned vx = (rb * 128 + drow) * 1280u + (((lane & 7) ^ (((wave & 1) << 2) | (lane >> 4))) * 16u);
  const unsigned vq = drow * 3840u + wcol, vg = drow * 3072u + wcol, vu = vg ^ 32u;
  const unsigned v1 = likeA ? vg : vq, v2 = likeA ? vu : vq;
  const i32x4 s1 = sel_srd(likeA, sg, sq), s2 = sel_srd(likeA, su, sq);
  const unsigned gl1 = spec ? 3 * (rb >> 1) + 1 + (rb & 1) : 3 * pr, gl2 = 3 * pr + 1 + tyB;
  const unsigned c0 = (4 * pr + tyB) * 128u, c1 = likeA ? gl1 * 128u : c0 + 128u, c2 = likeA ? c1 : c0 + 256u;
  const unsigned st1 = likeA ? 196608u : 245760u;
  const unsigned ldw = lb + wave * 1024u;
  auto issue = [&](int s, int b) {
    if (b < 2) dma16(ldw + L_X + (s % 3) * 16384u + b * UN, vx, sx, b * 81920u + sidx(s) * 128u);
    else if (b == 2) dma16(ldw + LW(s), vq, sq, sidx(s) * 245760u + c0);
    else if (b == 3) dma16(ldw + LW(s) + UN, v1, s1, sidx(s) * st1 + c1);
    else dma16(ldw + LW(s) + 2 * UN, v2, s2, sidx(s) * st1 + c2);
  };
  auto issue_g = [&](int g, int b) { dma16(ldw + LG(g) + b * UN, b ? vu : vg, b ? su : sg, sidx(KS(g)) * 196608u + gl2 * 128u); };
#pragma unroll
  for (int b = 0; b < 5; b++) issue(0, b);
  const i32x4 sc = mk_srd(p.ws, WS_BYTES);
  const unsigned srow = rb * 128 + jg * 8 + wave;
  unsigned vl = lane * 16u;
  unsigned ep = ld4<1>(0u, sc, 0);
  u32x4 r0 = ld16<0>(vl, sx, srow * 1280u);
  unsigned r1 = ld4<0, 1024>(lane * 4u, sx, srow * 1280u);
  bar();
#pragma unroll
  for (int b = 0; b < 5; b++) issue(1, b);
  TS(1);
  const i32x4 sdn = mk_srd(p.wd, WF_BYTES), so = mk_srd(p.wo, WO_BYTES);
  const unsigned kA = bid * 205u >> 10, cA = bid - kA * 5u, kB = (bid + 256u) * 205u >> 10, cB = bid + 256u - kB * 5u;
  const unsigned offB = bid < 84 ? 0u : OOB;
  const unsigned vpk = (4 * wave + (lane >> 4)) * 1280u + (lane & 15) * 16u;
  LAS char* pkr = sm + (L_PK + (8 * (lane >> 4) + ((lane & 15) >> 2)) * 256u + wave * 32u + (lane & 3) * 8u);
  int hi = lane >> 5;
  LAS char* xa[4];
#pragma unroll
  for (int ks = 0; ks < 4; ks++) { xa[ks] = sm + (L_X + (32 * mw + (lane & 31)) * 128u + (((2 * ks + hi) ^ ((lane >> 1) & 7)) * 16u)); PIN(xa[ks]); }
  LAS char *wa[3], *wr[2];
  {
    const unsigned ch = (lane >> 4) & 1, r = (lane >> 2) & 3, c = lane & 3;
    const unsigned wl = hi * 1024u + r * 128u + ((cw ^ (r >> 1)) * 64u) + (((c & 1) << 1 | c >> 1) * 8u);
    const unsigned q = wl + ch * 32u, g0 = wl + ch * (UN + 32u), g1 = wl + ch * UN + (ch ^ 1) * 32u;
    wa[0] = sm + q; wa[1] = sm + (UN + (likeA ? g0 : q)); wa[2] = sm + (likeA ? UN + g1 : 2 * UN + q);
    wr[0] = sm + (g0 + LG(0)); wr[1] = sm + (g1 + LG(0));
    PIN(wa[0]); PIN(wa[1]); PIN(wa[2]); PIN(wr[0]); PIN(wr[1]);
  }
  const int cst = loc & 7;
  LAS char* wc = (cst == 1 ? wa[1] : cst == 2 ? wa[2] : wa[0]) + mw * 2048;
  PIN(wc);
  const unsigned csb = C_CS + (jg * 10 + cw) * 512u;
  const u32x4 ones = {0x3f803f80u, 0x3f803f80u, 0x3f803f80u, 0x3f803f80u};
  const unsigned Rq = rb * 4 + mw;
  const unsigned fme = ((jg * 16 + rb) * 8 + wave) * 128u;
  const int head = xcd * 4 + (loc & 3), ch = loc >> 2, par = head & 1, o0 = 5 * head >> 1;
  const unsigned w3b = WS_W3 + (wave * 240 + ch * 5) * 1024u;
  unsigned w3c = 0;
#define SROT 1
  auto jr = [&](int j) {
    if (!SROT || j == 8) return (unsigned)(j < 6 ? j : j - 6);
    unsigned c = bid;
    asm volatile("" : "+s"(c));
    c >>= 5;
    return j < 2 ? j ^ (c & 1) : j < 6 ? 2 + ((j + 2 + c) & 3) : (j - 6) ^ (c & 1);
  };
  unsigned wbs = 0;
  auto w3a = [&](int j, int u) {
    if (u == 0) wbs = j < 6 ? w3b + jr(j) * 40960u : w3c + jr(j) * 327680u;
    return wbs + u * 1024u;
  };
  u32x4 RW[6][5], Q1, Q2;
  unsigned tag = 0, fe = 0;
  auto fpo = [&]() {
    int b3 = bid;
    asm volatile("" : "+s"(b3));
    const unsigned l = lane_id(), rt = (b3 & 7) * 4 + ((b3 >> 3) & 3);
    return (((2 * wave + (l >> 2)) * 16 + (rt >> 1)) * 8 + (l & 2) * 2 + (rt & 1) * 2 + (l & 1)) * 128u;
  };
  auto ld_w3 = [&](int buf, int j, int u) { RW[buf][u] = ld16<1>(vl, sc, w3a(j, u)); };
  auto kv_issue = [&]() {
    const int l = lane_id(), oi = ((l >> 2) & 3) ^ (l >> 4), R = ch * 8 - 8 + 2 * wave;
    const unsigned v = sel(oi < (R >= 0 ? 3 : 0), oi * 512u + ((l >> 4) * 4 + (l & 3)) * 16u, OOB);
    const unsigned b = WS_QKV + (unsigned)((R >= 0 ? R : 0) * 240 + 80 + o0) * 512u;
#pragma unroll
    for (int i = 0; i < 8; i++)
      dma16<1>(lb + AT_K + (i >> 2) * 32768u + wave * 4096u + (i & 3) * 1024u, v, sc, b + (i >> 2) * 40960u + ((i >> 1) & 1) * 122880u + (i & 1) * 256u);
  };
  auto q_issue = [&]() {
    const unsigned qb = WS_QKV + (unsigned)((ch * 8 + wave) * 240 + o0) * 512u;
    Q1 = ld16<1>(vl, sc, qb);
    Q2 = ld16<1, 1024>(sel(vl < 512u, vl, OOB), sc, qb);
  };

  LAS char *kp, *kp1, *vp[2], *cw_;
  f32x16 sa, sb;
  unsigned nmk[8];
  auto att_addr = [&](int k) {
    const int l = lane_id(), h = l >> 5, q = l & 31;
    if (k == 0) {
      kp = sm + (AT_K + wave * 2048 + (q >> 2) * 256 + ((h ^ ((q >> 2) & 3)) * 64) + (q & 3) * 16);
      kp1 = sm + (((unsigned)(size_t)kp - lb) ^ 128u);
      PIN(kp); PIN(kp1);
    } else if (k == 1) {
      cw_ = sm + (AT_K + wave * 4096 + h * 2048 + (q >> 2) * 256 + ((3 ^ ((q >> 2) & 3)) * 64) + (q & 3) * 16);
      PIN(cw_);
    } else {
      const int mq = 4 * ((l >> 4) & 1) + (l & 3), iq = sel(mq < 4, 2 * par + ((mq & 1) << 1 | mq >> 1), sel(mq == 4, par ? 1 : 4, 6));
#pragma unroll
      for (int g = 0; g < 2; g++) {
        vp[g] = sm + (AT_V + wave * 2048 + h * 256 + g * 512 + (((iq >> 1) ^ (2 * g + h)) * 64) + ((l >> 2) & 3) * 16 + (iq & 1) * 8);
        PIN(vp[g]);
      }
    }
  };
  float dlf;
  auto att_mask = [&](int k) {
    if (k == 0) { const int l = lane_id(); dlf = (float)((l & 31) - 4 * (l >> 5)) * 1.0e36f; PIN(dlf); }
#pragma unroll
    for (int r = 2 * k; r < 2 * k + 2; r++) {
      const float d = dlf + (0.5f - (float)((r & 3) + 8 * (r >> 2))) * 1.0e36f;
      float a = __builtin_fminf(-d, 0.f), b = __builtin_fminf(d, 0.f);
      PIN(a); PIN(b);
      sa[r] = a; sb[r] = b;
    }
    asm volatile("v_pk_ashrrev_i16 %0, 15, %1 op_sel_hi:[0,1]" : "=v"(nmk[k]) : "v"(v_cvt(sb[2 * k], sb[2 * k + 1])));
  };

#define PINA(n) "+v"(A[n][0]), "+v"(A[n][1]), "+v"(A[n][2]), "+v"(A[n][3])
#define PINW(n) "+v"(RW[n][0]), "+v"(RW[n][1]), "+v"(RW[n][2]), "+v"(RW[n][3]), "+v"(RW[n][4])
  auto rest = [&]() __attribute__((always_inline)) {
  lane = lane_id(); hi = lane >> 5; vl = lane * 16u;
  const int q32 = lane & 31;
  int bid2 = bid;
  asm volatile("" : "+s"(bid2));
  const int rt = (bid2 & 7) * 4 + ((bid2 >> 3) & 3), cg = bid2 >> 5;
  w3c = WS_W3 + ((48 + wave) * 40 + cg * 5) * 1024u;
  const unsigned ab = WS_AF + (rt * 4 * 68 + wave * 6) * 1024u, ac = WS_AF + (rt * 4 * 68 + 48 + wave) * 1024u;
  const unsigned r7 = wave < 4 ? R_A + 40960u + wave * 5120u : (wave - 4) * 5120u;
  u32x4 A[4][4];
  unsigned abs_ = 0;
  auto ld_a = [&](int buf, int j, int i, unsigned v) {
    if (i == 0) abs_ = j < 6 ? ab + jr(j) * 1024u : ac + jr(j) * 8192u;
    A[buf][i] = ld16<1>(v, sc, abs_ + i * 69632u);
  };
  auto dma_w3 = [&](unsigned dst, int j, int u) { dma16<1>(lb + dst + u * 1024u, vl, sc, w3a(j, u)); };
  f32x16 o;
  unsigned hf;
  const unsigned vfl = C_FO + ((cg * 8 + wave) * 32u + rt) * 128u;
  {
    *(LAS u32x4*)cw_ = u32x4{(ch == 0 && wave < 4) ? 0x3f80fe00u : 0x3f800000u, 0u, 0u, 0u};
    *(LAS u32x4*)(cw_ + 32768) = ones;
    {
      const bool z1 = par & !hi;
      Q1[0] = sel(z1, 0u, Q1[0]); Q1[1] = sel(z1, 0u, Q1[1]);
      Q2[2] = sel(par, Q2[2], 0u); Q2[3] = sel(par, Q2[3], 0u);
      Q2[0] = sel(hi, 0x00003f80u, Q2[0]);
    }
    const unsigned fp_ = fpo();
    unsigned vfe = sel(lane < 4, C_FE + fp_, OOB), vfh = sel(lane < 8, C_FH + fp_, OOB);
    PIN(vfe); PIN(vfh); PIN(Q1); PIN(Q2);
    WAIT_LGKM0();
    bar();
    TS(20);
    f32x16 z;
#pragma unroll
    for (int r = 0; r < 16; r++) z[r] = 0.f;
    auto scq = [&](int off, u32x4 q2) {
      const u32x4 a = *(LAS u32x4*)(kp + off), b = *(LAS u32x4*)(kp1 + off);
      return mfma32(b, q2, mfma32(a, Q1, z));
    };
    auto pv = [&](int off, const unsigned* pk_) {
      const u32x4 V1 = cat(trd(vp[0] + off), trd(vp[1] + off)), V2 = cat(trd(vp[0] + off + 1024), trd(vp[1] + off + 1024));
      o = mfma32(V1, u32x4{pk_[0], pk_[1], pk_[2], pk_[3]}, o);
      o = mfma32(V2, u32x4{pk_[4], pk_[5], pk_[6], pk_[7]}, o);
    };
    const int dl = q32 - 4 * hi;
    auto xmax = [&](float m) {
      auto r = __builtin_amdgcn_permlane32_swap(__float_as_uint(m), __float_as_uint(m), false, false);
      return __builtin_fmaxf(__uint_as_float(r[0]), __uint_as_float(r[1]));
    };
    auto edge = [&](const f32x16& ee, float m) {
      unsigned pa[8], pb[8];
#pragma unroll
      for (int r = 0; r < 16; r += 2) {
        const bool b0 = (r & 3) + 8 * (r >> 2) <= dl, b1 = ((r + 1) & 3) + 8 * (r >> 2) <= dl;
        const unsigned x = pk2(__builtin_amdgcn_exp2f(ee[r] - m), __builtin_amdgcn_exp2f(ee[r + 1] - m));
        const unsigned mk = (b0 ? 0xffffu : 0u) | (b1 ? 0xffff0000u : 0u);
        pb[r >> 1] = x & mk; pa[r >> 1] = x & ~mk;
      }
      pv(0, pa); pv(8 * 2048, pb);
    };
    auto merged = [&]() {
      const f32x16 s0 = scq(0, Q2), s8 = scq(8 * 2048, Q2);
      f32x16 ee;
#pragma unroll
      for (int r = 0; r < 16; r++) ee[r] = ((r & 3) + 8 * (r >> 2) <= dl) ? s8[r] : s0[r];
      return ee;
    };
    auto vmax = [&](const f32x16& ee, float m) {
#pragma unroll
      for (int r = 0; r < 16; r += 2) m = __builtin_fmaxf(__builtin_fmaxf(m, ee[r]), ee[r + 1]);
      return m;
    };
    unsigned va = 0;
    bool early = false;
    {
      u32x4 Ka, Kb, P0, P1;
      u32x2 Va[2], Vb[2];
      f32x16 S[2];
      float t[8];
      auto rdk = [&](int j) { Ka = *(LAS u32x4*)(kp + j * 2048); Kb = *(LAS u32x4*)(kp1 + j * 2048); };
      u32x4 Q3;
      {
        const u32x4 k0 = *(LAS u32x4*)kp, k8 = *(LAS u32x4*)(kp + 8 * 2048), k0b = *(LAS u32x4*)kp1, k8b = *(LAS u32x4*)(kp1 + 8 * 2048);
        rdk(1);
        const u32x4 v0 = cat(trd(vp[0]), trd(vp[1])), v0b = cat(trd(vp[0] + 1024), trd(vp[1] + 1024));
        SB();
        mfma32a(k0, Q1, sa); ld_w3(3, 3, 0);
        mfma32a(k8, Q1, sb); ld_w3(3, 3, 1);
        mfma32a(k0b, Q2, sa); ld_w3(3, 3, 2);
        mfma32a(k8b, Q2, sb); ld_w3(3, 3, 3);
        mfma32z(Ka, Q1, S[1]); ld_w3(3, 3, 4);
        const u32x4 v8 = cat(trd(vp[0] + 8 * 2048), trd(vp[1] + 8 * 2048)), v8b = cat(trd(vp[0] + (8 * 2048 + 1024)), trd(vp[1] + (8 * 2048 + 1024)));
        asm volatile("s_nop 7" : "+v"(sa), "+v"(sb) :: "memory");
        SB();
        f32x16 ee;
#pragma unroll
        for (int r = 0; r < 16; r++) { float x; asm volatile("v_max_f32 %0, %1, %2" : "=v"(x) : "v"(sa[r]), "v"(sb[r])); ee[r] = x; }
        const unsigned mb = pk2(xmax(vmax(ee, -3.0e38f)), 0.f) << 16;
        Q3 = u32x4{sel(hi, (mb ^ 0x80000000u) | 0x3f80u, Q2[0]), Q2[1], Q2[2], Q2[3]};
        PIN(Q3);
        f32x2 nm2 = {__uint_as_float(mb ^ 0x80000000u), __uint_as_float(mb ^ 0x80000000u)};
        PIN(nm2);
        SB();
        mfma32a(Kb, Q3, S[1]);
        rdk(2);
        u32x4 pa, pb;
        sfor<0, 2>([&](auto H) {
          constexpr int h = H.v;
#pragma unroll
          for (int r = 0; r < 4; r++) {
            f32x2 d = {ee[8 * h + 2 * r], ee[8 * h + 2 * r + 1]};
            asm volatile("v_pk_add_f32 %0, %0, %1" : "+v"(d) : "v"(nm2));
            t[2 * r] = d[0]; t[2 * r + 1] = d[1];
          }
#pragma unroll
          for (int r = 0; r < 8; r++) t[r] = v_exp(t[r]);
#pragma unroll
          for (int r = 0; r < 4; r++) pb[r] = v_cvt(t[2 * r], t[2 * r + 1]);
#pragma unroll
          for (int r = 0; r < 4; r++) pa[r] = pb[r] & nmk[4 * h + r];
          PIN(pa);
#pragma unroll
          for (int r = 0; r < 4; r++) pb[r] ^= pa[r];
          PIN(pb);
          SB();
          if constexpr (h == 0) mfma32z(v0, pa, o); else mfma32a(v0b, pa, o);
          mfma32a(h ? v8b : v8, pb, o);
          if constexpr (h == 0) { Va[0] = trd(vp[0] + 2048); Va[1] = trd(vp[1] + 2048); }
          else { Vb[0] = trd(vp[0] + 3072); Vb[1] = trd(vp[1] + 3072); }
          SB();
        });
      }
      auto vm = [&](auto Q_) {
        constexpr int q = Q_.v;
        if constexpr (q < 5) ld_w3(4, 4, q);
        else if constexpr (q < 10) dma_w3(r7, 7, q - 5);
        else if constexpr (q < 18) {
          if constexpr (q == 10) {
            asm volatile("s_waitcnt vmcnt(16)" : "+v"(fe) :: "memory");
            early = !any_lane((lane < 4) & (fe != tag));
            va = sel(early, vl, OOB);
            PIN(va);
          }
          ld_a((q - 10) >> 2, (q - 10) >> 2, (q - 10) & 3, va);
          if constexpr (q == 15) fe = ld4<1>(vfe, sc, 0);
        } else if constexpr (q < 23) dma_w3(R_A + wave * 5120u, 6, q - 18);
        else if constexpr (q == 23) {
          if (!early) {
            asm volatile("s_waitcnt vmcnt(5)" : "+v"(fe) :: "memory");
            poll_rest(fe, lane < 4, tag, vfe, sc);
#pragma unroll
            for (int i = 0; i < 8; i++)
              asm volatile("buffer_load_dwordx4 %0, %1, %2, %3 offen sc1" : "+v"(A[i >> 2][i & 3]) : "v"(vl), "s"(sc), "s"(ab + jr(i >> 2) * 1024u + (i & 3) * 69632u) : "memory");
          }
        } else if constexpr (q == 26) hf = ld4<1>(vfh, sc, 0);
        if constexpr (q == FHQ) {
          static_assert(FHQ < 10, "");
          TS(21);
          wait_vm<6 + FHQ>();
          st4s(tag, sel(lane == 0, C_FH + fme, OOB), sc);
          TS(22);
        }
        SB();
      };
      sfor<1, 8>([&](auto J) {
        constexpr int j = J.v;
        f32x16 &cu = S[j & 1], &nx = S[(j + 1) & 1];
        if constexpr (j < 7) mfma32z(Ka, Q1, nx);
        vm(IC<4 * j - 4>{});
#pragma unroll
        for (int r = 0; r < 4; r++) t[r] = v_exp(cu[r]);
        if constexpr (j < 7) mfma32a(Kb, Q3, nx);
        if constexpr (j < 6) rdk(j + 2);
        SB();
#pragma unroll
        for (int r = 4; r < 8; r++) t[r] = v_exp(cu[r]);
        vm(IC<4 * j - 3>{});
#pragma unroll
        for (int r = 0; r < 4; r++) P0[r] = v_cvt(t[2 * r], t[2 * r + 1]);
        SB();
#pragma unroll
        for (int r = 0; r < 2; r++) t[r] = v_exp(cu[8 + r]);
        mfma32a(cat(Va[0], Va[1]), P0, o);
        if constexpr (j < 7) { Va[0] = trd(vp[0] + (j + 1) * 2048); Va[1] = trd(vp[1] + (j + 1) * 2048); }
        vm(IC<4 * j - 2>{});
#pragma unroll
        for (int r = 2; r < 8; r++) t[r] = v_exp(cu[8 + r]);
        vm(IC<4 * j - 1>{});
#pragma unroll
        for (int r = 0; r < 4; r++) P1[r] = v_cvt(t[2 * r], t[2 * r + 1]);
        asm volatile("s_nop 0" ::: "memory");
        SB();
        mfma32a(cat(Vb[0], Vb[1]), P1, o);
        if constexpr (j < 7) { Vb[0] = trd(vp[0] + ((j + 1) * 2048 + 1024)); Vb[1] = trd(vp[1] + ((j + 1) * 2048 + 1024)); }
        SB();
      });
      asm volatile("s_nop 7\n\ts_nop 7" : "+v"(o) :: "memory");
    }
    {
      float mx = 0.f;
#pragma unroll
      for (int r = 0; r < 12; r += 2) mx = __builtin_fmaxf(__builtin_fmaxf(mx, __builtin_fabsf(o[r])), __builtin_fabsf(o[r + 1]));
      if (any_lane(!((o[12] >= 5.9604645e-8f) & (o[12] <= 16777216.f) & (mx < 3.0e38f)))) {
        float m = vmax(merged(), -3.0e38f);
#pragma nounroll
        for (int j = 1; j < 8; j++) m = vmax(scq(j * 2048, Q2), m);
        m = xmax(m);
        o = z;
        edge(merged(), m);
#pragma nounroll
        for (int j = 1; j < 8; j++) {
          const f32x16 s = scq(j * 2048, Q2);
          unsigned pk_[8];
#pragma unroll
          for (int r = 0; r < 16; r += 2) pk_[r >> 1] = pk2(__builtin_amdgcn_exp2f(s[r] - m), __builtin_amdgcn_exp2f(s[r + 1] - m));
          pv(j * 2048, pk_);
        }
      }
    }
    TS(23);
    const float inv = __builtin_amdgcn_rcpf(o[12]);
    const unsigned tb_ = (unsigned)((cg * 8 + wave) * 2 + (q32 >> 4)) * 68u + 48u;
    const unsigned oa = o0 + par + hi, ob = o0 + (par ? 0 : 2);
    st16<1>(u32x4{pk2(o[0] * inv, o[1] * inv), pk2(o[2] * inv, o[3] * inv), pk2(o[4] * inv, o[5] * inv), pk2(o[6] * inv, o[7] * inv)},
            (tb_ + (oa >> 2)) * 1024u + (oa & 3) * 256u + (q32 & 15) * 16u, sc, WS_AF);
    st8<1>(u32x2{pk2(o[8] * inv, o[9] * inv), pk2(o[10] * inv, o[11] * inv)},
           sel(hi, OOB, (tb_ + (ob >> 2)) * 1024u + (ob & 3) * 256u + (q32 & 15) * 16u + par * 8u), sc, WS_AF);
    asm volatile("s_waitcnt vmcnt(2)" : "+v"(hf), "+v"(fe), PINA(0), PINA(1), PINW(2), PINW(3), PINW(4) :: "memory");
    if (any_lane((lane < 8) & (hf != tag))) {
      wait_vm<0>();
      st4s(tag, sel(vl == 0, vfl, OOB), sc);
      poll_rest(hf, lane < 8, tag, vfh, sc);
    }
#pragma unroll
    for (int i = 0; i < 4; i++) ld_a(2, 2, i, vl);
  }
  const unsigned vl8 = sel(wave < 4, vl, OOB);
  unsigned vfo, vo[3], lo_[3], lo2[3];
  f32x4 fa[4][5];
  unsigned of = 0;
  u32x2 xr[3];
  TS(24);
  auto step = [&](auto J_) {
    constexpr int j = J_.v, wb = j < 6 ? j : j == 6 ? 1 : j == 7 ? 2 : 0, abuf = j & 3;
    if constexpr (j == 1) {
      wait_vm<4>();
      st4s(tag, sel(vl == 0, vfl, OOB), sc);
      TS(25);
    }
    if constexpr (j == 2) asm volatile("s_waitcnt vmcnt(10)" : PINA(2) :: "memory");
    if constexpr (j == 3) asm volatile("s_waitcnt vmcnt(14)" : PINA(3) :: "memory");
    if constexpr (j == 4) {
      asm volatile("s_waitcnt vmcnt(0)" : PINA(0), PINA(1), "+v"(of), PINW(0), PINW(5) :: "memory");
      TS(26);
      poll_rest(of, vfo != OOB, tag, vfo, sc);
      TS(27);
    }
    if constexpr (j == 6) asm volatile("s_waitcnt vmcnt(8)" : PINA(2) :: "memory");
    if constexpr (j == 7) asm volatile("s_waitcnt vmcnt(7)" : PINA(3) :: "memory");
    SB();
    sfor<0, 20>([&](auto K_) {
      constexpr int k = K_.v, u = k >> 2, i = k & 3;
      if constexpr (j == 0) mfma16z(RW[wb][u], A[abuf][i], fa[i][u]);
      else mfma16a(RW[wb][u], A[abuf][i], fa[i][u]);
      if constexpr (j >= 1 && j <= 3 && k < 4) ld_a((j + 2) & 3, j + 2, k, vl);
      if constexpr (j == 1 && k >= 4 && k < 9) ld_w3(5, 5, k - 4);
      if constexpr (j == 2 && k >= 4 && k < 9) RW[0][k - 4] = ld16<1>(vl8, sc, w3a(8, k - 4));
      if constexpr (j == 3 && k == 5) {
        const int l = lane_id(), h5 = 5 * (l & 31);
        auto ov = [&](int q) { return (h5 + 5 > 8 * q) & (h5 < 8 * q + 8); };
        vfo = sel(ov(wave) | ov(wave + 8) | ov(wave + 16), C_FO + (rt * 64u + l) * 128u, OOB);
        PIN(vfo);
      }
      if constexpr (j == 3 && k == 6) of = ld4<1>(vfo, sc, 0);
      if constexpr (j == 4 && k < 8) ld_a(2 + (k >> 2), 6 + (k >> 2), k & 3, vl);
      if constexpr (j == 5 && k < 4) ld_a(0, 8, k, vl8);
      if constexpr (j == 5 && k >= 8 && k < 13) RW[1][k - 8] = *(LAS u32x4*)(sm + (R_A + wave * 5120 + vl) + (k - 8) * 1024);
      if constexpr (j == 5 && k >= 13 && k < 18) RW[2][k - 13] = *(LAS u32x4*)(sm + (r7 + vl) + (k - 13) * 1024);
      if constexpr (j == 6 && k >= 2 && k < 14 && (k & 3) == 2) {
        constexpr int q = k >> 2;
        const unsigned c = ((wave < 4 ? 3 * wave : 4 + 2 * wave) + q) * 64u + lane_id(), row = c * 3277u >> 16, pos = c - row * 20u;
        vo[q] = sel(q == 2 && wave >= 4, OOB, (rt * 64u + row) * 1280u + cg * 160u + pos * 8u);
        const unsigned in = (row & 31) * 160u + ((pos ^ ((row >> 2) & 3)) * 8u);
        lo_[q] = R_A + (row >> 5) * 40960u + in;
        lo2[q] = sel(row >> 5, 0u, R_A + 20480u) + in;
        PIN(lo_[q]); PIN(lo2[q]);
        xr[q] = ld8<0>(vo[q], sx, 0);
      }
      SB();
    });
  };
  sfor<0, 8>(step);
  asm volatile("s_waitcnt vmcnt(0)" : PINA(0), "+v"(xr[0]), "+v"(xr[1]), "+v"(xr[2]) :: "memory");
  if (wave < 4) step(IC<8>{});
  asm volatile("s_nop 7" ::: "memory");
  SB();
  TS(28);
  lane = lane_id();
  const int t16 = lane & 15, g = lane >> 4;
  {
    const unsigned pin = t16 * 160 + ((g ^ (t16 >> 2)) * 8);
    LAS char* pw0 = sm + (R_A + wave * 5120 + pin);
    LAS char* pw1 = sm + (r7 + pin);
#pragma unroll
    for (int i = 0; i < 4; i++)
#pragma unroll
      for (int u = 0; u < 5; u++)
        *(LAS u32x2*)((i >> 1 ? pw1 : pw0) + ((i & 1) * 2560 + u * 32)) = u32x2{pk2(fa[i][u][0], fa[i][u][1]), pk2(fa[i][u][2], fa[i][u][3])};
    WAIT_LGKM0();
    bar();
    const i32x4 sout = mk_srd(p.out, X_BYTES);
    unsigned slo = 0x00003f80u, shi = 0x3f800000u;
    asm volatile("" : "+s"(slo), "+s"(shi));
    auto fin = [&](int q) {
      float a0 = blo(xr[q][0]), a1 = bhi(xr[q][0]), a2 = blo(xr[q][1]), a3 = bhi(xr[q][1]);
#pragma unroll
      for (int w = 0; w < 8; w++) {
        const u32x2 v = *(LAS u32x2*)(sm + (w < 4 ? lo_[q] : lo2[q]) + (w & 3) * 5120);
        a0 = dot2(v[0], slo, a0); a1 = dot2(v[0], shi, a1); a2 = dot2(v[1], slo, a2); a3 = dot2(v[1], shi, a3);
      }
      st8<0>(u32x2{pk2(a0, a1), pk2(a2, a3)}, vo[q], sout, 0);
    };
    fin(0); fin(1);
    if (wave < 4) fin(2);
  }
  TSOUT();
  __builtin_amdgcn_endpgm();
  };

  auto phase1 = [&](auto SP_) {
    constexpr int SP = SP_.v;
    u32x4 X[32], xt[2];
    f32x16 acc[3], cs;
    u32x2 wf[2][3][2], w2[4][2], pk[2], gs, gc[3];
    u32x4 As, Bc;
    float sd, rs, rg;
    unsigned pf, nm, tch = 0;
    float e[10], sacc = 0.f, mean = 0.f, cp[4];
    auto stat = [&](int k) {
      if (k < 5) { const unsigned w = k < 4 ? r0[k & 3] : r1; e[2 * k] = blo(w); e[2 * k + 1] = bhi(w); sacc += e[2 * k] + e[2 * k + 1]; }
      else if (k >= 11 && k < 16) {
        const float d0 = e[2 * k - 22] - mean, d1 = e[2 * k - 21] - mean;
        sacc = __builtin_fmaf(d0, d0, sacc); PIN(sacc); sacc = __builtin_fmaf(d1, d1, sacc);
      } else if (k < 22) {
        const int q = k < 11 ? k - 5 : k - 16;
        if (q == 0) sacc += dppf<0x128>(sacc);
        else if (q == 1) sacc += dppf<0x124>(sacc);
        else if (q == 2) sacc += dppf<0x122>(sacc);
        else if (q == 3) sacc += dppf<0x121>(sacc);
        else if (q == 4) { auto a = __builtin_amdgcn_permlane16_swap(__float_as_uint(sacc), __float_as_uint(sacc), false, false); sacc = __uint_as_float(a[0]) + __uint_as_float(a[1]); }
        else {
          auto a = __builtin_amdgcn_permlane32_swap(__float_as_uint(sacc), __float_as_uint(sacc), false, false);
          sacc = (__uint_as_float(a[0]) + __uint_as_float(a[1])) * (1.0f / 640.0f);
          if (k == 10) { mean = sacc; sacc = 0.f; PIN(mean); }
        }
      } else if (k == 22) sacc = __builtin_amdgcn_sqrtf(rbf(rbf(sacc) + 1e-5f));
      PIN(sacc);
    };
    auto rdw = [&](int n, int t) {
      wf[n & 1][t][0] = trd(wa[t] + (LW(n >> 2) + (n & 3) * 2048));
      wf[n & 1][t][1] = trd(wa[t] + (LW(n >> 2) + (n & 3) * 2048 + 512));
    };
    auto rdx = [&](int n) {
      const u32x4 v = *(LAS u32x4*)(xa[n & 3] + ((n >> 2) % 3) * 16384);
      if (n < 32) X[n] = v; else xt[n & 1] = v;
    };
    auto rdc = [&](int s) { pk[0] = trd(wc + LW(s)); pk[1] = trd(wc + (LW(s) + 512)); };
    auto wfr = [&](int n, int t) { return cat(wf[n & 1][t][0], wf[n & 1][t][1]); };
    const unsigned vl8 = lane * 8u;
    auto ld_gran = [&](int k) {
      if (k == 0) gs = ld8<1>((vl8 & 56u) | (vl8 & 192u) << 1, sc, C_STAT + Rq * 512u);
      else if (k < 4) gc[k - 1] = ld8<1>(vl8, sc, csb + (k - 1) * 1024u);
      else pf = ld4<1>((vl8 & 248u) << 4, sc, C_PF + wave * 4096u);
    };
    auto split3 = [&](unsigned g) {
      const float c = __uint_as_float(g), r1_ = c - rbf(c);
      const unsigned a0 = pk2(c, r1_);
      u32x4 a = {a0, pk2(r1_ - bhi(a0), 0.f), 0u, 0u};
      PIN(a);
      return a;
    };
    auto mk_bc = [&]() { u32x4 b = {nm | nm << 16, nm, 0u, 0u}; PIN(b); return b; };
    auto prep = [&](int k) {
      if (k == 0) {
        if (any_lane((gs[1] != tag) | (gc[0][1] != tag) | (gc[1][1] != tag) | (gc[2][1] != tag))) {
          for (int it = 0; it < SPIN; it++) {
            __builtin_amdgcn_s_sleep(1);
#pragma unroll
            for (int i = 0; i < 4; i++) ld_gran(i);
            asm volatile("s_waitcnt vmcnt(0)" : "+v"(gs), "+v"(gc[0]), "+v"(gc[1]), "+v"(gc[2]) :: "memory");
            if (!any_lane((gs[1] != tag) | (gc[0][1] != tag) | (gc[1][1] != tag) | (gc[2][1] != tag))) break;
          }
        }
      } else if (k == 1) {
        nm = (gs[0] & 0xffffu) ^ 0x8000u; PIN(nm);
        Bc = mk_bc();
        sd = bhi(gs[0]); PIN(sd); rs = __builtin_amdgcn_rcpf(sd); PIN(rs);
      } else if (k == 8) { rg = -1.44269504089f * rs; PIN(rg); }
      else if (k < (SP ? 4 : 8)) { if (k & 1) mfma32a(As, Bc, acc[(k - 3) >> 1]); else As = split3(gc[(k - 2) >> 1][0]); }
    };
#pragma unroll
    for (int r = 0; r < 16; r++) cs[r] = 0.f;
    PIN(cs);
    u32x4 on1 = ones;
    PIN(on1);
    wait_vm<8>();
    bar();
    TS(2);
    rdx(0);
#pragma unroll
    for (int t = 0; t < 3; t++) rdw(0, t);
    rdx(1);
#pragma unroll
    for (int t = 0; t < 3; t++) rdw(1, t);
    rdc(0);
    SB();
    sfor<0, 120>([&](auto I_) {
      constexpr int n = I_.v / 3, t = I_.v % 3, s = n >> 2, ks = n & 3;
      constexpr int sb = (I_.v + 4) / 12 - 1, m = (I_.v + 4) % 12;
      const u32x4 xf = n < 32 ? X[n < 32 ? n : 0] : xt[n & 1];
      if constexpr (SP && t > 0) { if constexpr (n == 0) mfma32z(on1, wfr(n, t), acc[t]); else mfma32a(on1, wfr(n, t), acc[t]); }
      else { if constexpr (n == 0) mfma32z(wfr(n, t), xf, acc[t]); else mfma32a(wfr(n, t), xf, acc[t]); }
      if constexpr (m == 11) {
        SB();
        WAIT_LGKM0();
        if constexpr (s == 0) { asm volatile("s_waitcnt vmcnt(6)" : "+v"(ep), "+v"(r0), "+v"(r1) :: "memory"); tag = __builtin_amdgcn_readfirstlane(ep) + 1u; }
        else if constexpr (s == 1) wait_vm<5>();
        else if constexpr (s == 2) wait_vm<6>();
        else if constexpr (s == 3) wait_vm<7>();
        else if constexpr (s == 4) wait_vm<6>();
        else if constexpr (s == 5) wait_vm<7>();
        else if constexpr (s == 6) wait_vm<5>();
        else if constexpr (s == 7) wait_vm<12>();
        else if constexpr (s == 8) asm volatile("s_waitcnt vmcnt(6)" : "+v"(gs), "+v"(gc[0]), "+v"(gc[1]), "+v"(gc[2]) :: "memory");
        else asm volatile("s_waitcnt vmcnt(4)" : "+v"(tch) :: "memory");
        bar();
        TS(3 + s);
        SB();
        if constexpr (n < 38) { rdw(n + 2, 0); rdw(n + 2, 1); }
      } else if constexpr (n < 38) {
        if constexpr (m != 10) rdw(n + 2, t);
        if constexpr (t == 2) rdx(n + 2);
      } else if constexpr (n == 39) { w2[t][0] = trd(wr[t & 1] + (t >> 1) * 2048); w2[t][1] = trd(wr[t & 1] + ((t >> 1) * 2048 + 512)); }
      if constexpr (sb >= 0 && sb < 4 && m == 0) rdc(sb + 1);
      if constexpr ((sb == -1 && m == 6) || (sb >= 0 && sb < 4 && m == 3))
        asm volatile("s_cmp_gt_u32 %3, 2\n\ts_cbranch_scc1 .Lce%=\n\tv_mfma_f32_32x32x16_bf16 %0, %1, %2, %0\n.Lce%=:"
                     : "+v"(cs) : "v"(on1), "v"(cat(pk[0], pk[1])), "s"(cst) : "scc");
      if constexpr (sb == 3 && m == 8) { PIN(cs); *(LAS float*)(sm + L_CSR + wave * 128 + (lane & 31) * 4) = cs[0]; }
      if constexpr (sb == 4 && m < 4) cp[m] = *(LAS float*)(sm + L_CSR + cw * 512 + m * 128 + (lane & 31) * 4);
      if constexpr (sb == 4 && m == 4)
        st8<1>(u32x2{__float_as_uint((cp[0] + cp[1]) + (cp[2] + cp[3])), tag}, sel(vl8 < (cst < 3 && mw == 0 ? 256u : 0u), vl8, OOB), sc, csb + cst * 1024u + (xcd & 1) * 256u);
      if constexpr (sb == 0 || sb == 1) stat(sb * 12 + m);
      if constexpr (sb == 2 && m == 1) *(LAS unsigned*)(sm + L_CSR + 2048 + wave * 256 + lane * 4) = pk2(mean, sacc);
      if constexpr (sb == 3 && m == 1) pf = *(LAS unsigned*)(sm + L_CSR + 2048 + (lane & 7) * 256);
      if constexpr (sb == 3 && m == 4) st8<1>(u32x2{pf, tag}, sel(vl8 < (wave == 0 ? 64u : 0u), vl8, OOB), sc, C_STAT + (rb * 16 + jg) * 128u);
      if constexpr ((sb == 1 || sb == 4) && (m == 5 || m == 6)) pk[m - 5] = trd(pkr + (m - 5) * 1024);
      if constexpr (sb == 1 && m == 10) st16<1>(cat(pk[0], pk[1]), vl, sc, WS_W3 + (kA * 40u + cA * 8u + wave) * 1024u);
      if constexpr (sb == 4 && m == 10) st16<1>(cat(pk[0], pk[1]), vl | offB, sc, WS_W3 + (kB * 40u + cB * 8u + wave) * 1024u);
      if constexpr (sb == 2 && m == 10) dma16(ldw + L_PK, vpk | offB, mk_srd(p.wo, WO_BYTES), (kB - 48) * 40960u + cB * 256u);
      if constexpr (sb == -1 && m < 9) issue(2, m - 4);
      if constexpr (sb == -1 && m == 9) dma16(ldw + L_PK, vpk, sel_srd(kA < 48, sdn, so), (kA < 48 ? kA : kA - 48) * 40960u + cA * 256u);
      if constexpr (sb == 6 && m == 0) st4s(tag, sel(vl8 == 0, wave == 0 ? C_PF + bid * 128u : OOB, OOB), sc);
      if constexpr (sb >= 0 && sb < 7 && (m & 1) && m < 10) issue(sb + 3, m >> 1);
      if constexpr (sb == 6 && m == 10) { issue_g(0, 0); issue_g(0, 1); ld_gran(0); ld_gran(1); ld_gran(2); ld_gran(3); }
      if constexpr (sb == 7 && !(m & 1)) issue_g(1 + m / 4, (m >> 1) & 1);
      if constexpr (sb == 8 && (m & 1) && m < 9) issue_g(4 + m / 4, (m >> 1) & 1);
#define TOUCH 0
      if constexpr (TOUCH && sb == 8 && m == 0) tch = ld4<1>(0u, sc, C_PF + (wave * 32 + loc) * 128u);
      if constexpr (sb == 9 && m == 0) ld_gran(4);
      if constexpr (sb == 9 && m > 0 && m < 3) { issue_g(5 + m, 0); issue_g(5 + m, 1); }
      if constexpr (sb == 8 && m < 9) prep(m);
      SB();
    });
    TS(13);
    auto round2 = [&](auto TY_) {
      constexpr int TY = TY_.v;
      f32x16 ac2[2];
      u32x4 qv, hv[2], A4[2];
      u32x2 gc2[2];
      float t[8], rq;
      unsigned fq, vf, vh;
      bool late;
      auto qop = [&](auto U_, auto K_) {
        constexpr int u = U_.v, k = K_.v;
        const unsigned ln = 4 * pr + tyB + u;
        if constexpr (k == 0) {
          const float qs = ln < 10u ? 0.22360679775f * 1.44269504089f : 1.0f;
          asm volatile("v_mul_f32 %0, %1, %2" : "=v"(rq) : "s"(qs), "v"(rs));
        } else {
          constexpr int h = (k - 1) / 13, j = (k - 1) % 13;
          if constexpr (j < 8) t[j] = v_mul(acc[u][8 * h + j], rq);
          else if constexpr (j < 12) qv[j - 8] = v_cvt(t[2 * j - 16], t[2 * j - 15]);
          else st16<1>(qv, vl, sc, WS_QKV + (Rq * 240u + ln * 8u + cw * 4u + h * 2u) * 512u);
        }
      };
      auto gop = [&](f32x16& a, u32x4& o, auto K_) {
        constexpr int k = K_.v, ph = k >> 3, j = k & 7;
        if constexpr (ph == 0) t[j] = v_mul(a[j], rg);
        else if constexpr (ph == 1) t[j] = v_exp(t[j]);
        else if constexpr (ph == 2) t[j] = v_fma(t[j], sd, sd);
        else if constexpr (ph == 3) t[j] = v_rcp(t[j]);
        else if constexpr (ph == 4) t[j] = v_mul(t[j], a[8 + j]);
        else o[j] = v_cvt(t[2 * j], t[2 * j + 1]);
      };
      auto st_h = [&](u32x4 v, unsigned gl, int a) { st16<1>(v, vh, sc, WS_AF + ((rb * 8 + 2 * mw) * 68u + 2 * gl + cw) * 1024u + a * 512u); };
      auto epi = [&](auto K_) {
        constexpr int k = K_.v;
        if constexpr (k < 27) qop(IC<0>{}, K_);
        else if constexpr (TY == 1) { if constexpr (k < 81) qop(IC<k / 27>{}, IC<k % 27>{}); }
        else if constexpr (TY == 0) {
          if constexpr (k < 117) {
            constexpr int u = (k - 27) / 45, q = (k - 27) % 45;
            if constexpr (q < 44) gop(acc[1 + u], hv[u], IC<q>{}); else st_h(hv[u], gl1, u);
          }
        } else if constexpr (k < 29) {
          PIN(acc[k - 26]);
          st8<1>(u32x2{__float_as_uint(acc[k - 26][0] * 0.5f), tag}, sel(mw == 0, vl8, OOB), sc, C_CS + (rb * 10 + cw) * 512u + (k - 24) * 1024u);
        }
      };
      constexpr auto EK = [](int q) { return q < 1 ? 0 : (TY == 0 && q > 7) ? cl(30 + (q - 7) * 3, 0, 117) : cl((q - 1) * 5, 0, 117); };
      sfor<0, 80>([&](auto M_) {
        constexpr int m = M_.v;
        constexpr auto KK = [](int q) { return q < 64 ? q >> 1 : 32 + (q & 7); };
        constexpr auto AA = [](int q) { return q < 64 ? q & 1 : (q >> 3) & 1; };
        constexpr int kk = KK(m), a = AA(m), g = kk >> 2;
        if constexpr (TOUCH && m == 1) {
          const unsigned un = wave * 32 + loc, vt = sel(tch == tag, vl8 << 4, OOB);
          asm volatile("buffer_load_dword %0, %1, %2, %3 offen sc1" : "=&v"(tch) : "v"(vt), "s"(sc), "s"(WS_W3 + un * 8192u) : "memory");
          asm volatile("buffer_load_dword %0, %1, %2, %3 offen sc1" : "+v"(tch) : "v"(vt | (un < 84 ? 0u : OOB)), "s"(sc), "s"(WS_W3 + (256 + un) * 8192u) : "memory");
        }
        if constexpr (m == 16) {
          asm volatile("s_waitcnt vmcnt(%1)" : "+v"(pf) : "i"((TY == 1 ? 5 : TY == 0 ? 2 : 4) + 2 * TOUCH) : "memory");
          poll_rest(pf, true, tag, C_PF + wave * 4096u + ((vl8 & 248u) << 4), sc);
          bar();
          TS(14);
          st4s(tag, sel(vl8 == 0, (bid | wave) == 0 ? 0u : OOB, OOB), sc);
          SB();
        }
        if constexpr (m == 44) {
          asm volatile("s_waitcnt vmcnt(%1)" : "+v"(tch) : "i"(TY == 0 ? 11 : 10) : "memory");
          st4s(tag, sel(vl8 == 0, C_FQ + fme, OOB), sc);
          TS(15);
          SB();
        }
        if constexpr (m == 48) { bar(); SB(); }
        if constexpr (m == 50) {
          {
            const int l = lane_id(), sec = (l + 2) >> 2, fr = sel(l < 2, ch * 8 + wave, ch * 8 - 8 + 2 * wave + ((l >> 1) & 1));
            const unsigned oc = sec * 80u + o0 + (l & 1) * 2u, jp = (oc >> 5) * 2 + ((oc & 24u) != 0);
            vf = sel((l < 10) & (fr >= 0), C_FQ + ((jp * 16 + (fr >> 2)) * 8 + (oc & 4u) + (fr & 3)) * 128u, OOB);
          }
          gc2[0] = ld8<1>(vl8, sc, csb + 3072u); gc2[1] = ld8<1>(vl8, sc, csb + 4096u);
          fq = ld4<1>(vf, sc, 0);
          SB();
        }
        if constexpr (m == 59) {
          asm volatile("s_waitcnt vmcnt(0)" : "+v"(fq), "+v"(gc2[0]), "+v"(gc2[1]) :: "memory");
          TS(16);
          if constexpr (TY == 0) st4s(tag, sel(vl8 == 0, C_FE + fme, OOB), sc);
          if (any_lane((gc2[0][1] != tag) | (gc2[1][1] != tag))) {
            for (int it = 0; it < SPIN; it++) {
              __builtin_amdgcn_s_sleep(1);
              gc2[0] = ld8<1>(vl8, sc, csb + 3072u); gc2[1] = ld8<1>(vl8, sc, csb + 4096u);
              asm volatile("s_waitcnt vmcnt(0)" : "+v"(gc2[0]), "+v"(gc2[1]) :: "memory");
              if (!any_lane((gc2[0][1] != tag) | (gc2[1][1] != tag))) break;
            }
          }
          late = any_lane((vf != OOB) & (fq != tag));
          fq = ld4<1>(vf, sc, 0);
          if (!late) kv_issue();
          SB();
        }
        if constexpr (m == 68) {
          if (late) {
            asm volatile("s_waitcnt vmcnt(1)" : "+v"(fq) :: "memory");
            poll_rest(fq, vf != OOB, tag, vf, sc);
            kv_issue();
          }
          q_issue();
          TS(17);
          SB();
        }
        const u32x4 xf = (g == 2 || g == 3) ? xt[kk & 1] : X[(g == 2 || g == 3) ? 0 : KS(g) * 4 + (kk & 3)];
        if constexpr (m < 2) mfma32z(cat(w2[m & 3][0], w2[m & 3][1]), xf, ac2[a]);
        else mfma32a(cat(w2[m & 3][0], w2[m & 3][1]), xf, ac2[a]);
        if constexpr (m + 3 < 80) {
          constexpr int k2 = KK(m + 3), a2 = AA(m + 3), g2 = k2 >> 2, gp = (k2 - 1) >> 2;
          if constexpr ((LG(g2) >> 16) != (LG(gp) >> 16)) { wr[a2] += (int)(LG(g2) & ~65535u) - (int)(LG(gp) & ~65535u); PIN(wr[a2]); }
          w2[(m + 3) & 3][0] = trd(wr[a2] + ((LG(g2) & 65535u) + (k2 & 3) * 2048));
          w2[(m + 3) & 3][1] = trd(wr[a2] + ((LG(g2) & 65535u) + (k2 & 3) * 2048 + 512));
          if constexpr (a2 == 0 && (g2 == 2 || g2 == 3)) xt[k2 & 1] = *(LAS u32x4*)(xa[k2 & 3] + (KS(g2) % 3) * 16384);
        }
        if constexpr (m == 8) { const unsigned l = lane_id(); vh = (l & 15) * 16u + ((l >> 4) & 1) * 69632u + (l >> 5) * 256u; PIN(vh); }
        sfor<EK(m), EK(m + 1)>(epi);
        if constexpr (m == 16 || m == 17) { issue_g(m - 8, 0); issue_g(m - 8, 1); }
        if constexpr (m >= 24 && m < 44 && !(m & 1)) ld_w3((m - 24) / 10, (m - 24) / 10, ((m - 24) >> 1) % 5);
        if constexpr (m >= 52 && m < 55) att_addr(m - 52);
        if constexpr (m >= 65 && m < 73) att_mask(m - 65);
        if constexpr (m == 60 || m == 61) A4[m - 60] = split3(gc2[m - 60][0]);
        if constexpr (m == 62) Bc = mk_bc();
        if constexpr (m == 64 || m == 65) mfma32a(A4[m - 64], Bc, ac2[m - 64]);
        if constexpr (m == 66 || (m >= 70 && m < 78 && !(m & 1))) ld_w3(2, 2, m == 66 ? 0 : (m - 68) >> 1);
        if constexpr (m >= 73) sfor<(m - 73) * 7, cl((m - 72) * 7, 0, 44)>([&](auto K_) { gop(ac2[0], hv[0], K_); });
        SB();
      });
      TS(18);
      fe = ld4<1>(sel(vl8 < 32u, C_FE + fpo(), OOB), sc, 0);
      st_h(hv[0], gl2, 0);
      asm volatile("s_nop 7" ::: "memory");
      sfor<0, 44>([&](auto K_) { gop(ac2[1], hv[1], K_); });
      st_h(hv[1], gl2, 1);
      u32x4 (&rw)[6][5] = RW; u32x4 &q1 = Q1, &q2 = Q2;
      asm volatile("s_waitcnt vmcnt(7)" : "+v"(q1), "+v"(q2), "+v"(fq), "+v"(rw[0][0]), "+v"(rw[0][1]), "+v"(rw[0][2]), "+v"(rw[0][3]), "+v"(rw[0][4]),
                   "+v"(rw[1][0]), "+v"(rw[1][1]), "+v"(rw[1][2]), "+v"(rw[1][3]), "+v"(rw[1][4]) :: "memory");
      TS(19);
      rest();
    };
    if constexpr (SP) round2(IC<2>{});
    else { if (tyB) round2(IC<1>{}); else round2(IC<0>{}); }
  };
  if (spec) phase1(IC<1>{}); else phase1(IC<0>{});
}

extern "C" void kernel_launch(void* const* d_in, const int* in_sizes, int n_in, void* d_out, int out_size, void* d_ws,
                              size_t ws_size, hipStream_t stream) {
  Params p;
  p.x = (const char*)d_in[0]; p.wqkv = (const char*)d_in[1]; p.wo = (const char*)d_in[2];
  p.wg = (const char*)d_in[4]; p.wu = (const char*)d_in[5]; p.wd = (const char*)d_in[6];
  p.out = (char*)d_out; p.ws = (char*)d_ws;
  (void)hipFuncSetAttribute((const void*)k_fused, hipFuncAttributeMaxDynamicSharedMemorySize, LDS_BYTES);
  k_fused<<<256, 512, LDS_BYTES, stream>>>(p);
}
```

```cpp
#include <hip/hip_runtime.h>

typedef int i32x4 __attribute__((ext_vector_type(4)));
typedef unsigned u32x4 __attribute__((ext_vector_type(4)));
typedef unsigned u32x2 __attribute__((ext_vector_type(2)));
typedef float f32x4 __attribute__((ext_vector_type(4)));
typedef float f32x2 __attribute__((ext_vector_type(2)));
typedef float f32x16 __attribute__((ext_vector_type(16)));
typedef __bf16 bf16x8 __attribute__((ext_vector_type(8)));
typedef __bf16 bf16x2 __attribute__((ext_vector_type(2)));
typedef short s16x4 __attribute__((ext_vector_type(4)));
#define LAS __attribute__((address_space(3)))
#define DEVI __device__ __forceinline__

constexpr int S_ = 2048, D_ = 640, F_ = 1536;
constexpr unsigned X_BYTES = S_ * D_ * 2, WQ_BYTES = D_ * 3 * D_ * 2, WF_BYTES = D_ * F_ * 2, WO_BYTES = D_ * D_ * 2;
constexpr unsigned QKV_BYTES = S_ * 3 * D_ * 2;
constexpr unsigned AF_BYTES = 128u * 68u * 1024u;
constexpr unsigned W3_BYTES = 68u * 40u * 1024u;
constexpr unsigned WS_QKV = 2u << 20, WS_AF = 16u << 20, WS_W3 = 32u << 20, WS_BYTES = 40u << 20;
constexpr unsigned C_PF = 0x8000, C_STAT = 0x10000, C_CS = 0x20000, C_FQ = 0x40000, C_FH = 0x80000, C_FO = 0xC0000, C_FE = 0x100000;
constexpr unsigned LDS_BYTES = 163840;
constexpr unsigned OOB = 0x80000000u;

DEVI i32x4 mk_srd(const void* p, unsigned n) {
  unsigned long long a = (unsigned long long)p;
  i32x4 r;
  r.x = __builtin_amdgcn_readfirstlane((int)(unsigned)a);
  r.y = __builtin_amdgcn_readfirstlane((int)(unsigned)(a >> 32));
  r.z = (int)n; r.w = 0x00020000;
  return r;
}
DEVI i32x4 sel_srd(bool c, i32x4 a, i32x4 b) { i32x4 r; r.x = c ? a.x : b.x; r.y = c ? a.y : b.y; r.z = c ? a.z : b.z; r.w = a.w; return r; }
template <int SC = 0> DEVI void dma16(unsigned ldsaddr, unsigned voff, i32x4 srd, unsigned soff) {
  if (SC) asm volatile("s_mov_b32 m0, %0\n\ts_nop 0\n\tbuffer_load_dwordx4 %1, %2, %3 offen sc1 lds"
                       :: "s"(ldsaddr), "v"(voff), "s"(srd), "s"(soff) : "memory");
  else    asm volatile("s_mov_b32 m0, %0\n\ts_nop 0\n\tbuffer_load_dwordx4 %1, %2, %3 offen lds"
                       :: "s"(ldsaddr), "v"(voff), "s"(srd), "s"(soff) : "memory");
}
template <int SC, int OFF = 0> DEVI u32x4 ld16(unsigned voff, i32x4 srd, unsigned soff) {
  u32x4 r;
  if (SC) asm volatile("buffer_load_dwordx4 %0, %1, %2, %3 offen offset:%4 sc1" : "=v"(r) : "v"(voff), "s"(srd), "s"(soff), "i"(OFF) : "memory");
  else    asm volatile("buffer_load_dwordx4 %0, %1, %2, %3 offen offset:%4" : "=v"(r) : "v"(voff), "s"(srd), "s"(soff), "i"(OFF) : "memory");
  return r;
}
template <int SC, int OFF = 0> DEVI u32x2 ld8(unsigned voff, i32x4 srd, unsigned soff) {
  u32x2 r;
  if (SC) asm volatile("buffer_load_dwordx2 %0, %1, %2, %3 offen offset:%4 sc1" : "=v"(r) : "v"(voff), "s"(srd), "s"(soff), "i"(OFF) : "memory");
  else    asm volatile("buffer_load_dwordx2 %0, %1, %2, %3 offen offset:%4" : "=v"(r) : "v"(voff), "s"(srd), "s"(soff), "i"(OFF) : "memory");
  return r;
}
template <int SC, int OFF = 0> DEVI unsigned ld4(unsigned voff, i32x4 srd, unsigned soff) {
  unsigned r;
  if (SC) asm volatile("buffer_load_dword %0, %1, %2, %3 offen offset:%4 sc1" : "=v"(r) : "v"(voff), "s"(srd), "s"(soff), "i"(OFF) : "memory");
  else    asm volatile("buffer_load_dword %0, %1, %2, %3 offen offset:%4" : "=v"(r) : "v"(voff), "s"(srd), "s"(soff), "i"(OFF) : "memory");
  return r;
}
template <int SC> DEVI void st16(u32x4 v, unsigned voff, i32x4 srd, unsigned soff) {
  if (SC) asm volatile("buffer_store_dwordx4 %0, %1, %2, %3 offen sc1\n\ts_nop 1" :: "v"(v), "v"(voff), "s"(srd), "s"(soff) : "memory");
  else    asm volatile("buffer_store_dwordx4 %0, %1, %2, %3 offen\n\ts_nop 1" :: "v"(v), "v"(voff), "s"(srd), "s"(soff) : "memory");
}
template <int SC, int OFF = 0> DEVI void st8(u32x2 v, unsigned voff, i32x4 srd, unsigned soff) {
  if (SC) asm volatile("buffer_store_dwordx2 %0, %1, %2, %3 offen offset:%4 sc1" :: "v"(v), "v"(voff), "s"(srd), "s"(soff), "i"(OFF) : "memory");
  else    asm volatile("buffer_store_dwordx2 %0, %1, %2, %3 offen offset:%4" :: "v"(v), "v"(voff), "s"(srd), "s"(soff), "i"(OFF) : "memory");
}
DEVI void st4s(unsigned v, unsigned voff, i32x4 srd) {
  asm volatile("buffer_store_dword %0, %1, %2, 0 offen sc1" :: "v"(v), "v"(voff), "s"(srd) : "memory");
}
template <int N> DEVI void wait_vm() { asm volatile("s_waitcnt vmcnt(%0)" :: "i"(N) : "memory"); }
#define WAIT_LGKM0() asm volatile("s_waitcnt lgkmcnt(0)" ::: "memory")
DEVI void bar() { asm volatile("s_barrier" ::: "memory"); }
#define SB() __builtin_amdgcn_sched_barrier(0)
#define PIN(x) asm volatile("" : "+v"(x))

DEVI unsigned pk2(float a, float b) { bf16x2 v = {(__bf16)a, (__bf16)b}; return __builtin_bit_cast(unsigned, v); }
DEVI float rbf(float a) { return (float)(__bf16)a; }
DEVI bf16x8 asbf(u32x4 v) { return __builtin_bit_cast(bf16x8, v); }
DEVI u32x2 trd(LAS char* p) { return __builtin_bit_cast(u32x2, __builtin_amdgcn_ds_read_tr16_b64_v4i16((LAS s16x4*)p)); }
DEVI u32x4 cat(u32x2 a, u32x2 b) { return __builtin_shufflevector(a, b, 0, 1, 2, 3); }
DEVI f32x4 mfma16(u32x4 a, u32x4 b, f32x4 c) { return __builtin_amdgcn_mfma_f32_16x16x32_bf16(asbf(a), asbf(b), c, 0, 0, 0); }
DEVI void mfma16a(u32x4 a, u32x4 b, f32x4& c) { asm volatile("v_mfma_f32_16x16x32_bf16 %0, %1, %2, %0" : "+v"(c) : "v"(a), "v"(b)); }
DEVI void mfma16z(u32x4 a, u32x4 b, f32x4& c) { asm volatile("v_mfma_f32_16x16x32_bf16 %0, %1, %2, 0" : "=&v"(c) : "v"(a), "v"(b)); }
DEVI void mfma32a(u32x4 a, u32x4 b, f32x16& c) { asm volatile("v_mfma_f32_32x32x16_bf16 %0, %1, %2, %0" : "+v"(c) : "v"(a), "v"(b)); }
DEVI void mfma32z(u32x4 a, u32x4 b, f32x16& c) { asm volatile("v_mfma_f32_32x32x16_bf16 %0, %1, %2, 0" : "=&v"(c) : "v"(a), "v"(b)); }
DEVI int lane_id() { int l; asm volatile("v_mbcnt_lo_u32_b32 %0, -1, 0\n\tv_mbcnt_hi_u32_b32 %0, -1, %0" : "=v"(l)); return l; }
DEVI f32x16 mfma32(u32x4 a, u32x4 b, f32x16 c) { return __builtin_amdgcn_mfma_f32_32x32x16_bf16(asbf(a), asbf(b), c, 0, 0, 0); }
DEVI float blo(unsigned r) { return __uint_as_float(r << 16); }
DEVI float bhi(unsigned r) { return __uint_as_float(r & 0xffff0000u); }
template <int C> DEVI float dppf(float v) { return __uint_as_float(__builtin_amdgcn_update_dpp(0, __float_as_uint(v), C, 0xf, 0xf, false)); }
DEVI float wave_sum(float v) {
  v += dppf<0x128>(v); v += dppf<0x124>(v); v += dppf<0x122>(v); v += dppf<0x121>(v);
  auto a = __builtin_amdgcn_permlane16_swap(__float_as_uint(v), __float_as_uint(v), false, false);
  v = __uint_as_float(a[0]) + __uint_as_float(a[1]);
  auto b = __builtin_amdgcn_permlane32_swap(__float_as_uint(v), __float_as_uint(v), false, false);
  return __uint_as_float(b[0]) + __uint_as_float(b[1]);
}
DEVI float v_fma(float a, float b, float c) { float r; asm volatile("v_fma_f32 %0, %1, %2, %3" : "=v"(r) : "v"(a), "v"(b), "v"(c)); return r; }
DEVI float v_mul(float a, float b) { float r; asm volatile("v_mul_f32 %0, %1, %2" : "=v"(r) : "v"(a), "v"(b)); return r; }
DEVI float v_exp(float a) { float r; asm volatile("v_exp_f32 %0, %1" : "=v"(r) : "v"(a)); return r; }
DEVI float v_rcp(float a) { float r; asm volatile("v_rcp_f32 %0, %1" : "=v"(r) : "v"(a)); return r; }
DEVI unsigned v_cvt(float a, float b) { unsigned r; asm volatile("v_cvt_pk_bf16_f32 %0, %1, %2" : "=v"(r) : "v"(a), "v"(b)); return r; }
DEVI float dot2(unsigned w, unsigned sel, float c) { return __builtin_amdgcn_fdot2_f32_bf16(__builtin_bit_cast(bf16x2, w), __builtin_bit_cast(bf16x2, sel), c, false); }
DEVI unsigned sel(bool c, unsigned a, unsigned b) { return c ? a : b; }
DEVI bool any_lane(bool c) { return __builtin_amdgcn_ballot_w64(c) != 0; }
constexpr int SPIN = 1 << 15;
DEVI void poll_rest(unsigned v, bool need, unsigned tag, unsigned voff, i32x4 srd) {
  if (any_lane(need & (v != tag))) {
    for (int it = 0; it < SPIN; it++) {
      __builtin_amdgcn_s_sleep(1);
      v = ld4<1>(voff, srd, 0);
      asm volatile("s_waitcnt vmcnt(0)" : "+v"(v) :: "memory");
      if (!any_lane(need & (v != tag))) break;
    }
  }
}

#define TS(i)
#define TSDECL
#define TSOUT()
template <int I> struct IC { static constexpr int v = I; };
template <int B, int E, class F> DEVI void sfor(F&& f) { if constexpr (B < E) { f(IC<B>{}); sfor<B + 1, E>(f); } }

struct Params {
  const char *x, *wqkv, *wo, *wg, *wu, *wd;
  char *out, *ws;
};

extern __shared__ __attribute__((aligned(16))) char smem_[];

constexpr unsigned UN = 8192;
constexpr unsigned LW(int s) { return s % 3 == 0 ? 0 : s % 3 == 1 ? 4 * UN : 7 * UN; }
constexpr unsigned L_PK = 3 * UN, L_X = 10 * UN, L_CSR = 16 * UN;
constexpr unsigned LG(int g) { return (g == 0 || g == 8 ? 16 : g == 1 || g == 9 ? 18 : g == 2 ? 4 : g == 3 ? 12 : g == 4 ? 6 : g == 5 ? 8 : g == 6 ? 0 : 2) * UN; }
constexpr int KS(int g) { return g < 2 ? g : g < 4 ? g + 6 : g - 2; }
constexpr unsigned AT_K = 4 * UN, AT_V = 8 * UN, R_A = 12 * UN;
constexpr int cl(int v, int lo, int hi) { return v < lo ? lo : v > hi ? hi : v; }

__global__ __launch_bounds__(512, 2) void k_fused(Params p) {
  TSDECL;
  LAS char* sm = (LAS char*)smem_;
  const unsigned lb = (unsigned)(size_t)sm;
  const int bid = blockIdx.x, xcd = bid & 7, loc = bid >> 3;
  const int wave = __builtin_amdgcn_readfirstlane(threadIdx.x >> 6);
  int lane = threadIdx.x & 63;
  const int rb = (xcd & 1) * 8 + (loc & 7), jg = (xcd >> 1) * 4 + (loc >> 3), pr = jg >> 1, tyB = jg & 1;
  const int mw = wave & 3, cw = wave >> 2;
  const bool spec = jg == 15, likeA = !tyB || spec;
  const i32x4 sx = mk_srd(p.x, X_BYTES), sq = mk_srd(p.wqkv, WQ_BYTES), sg = mk_srd(p.wg, WF_BYTES), su = mk_srd(p.wu, WF_BYTES);
  const int rot = (xcd & 1) * 5;
#define FHQ 3
#define ROT 0
  const int rr = ROT ? ((loc & 7) + (loc >> 3)) % 5 : 0;
  auto sidx = [&](int s) {
    int r2 = rr;
    if (ROT) asm volatile("" : "+s"(r2));
    const int v = s % 5 + r2;
    return (unsigned)((v >= 5 ? v - 5 : v) + (s < 5 ? rot : 5 - rot));
  };
  const unsigned drow = 8 * wave + (lane >> 3), wcol = ((lane & 7) ^ ((lane >> 2) & 4)) * 16u;
  const unsigned vx = (rb * 128 + drow) * 1280u + (((lane & 7) ^ (((wave & 1) << 2) | (lane >> 4))) * 16u);
  const unsigned vq = drow * 3840u + wcol, vg = drow * 3072u + wcol, vu = vg ^ 32u;
  const unsigned v1 = likeA ? vg : vq, v2 = likeA ? vu : vq;
  const i32x4 s1 = sel_srd(likeA, sg, sq), s2 = sel_srd(likeA, su, sq);
  const unsigned gl1 = spec ? 3 * (rb >> 1) + 1 + (rb & 1) : 3 * pr, gl2 = 3 * pr + 1 + tyB;
  const unsigned c0 = (4 * pr + tyB) * 128u, c1 = likeA ? gl1 * 128u : c0 + 128u, c2 = likeA ? c1 : c0 + 256u;
  const unsigned st1 = likeA ? 196608u : 245760u;
  const unsigned ldw = lb + wave * 1024u;
  auto issue = [&](int s, int b) {
    if (b < 2) dma16(ldw + L_X + (s % 3) * 16384u + b * UN, vx, sx, b * 81920u + sidx(s) * 128u);
    else if (b == 2) dma16(ldw + LW(s), vq, sq, sidx(s) * 245760u + c0);
    else if (b == 3) dma16(ldw + LW(s) + UN, v1, s1, sidx(s) * st1 + c1);
    else dma16(ldw + LW(s) + 2 * UN, v2, s2, sidx(s) * st1 + c2);
  };
  auto issue_g = [&](int g, int b) { dma16(ldw + LG(g) + b * UN, b ? vu : vg, b ? su : sg, sidx(KS(g)) * 196608u + gl2 * 128u); };
#pragma unroll
  for (int b = 0; b < 5; b++) issue(0, b);
  const i32x4 sc = mk_srd(p.ws, WS_BYTES);
  const unsigned srow = rb * 128 + jg * 8 + wave;
  unsigned vl = lane * 16u;
  unsigned ep = ld4<1>(0u, sc, 0);
  u32x4 r0 = ld16<0>(vl, sx, srow * 1280u);
  unsigned r1 = ld4<0, 1024>(lane * 4u, sx, srow * 1280u);
  bar();
#pragma unroll
  for (int b = 0; b < 5; b++) issue(1, b);
#define WARM 0
  unsigned wrm = 0;
  if (WARM) {
    unsigned long long pc; unsigned cn;
    asm volatile("s_getpc_b64 %0\n.Lpc%=:\n\ts_mov_b32 %1, .Lkend0-.Lpc%=\n\ts_max_i32 %1, %1, .Lkend1-.Lpc%=\n\ts_max_i32 %1, %1, .Lkend2-.Lpc%="
                 : "=s"(pc), "=s"(cn) :: "scc");
    const char* b = wave == 0 ? p.wqkv : wave == 1 ? p.wg : wave == 2 ? p.wu : wave == 3 ? p.ws : wave == 4 ? p.out : wave == 5 ? p.wd : (const char*)pc;
    unsigned n = wave == 0 ? WQ_BYTES : wave == 3 ? WS_BYTES : wave == 4 ? X_BYTES : wave < 6 ? WF_BYTES : cn - 4;
    if (!(WARM & 1) && wave < 6) n = 0;
    if (!(WARM & 2) && wave >= 6) n = 0;
    const unsigned st = wave == 0 ? 245760u : wave == 3 || wave == 4 ? 655360u : wave < 6 ? 196608u : 128u;
    wrm = ld4<0>(sel(wave < 6 || lane < 14, lane * st, OOB), mk_srd(b, n), wave < 6 ? loc * 128u : (loc * 2 + wave - 6) * 1792u);
  }
  TS(1);
  const i32x4 sdn = mk_srd(p.wd, WF_BYTES), so = mk_srd(p.wo, WO_BYTES);
  const unsigned kA = bid * 205u >> 10, cA = bid - kA * 5u, kB = (bid + 256u) * 205u >> 10, cB = bid + 256u - kB * 5u;
  const unsigned offB = bid < 84 ? 0u : OOB;
  const unsigned vpk = (4 * wave + (lane >> 4)) * 1280u + (lane & 15) * 16u;
  LAS char* pkr = sm + (L_PK + (8 * (lane >> 4) + ((lane & 15) >> 2)) * 256u + wave * 32u + (lane & 3) * 8u);
  int hi = lane >> 5;
  LAS char* xa[4];
#pragma unroll
  for (int ks = 0; ks < 4; ks++) { xa[ks] = sm + (L_X + (32 * mw + (lane & 31)) * 128u + (((2 * ks + hi) ^ ((lane >> 1) & 7)) * 16u)); PIN(xa[ks]); }
  LAS char *wa[3], *wr[2];
  {
    const unsigned ch = (lane >> 4) & 1, r = (lane >> 2) & 3, c = lane & 3;
    const unsigned wl = hi * 1024u + r * 128u + ((cw ^ (r >> 1)) * 64u) + (((c & 1) << 1 | c >> 1) * 8u);
    const unsigned q = wl + ch * 32u, g0 = wl + ch * (UN + 32u), g1 = wl + ch * UN + (ch ^ 1) * 32u;
    wa[0] = sm + q; wa[1] = sm + (UN + (likeA ? g0 : q)); wa[2] = sm + (likeA ? UN + g1 : 2 * UN + q);
    wr[0] = sm + (g0 + LG(0)); wr[1] = sm + (g1 + LG(0));
    PIN(wa[0]); PIN(wa[1]); PIN(wa[2]); PIN(wr[0]); PIN(wr[1]);
  }
  const int cst = loc & 7;
  LAS char* wc = (cst == 1 ? wa[1] : cst == 2 ? wa[2] : wa[0]) + mw * 2048;
  PIN(wc);
  const unsigned csb = C_CS + (jg * 10 + cw) * 512u;
  const u32x4 ones = {0x3f803f80u, 0x3f803f80u, 0x3f803f80u, 0x3f803f80u};
  const unsigned Rq = rb * 4 + mw;
  const unsigned fme = ((jg * 16 + rb) * 8 + wave) * 128u;
  const int head = xcd * 4 + (loc & 3), ch = loc >> 2, par = head & 1, o0 = 5 * head >> 1;
  const unsigned w3b = WS_W3 + (wave * 240 + ch * 5) * 1024u;
  unsigned w3c = 0;
#define SROT 1
  auto jr = [&](int j) {
    if (!SROT || j == 8) return (unsigned)(j < 6 ? j : j - 6);
    unsigned c = bid;
    asm volatile("" : "+s"(c));
    c >>= 5;
    return j < 2 ? j ^ (c & 1) : j < 6 ? 2 + ((j + 2 + c) & 3) : (j - 6) ^ (c & 1);
  };
  unsigned wbs = 0;
  auto w3a = [&](int j, int u) {
    if (u == 0) wbs = j < 6 ? w3b + jr(j) * 40960u : w3c + jr(j) * 327680u;
    return wbs + u * 1024u;
  };
  u32x4 RW[6][5], Q1, Q2;
  unsigned tag = 0, fe = 0;
  auto fpo = [&]() {
    int b3 = bid;
    asm volatile("" : "+s"(b3));
    const unsigned l = lane_id(), rt = (b3 & 7) * 4 + ((b3 >> 3) & 3);
    return (((2 * wave + (l >> 2)) * 16 + (rt >> 1)) * 8 + (l & 2) * 2 + (rt & 1) * 2 + (l & 1)) * 128u;
  };
  auto ld_w3 = [&](int buf, int j, int u) { RW[buf][u] = ld16<1>(vl, sc, w3a(j, u)); };
  unsigned kvv = 0, kvb = 0;
  auto kv_addr = [&]() {
    const int l = lane_id(), oi = ((l >> 2) & 3) ^ (l >> 4), R = ch * 8 - 8 + 2 * wave;
    kvv = sel(oi < (R >= 0 ? 3 : 0), oi * 512u + ((l >> 4) * 4 + (l & 3)) * 16u, OOB);
    kvb = WS_QKV + (unsigned)((R >= 0 ? R : 0) * 240 + 80 + o0) * 512u;
    PIN(kvv);
  };
  auto kv1 = [&](int i, unsigned v) {
    dma16<1>(lb + AT_K + (i >> 2) * 32768u + wave * 4096u + (i & 3) * 1024u, v, sc, kvb + (i >> 2) * 40960u + ((i >> 1) & 1) * 122880u + (i & 1) * 256u);
  };
  auto q_issue = [&](int i) {
    const unsigned qb = WS_QKV + (unsigned)((ch * 8 + wave) * 240 + o0) * 512u;
    if (i == 0) Q1 = ld16<1>(vl, sc, qb);
    else Q2 = ld16<1, 1024>(sel(vl < 512u, vl, OOB), sc, qb);
  };
  LAS char *kp, *kp1, *vp[2], *cw_;
  f32x16 sa, sb;
  unsigned nmk[8];
  auto att_addr = [&](int k) {
    const int l = lane_id(), h = l >> 5, q = l & 31;
    if (k == 0) {
      kp = sm + (AT_K + wave * 2048 + (q >> 2) * 256 + ((h ^ ((q >> 2) & 3)) * 64) + (q & 3) * 16);
      kp1 = sm + (((unsigned)(size_t)kp - lb) ^ 128u);
      PIN(kp); PIN(kp1);
    } else if (k == 1) {
      cw_ = sm + (AT_K + wave * 4096 + h * 2048 + (q >> 2) * 256 + ((3 ^ ((q >> 2) & 3)) * 64) + (q & 3) * 16);
      PIN(cw_);
    } else {
      const int mq = 4 * ((l >> 4) & 1) + (l & 3), iq = sel(mq < 4, 2 * par + ((mq & 1) << 1 | mq >> 1), sel(mq == 4, par ? 1 : 4, 6));
#pragma unroll
      for (int g = 0; g < 2; g++) {
        vp[g] = sm + (AT_V + wave * 2048 + h * 256 + g * 512 + (((iq >> 1) ^ (2 * g + h)) * 64) + ((l >> 2) & 3) * 16 + (iq & 1) * 8);
        PIN(vp[g]);
      }
    }
  };
  float dlf;
  auto att_mask = [&](int k) {
    if (k == 0) { const int l = lane_id(); dlf = (float)((l & 31) - 4 * (l >> 5)) * 1.0e36f; PIN(dlf); }
#pragma unroll
    for (int r = 2 * k; r < 2 * k + 2; r++) {
      const float d = dlf + (0.5f - (float)((r & 3) + 8 * (r >> 2))) * 1.0e36f;
      float a = __builtin_fminf(-d, 0.f), b = __builtin_fminf(d, 0.f);
      PIN(a); PIN(b);
      sa[r] = a; sb[r] = b;
    }
    asm volatile("v_pk_ashrrev_i16 %0, 15, %1 op_sel_hi:[0,1]" : "=v"(nmk[k]) : "v"(v_cvt(sb[2 * k], sb[2 * k + 1])));
  };

#define PINA(n) "+v"(A[n][0]), "+v"(A[n][1]), "+v"(A[n][2]), "+v"(A[n][3])
#define PINW(n) "+v"(RW[n][0]), "+v"(RW[n][1]), "+v"(RW[n][2]), "+v"(RW[n][3]), "+v"(RW[n][4])
  auto rest = [&](auto TY_) __attribute__((always_inline)) {
  lane = lane_id(); hi = lane >> 5; vl = lane * 16u;
  const int q32 = lane & 31;
  int bid2 = bid;
  asm volatile("" : "+s"(bid2));
  const int rt = (bid2 & 7) * 4 + ((bid2 >> 3) & 3), cg = bid2 >> 5;
  w3c = WS_W3 + ((48 + wave) * 40 + cg * 5) * 1024u;
  const unsigned ab = WS_AF + (rt * 4 * 68 + wave * 6) * 1024u, ac = WS_AF + (rt * 4 * 68 + 48 + wave) * 1024u;
  const unsigned r7 = wave < 4 ? R_A + 40960u + wave * 5120u : (wave - 4) * 5120u;
  u32x4 A[4][4];
  unsigned abs_ = 0;
  auto ld_a = [&](int buf, int j, int i, unsigned v) {
    if (i == 0) abs_ = j < 6 ? ab + jr(j) * 1024u : ac + jr(j) * 8192u;
    A[buf][i] = ld16<1>(v, sc, abs_ + i * 69632u);
  };
  auto dma_w3 = [&](unsigned dst, int j, int u) { dma16<1>(lb + dst + u * 1024u, vl, sc, w3a(j, u)); };
  f32x16 o;
  unsigned hf;
  const unsigned vfl = C_FO + ((cg * 8 + wave) * 32u + rt) * 128u;
  {
    *(LAS u32x4*)cw_ = u32x4{(ch == 0 && wave < 4) ? 0x3f80fe00u : 0x3f800000u, 0u, 0u, 0u};
    *(LAS u32x4*)(cw_ + 32768) = ones;
    {
      const bool z1 = par & !hi;
      Q1[0] = sel(z1, 0u, Q1[0]); Q1[1] = sel(z1, 0u, Q1[1]);
      Q2[2] = sel(par, Q2[2], 0u); Q2[3] = sel(par, Q2[3], 0u);
      Q2[0] = sel(hi, 0x00003f80u, Q2[0]);
    }
    const unsigned fp_ = fpo();
    unsigned vfe = sel(lane < 4, C_FE + fp_, OOB), vfh = sel(lane < 8, C_FH + fp_, OOB);
    PIN(vfe); PIN(vfh); PIN(Q1); PIN(Q2);
    WAIT_LGKM0();
    bar();
    TS(20);
#define PRIO 1
    if (PRIO) __builtin_amdgcn_s_setprio(2);
    f32x16 z;
#pragma unroll
    for (int r = 0; r < 16; r++) z[r] = 0.f;
    auto scq = [&](int off, u32x4 q2) {
      const u32x4 a = *(LAS u32x4*)(kp + off), b = *(LAS u32x4*)(kp1 + off);
      return mfma32(b, q2, mfma32(a, Q1, z));
    };
    auto pv = [&](int off, const unsigned* pk_) {
      const u32x4 V1 = cat(trd(vp[0] + off), trd(vp[1] + off)), V2 = cat(trd(vp[0] + off + 1024), trd(vp[1] + off + 1024));
      o = mfma32(V1, u32x4{pk_[0], pk_[1], pk_[2], pk_[3]}, o);
      o = mfma32(V2, u32x4{pk_[4], pk_[5], pk_[6], pk_[7]}, o);
    };
    const int dl = q32 - 4 * hi;
    auto xmax = [&](float m) {
      auto r = __builtin_amdgcn_permlane32_swap(__float_as_uint(m), __float_as_uint(m), false, false);
      return __builtin_fmaxf(__uint_as_float(r[0]), __uint_as_float(r[1]));
    };
    auto edge = [&](const f32x16& ee, float m) {
      unsigned pa[8], pb[8];
#pragma unroll
      for (int r = 0; r < 16; r += 2) {
        const bool b0 = (r & 3) + 8 * (r >> 2) <= dl, b1 = ((r + 1) & 3) + 8 * (r >> 2) <= dl;
        const unsigned x = pk2(__builtin_amdgcn_exp2f(ee[r] - m), __builtin_amdgcn_exp2f(ee[r + 1] - m));
        const unsigned mk = (b0 ? 0xffffu : 0u) | (b1 ? 0xffff0000u : 0u);
        pb[r >> 1] = x & mk; pa[r >> 1] = x & ~mk;
      }
      pv(0, pa); pv(8 * 2048, pb);
    };
    auto merged = [&]() {
      const f32x16 s0 = scq(0, Q2), s8 = scq(8 * 2048, Q2);
      f32x16 ee;
#pragma unroll
      for (int r = 0; r < 16; r++) ee[r] = ((r & 3) + 8 * (r >> 2) <= dl) ? s8[r] : s0[r];
      return ee;
    };
    auto vmax = [&](const f32x16& ee, float m) {
#pragma unroll
      for (int r = 0; r < 16; r += 2) m = __builtin_fmaxf(__builtin_fmaxf(m, ee[r]), ee[r + 1]);
      return m;
    };
    unsigned va = 0;
    bool early = false;
    {
      u32x4 Ka, Kb, P0, P1;
      u32x2 Va[2], Vb[2];
      f32x16 S[2];
      float t[8];
      auto rdk = [&](int j) { Ka = *(LAS u32x4*)(kp + j * 2048); Kb = *(LAS u32x4*)(kp1 + j * 2048); };
      u32x4 Q3;
      {
        const u32x4 k0 = *(LAS u32x4*)kp, k8 = *(LAS u32x4*)(kp + 8 * 2048), k0b = *(LAS u32x4*)kp1, k8b = *(LAS u32x4*)(kp1 + 8 * 2048);
        rdk(1);
        const u32x4 v0 = cat(trd(vp[0]), trd(vp[1])), v0b = cat(trd(vp[0] + 1024), trd(vp[1] + 1024));
        SB();
        mfma32a(k0, Q1, sa); ld_w3(3, 3, 0);
        mfma32a(k8, Q1, sb); ld_w3(3, 3, 1);
        mfma32a(k0b, Q2, sa); ld_w3(3, 3, 2);
        mfma32a(k8b, Q2, sb); ld_w3(3, 3, 3);
        mfma32z(Ka, Q1, S[1]); ld_w3(3, 3, 4);
        const u32x4 v8 = cat(trd(vp[0] + 8 * 2048), trd(vp[1] + 8 * 2048)), v8b = cat(trd(vp[0] + (8 * 2048 + 1024)), trd(vp[1] + (8 * 2048 + 1024)));
        asm volatile("s_nop 7" : "+v"(sa), "+v"(sb) :: "memory");
        SB();
        f32x16 ee;
#pragma unroll
        for (int r = 0; r < 16; r++) { float x; asm volatile("v_max_f32 %0, %1, %2" : "=v"(x) : "v"(sa[r]), "v"(sb[r])); ee[r] = x; }
        const unsigned mb = pk2(xmax(vmax(ee, -3.0e38f)), 0.f) << 16;
        Q3 = u32x4{sel(hi, (mb ^ 0x80000000u) | 0x3f80u, Q2[0]), Q2[1], Q2[2], Q2[3]};
        PIN(Q3);
        f32x2 nm2 = {__uint_as_float(mb ^ 0x80000000u), __uint_as_float(mb ^ 0x80000000u)};
        PIN(nm2);
        SB();
        mfma32a(Kb, Q3, S[1]);
        rdk(2);
        u32x4 pa, pb;
        sfor<0, 2>([&](auto H) {
          constexpr int h = H.v;
#pragma unroll
          for (int r = 0; r < 4; r++) {
            f32x2 d = {ee[8 * h + 2 * r], ee[8 * h + 2 * r + 1]};
            asm volatile("v_pk_add_f32 %0, %0, %1" : "+v"(d) : "v"(nm2));
            t[2 * r] = d[0]; t[2 * r + 1] = d[1];
          }
#pragma unroll
          for (int r = 0; r < 8; r++) t[r] = v_exp(t[r]);
#pragma unroll
          for (int r = 0; r < 4; r++) pb[r] = v_cvt(t[2 * r], t[2 * r + 1]);
#pragma unroll
          for (int r = 0; r < 4; r++) pa[r] = pb[r] & nmk[4 * h + r];
          PIN(pa);
#pragma unroll
          for (int r = 0; r < 4; r++) pb[r] ^= pa[r];
          PIN(pb);
          SB();
          if constexpr (h == 0) mfma32z(v0, pa, o); else mfma32a(v0b, pa, o);
          mfma32a(h ? v8b : v8, pb, o);
          if constexpr (h == 0) { Va[0] = trd(vp[0] + 2048); Va[1] = trd(vp[1] + 2048); }
          else { Vb[0] = trd(vp[0] + 3072); Vb[1] = trd(vp[1] + 3072); }
          SB();
        });
      }
      auto vm = [&](auto Q_) {
        constexpr int q = Q_.v;
        if constexpr (q < 5) ld_w3(4, 4, q);
        else if constexpr (q < 10) dma_w3(r7, 7, q - 5);
        else if constexpr (q < 18) {
          if constexpr (q == 10) {
            asm volatile("s_waitcnt vmcnt(16)" : "+v"(fe) :: "memory");
            early = !any_lane((lane < 4) & (fe != tag));
            va = sel(early, vl, OOB);
            PIN(va);
          }
          ld_a((q - 10) >> 2, (q - 10) >> 2, (q - 10) & 3, va);
          if constexpr (q == 15) fe = ld4<1>(vfe, sc, 0);
        } else if constexpr (q < 23) dma_w3(R_A + wave * 5120u, 6, q - 18);
        else if constexpr (q == 23) {
          if (!early) {
            asm volatile("s_waitcnt vmcnt(5)" : "+v"(fe) :: "memory");
            poll_rest(fe, lane < 4, tag, vfe, sc);
#pragma unroll
            for (int i = 0; i < 8; i++)
              asm volatile("buffer_load_dwordx4 %0, %1, %2, %3 offen sc1" : "+v"(A[i >> 2][i & 3]) : "v"(vl), "s"(sc), "s"(ab + jr(i >> 2) * 1024u + (i & 3) * 69632u) : "memory");
          }
        } else if constexpr (q == 26) hf = ld4<1>(vfh, sc, 0);
        if constexpr (q == FHQ) {
          static_assert(FHQ < 10, "");
          TS(21);
          wait_vm<6 + FHQ>();
          st4s(tag, sel(lane == 0, C_FH + fme, OOB), sc);
          TS(22);
        }
        SB();
      };
      sfor<1, 8>([&](auto J) {
        constexpr int j = J.v;
        f32x16 &cu = S[j & 1], &nx = S[(j + 1) & 1];
        if constexpr (j < 7) mfma32z(Ka, Q1, nx);
        vm(IC<4 * j - 4>{});
#pragma unroll
        for (int r = 0; r < 4; r++) t[r] = v_exp(cu[r]);
        if constexpr (j < 7) mfma32a(Kb, Q3, nx);
        if constexpr (j < 6) rdk(j + 2);
        SB();
#pragma unroll
        for (int r = 4; r < 8; r++) t[r] = v_exp(cu[r]);
        vm(IC<4 * j - 3>{});
#pragma unroll
        for (int r = 0; r < 4; r++) P0[r] = v_cvt(t[2 * r], t[2 * r + 1]);
        SB();
#pragma unroll
        for (int r = 0; r < 2; r++) t[r] = v_exp(cu[8 + r]);
        mfma32a(cat(Va[0], Va[1]), P0, o);
        if constexpr (j < 7) { Va[0] = trd(vp[0] + (j + 1) * 2048); Va[1] = trd(vp[1] + (j + 1) * 2048); }
        vm(IC<4 * j - 2>{});
#pragma unroll
        for (int r = 2; r < 8; r++) t[r] = v_exp(cu[8 + r]);
        vm(IC<4 * j - 1>{});
#pragma unroll
        for (int r = 0; r < 4; r++) P1[r] = v_cvt(t[2 * r], t[2 * r + 1]);
        asm volatile("s_nop 0" ::: "memory");
        SB();
        mfma32a(cat(Vb[0], Vb[1]), P1, o);
        if constexpr (j < 7) { Vb[0] = trd(vp[0] + ((j + 1) * 2048 + 1024)); Vb[1] = trd(vp[1] + ((j + 1) * 2048 + 1024)); }
        SB();
      });
      asm volatile("s_nop 7\n\ts_nop 7" : "+v"(o) :: "memory");
    }
    {
      float mx = 0.f;
#pragma unroll
      for (int r = 0; r < 12; r += 2) mx = __builtin_fmaxf(__builtin_fmaxf(mx, __builtin_fabsf(o[r])), __builtin_fabsf(o[r + 1]));
      if (any_lane(!((o[12] >= 5.9604645e-8f) & (o[12] <= 16777216.f) & (mx < 3.0e38f)))) {
        float m = vmax(merged(), -3.0e38f);
#pragma nounroll
        for (int j = 1; j < 8; j++) m = vmax(scq(j * 2048, Q2), m);
        m = xmax(m);
        o = z;
        edge(merged(), m);
#pragma nounroll
        for (int j = 1; j < 8; j++) {
          const f32x16 s = scq(j * 2048, Q2);
          unsigned pk_[8];
#pragma unroll
          for (int r = 0; r < 16; r += 2) pk_[r >> 1] = pk2(__builtin_amdgcn_exp2f(s[r] - m), __builtin_amdgcn_exp2f(s[r + 1] - m));
          pv(j * 2048, pk_);
        }
      }
    }
    TS(23);
    if (PRIO) asm volatile("s_setprio 1\n\ts_cmp_gt_u32 %0, 3\n\ts_cbranch_scc1 .Lpr%=\n\ts_setprio 0\n.Lpr%=:" :: "s"(wave) : "scc");
    const float inv = __builtin_amdgcn_rcpf(o[12]);
    const unsigned tb_ = (unsigned)((cg * 8 + wave) * 2 + (q32 >> 4)) * 68u + 48u;
    const unsigned oa = o0 + par + hi, ob = o0 + (par ? 0 : 2);
    st16<1>(u32x4{pk2(o[0] * inv, o[1] * inv), pk2(o[2] * inv, o[3] * inv), pk2(o[4] * inv, o[5] * inv), pk2(o[6] * inv, o[7] * inv)},
            (tb_ + (oa >> 2)) * 1024u + (oa & 3) * 256u + (q32 & 15) * 16u, sc, WS_AF);
    st8<1>(u32x2{pk2(o[8] * inv, o[9] * inv), pk2(o[10] * inv, o[11] * inv)},
           sel(hi, OOB, (tb_ + (ob >> 2)) * 1024u + (ob & 3) * 256u + (q32 & 15) * 16u + par * 8u), sc, WS_AF);
    asm volatile("s_waitcnt vmcnt(2)" : "+v"(hf), "+v"(fe), PINA(0), PINA(1), PINW(2), PINW(3), PINW(4) :: "memory");
    if (any_lane((lane < 8) & (hf != tag))) {
      wait_vm<0>();
      st4s(tag, sel(vl == 0, vfl, OOB), sc);
      poll_rest(hf, lane < 8, tag, vfh, sc);
    }
#pragma unroll
    for (int i = 0; i < 4; i++) ld_a(2, 2, i, vl);
  }
  const unsigned vl8 = sel(wave < 4, vl, OOB);
  unsigned vfo, vo[3], lo_[3], lo2[3];
  f32x4 fa[4][5];
  unsigned of = 0;
  u32x2 xr[3];
  TS(24);
  auto step = [&](auto J_) {
    constexpr int j = J_.v, wb = j < 6 ? j : j == 6 ? 1 : j == 7 ? 2 : 0, abuf = j & 3;
    if constexpr (j == 1) {
      wait_vm<4>();
      st4s(tag, sel(vl == 0, vfl, OOB), sc);
      TS(25);
    }
    if constexpr (j == 2) asm volatile("s_waitcnt vmcnt(10)" : PINA(2) :: "memory");
    if constexpr (j == 3) asm volatile("s_waitcnt vmcnt(14)" : PINA(3) :: "memory");
    if constexpr (j == 4) {
      asm volatile("s_waitcnt vmcnt(0)" : PINA(0), PINA(1), "+v"(of), PINW(0), PINW(5) :: "memory");
      TS(26);
      poll_rest(of, vfo != OOB, tag, vfo, sc);
      TS(27);
    }
    if constexpr (j == 6) asm volatile("s_waitcnt vmcnt(8)" : PINA(2) :: "memory");
    if constexpr (j == 7) asm volatile("s_waitcnt vmcnt(7)" : PINA(3) :: "memory");
    SB();
    sfor<0, 20>([&](auto K_) {
      constexpr int k = K_.v, u = k >> 2, i = k & 3;
      if constexpr (j == 0) mfma16z(RW[wb][u], A[abuf][i], fa[i][u]);
      else mfma16a(RW[wb][u], A[abuf][i], fa[i][u]);
      if constexpr (j >= 1 && j <= 3 && k < 4) ld_a((j + 2) & 3, j + 2, k, vl);
      if constexpr (j == 1 && k >= 4 && k < 9) ld_w3(5, 5, k - 4);
      if constexpr (j == 2 && k >= 4 && k < 9) RW[0][k - 4] = ld16<1>(vl8, sc, w3a(8, k - 4));
      if constexpr (j == 3 && k == 5) {
        const int l = lane_id(), h5 = 5 * (l & 31);
        auto ov = [&](int q) { return (h5 + 5 > 8 * q) & (h5 < 8 * q + 8); };
        vfo = sel(ov(wave) | ov(wave + 8) | ov(wave + 16), C_FO + (rt * 64u + l) * 128u, OOB);
        PIN(vfo);
      }
      if constexpr (j == 3 && k == 6) of = ld4<1>(vfo, sc, 0);
      if constexpr (j == 4 && k < 8) ld_a(2 + (k >> 2), 6 + (k >> 2), k & 3, vl);
      if constexpr (j == 5 && k < 4) ld_a(0, 8, k, vl8);
      if constexpr (j == 5 && k >= 8 && k < 13) RW[1][k - 8] = *(LAS u32x4*)(sm + (R_A + wave * 5120 + vl) + (k - 8) * 1024);
      if constexpr (j == 5 && k >= 13 && k < 18) RW[2][k - 13] = *(LAS u32x4*)(sm + (r7 + vl) + (k - 13) * 1024);
      if constexpr (j == 6 && k >= 2 && k < 14 && (k & 3) == 2) {
        constexpr int q = k >> 2;
        const unsigned c = ((wave < 4 ? 3 * wave : 4 + 2 * wave) + q) * 64u + lane_id(), row = c * 3277u >> 16, pos = c - row * 20u;
        vo[q] = sel(q == 2 && wave >= 4, OOB, (rt * 64u + row) * 1280u + cg * 160u + pos * 8u);
        const unsigned in = (row & 31) * 160u + ((pos ^ ((row >> 2) & 3)) * 8u);
        lo_[q] = R_A + (row >> 5) * 40960u + in;
        lo2[q] = sel(row >> 5, 0u, R_A + 20480u) + in;
        PIN(lo_[q]); PIN(lo2[q]);
        xr[q] = ld8<0>(vo[q], sx, 0);
      }
      SB();
    });
  };
  sfor<0, 8>(step);
  asm volatile("s_waitcnt vmcnt(0)" : PINA(0), "+v"(xr[0]), "+v"(xr[1]), "+v"(xr[2]) :: "memory");
  if (wave < 4) step(IC<8>{});
  asm volatile("s_nop 7" ::: "memory");
  SB();
  TS(28);
  lane = lane_id();
  const int t16 = lane & 15, g = lane >> 4;
  {
    const unsigned pin = t16 * 160 + ((g ^ (t16 >> 2)) * 8);
    LAS char* pw0 = sm + (R_A + wave * 5120 + pin);
    LAS char* pw1 = sm + (r7 + pin);
#pragma unroll
    for (int i = 0; i < 4; i++)
#pragma unroll
      for (int u = 0; u < 5; u++)
        *(LAS u32x2*)((i >> 1 ? pw1 : pw0) + ((i & 1) * 2560 + u * 32)) = u32x2{pk2(fa[i][u][0], fa[i][u][1]), pk2(fa[i][u][2], fa[i][u][3])};
    WAIT_LGKM0();
    bar();
    const i32x4 sout = mk_srd(p.out, X_BYTES);
    unsigned slo = 0x00003f80u, shi = 0x3f800000u;
    asm volatile("" : "+s"(slo), "+s"(shi));
    auto fin = [&](int q) {
      float a0 = blo(xr[q][0]), a1 = bhi(xr[q][0]), a2 = blo(xr[q][1]), a3 = bhi(xr[q][1]);
#pragma unroll
      for (int w = 0; w < 8; w++) {
        const u32x2 v = *(LAS u32x2*)(sm + (w < 4 ? lo_[q] : lo2[q]) + (w & 3) * 5120);
        a0 = dot2(v[0], slo, a0); a1 = dot2(v[0], shi, a1); a2 = dot2(v[1], slo, a2); a3 = dot2(v[1], shi, a3);
      }
      st8<0>(u32x2{pk2(a0, a1), pk2(a2, a3)}, vo[q], sout, 0);
    };
    fin(0); fin(1);
    if (wave < 4) fin(2);
  }
  TSOUT();
  asm volatile(".Lkend%0:" :: "i"(TY_.v));
  __builtin_amdgcn_endpgm();
  };

  auto phase1 = [&](auto SP_) {
    constexpr int SP = SP_.v;
    u32x4 X[32], xt[2];
    f32x16 acc[3], cs;
    u32x2 wf[2][3][2], w2[4][2], pk[2], gs, gc[3];
    u32x4 As, Bc;
    float sd, rs, rg;
    unsigned pf, nm, tch = 0;
    float e[10], sacc = 0.f, mean = 0.f, cp[4];
    auto stat = [&](int k) {
      if (k < 5) { const unsigned w = k < 4 ? r0[k & 3] : r1; e[2 * k] = blo(w); e[2 * k + 1] = bhi(w); sacc += e[2 * k] + e[2 * k + 1]; }
      else if (k >= 11 && k < 16) {
        const float d0 = e[2 * k - 22] - mean, d1 = e[2 * k - 21] - mean;
        sacc = __builtin_fmaf(d0, d0, sacc); PIN(sacc); sacc = __builtin_fmaf(d1, d1, sacc);
      } else if (k < 22) {
        const int q = k < 11 ? k - 5 : k - 16;
        if (q == 0) sacc += dppf<0x128>(sacc);
        else if (q == 1) sacc += dppf<0x124>(sacc);
        else if (q == 2) sacc += dppf<0x122>(sacc);
        else if (q == 3) sacc += dppf<0x121>(sacc);
        else if (q == 4) { auto a = __builtin_amdgcn_permlane16_swap(__float_as_uint(sacc), __float_as_uint(sacc), false, false); sacc = __uint_as_float(a[0]) + __uint_as_float(a[1]); }
        else {
          auto a = __builtin_amdgcn_permlane32_swap(__float_as_uint(sacc), __float_as_uint(sacc), false, false);
          sacc = (__uint_as_float(a[0]) + __uint_as_float(a[1])) * (1.0f / 640.0f);
          if (k == 10) { mean = sacc; sacc = 0.f; PIN(mean); }
        }
      } else if (k == 22) sacc = __builtin_amdgcn_sqrtf(rbf(rbf(sacc) + 1e-5f));
      PIN(sacc);
    };
    auto rdw = [&](int n, int t) {
      wf[n & 1][t][0] = trd(wa[t] + (LW(n >> 2) + (n & 3) * 2048));
      wf[n & 1][t][1] = trd(wa[t] + (LW(n >> 2) + (n & 3) * 2048 + 512));
    };
    auto rdx = [&](int n) {
      const u32x4 v = *(LAS u32x4*)(xa[n & 3] + ((n >> 2) % 3) * 16384);
      if (n < 32) X[n] = v; else xt[n & 1] = v;
    };
    auto rdc = [&](int s) { pk[0] = trd(wc + LW(s)); pk[1] = trd(wc + (LW(s) + 512)); };
    auto wfr = [&](int n, int t) { return cat(wf[n & 1][t][0], wf[n & 1][t][1]); };
    const unsigned vl8 = lane * 8u;
    auto ld_gran = [&](int k) {
      if (k == 0) gs = ld8<1>((vl8 & 56u) | (vl8 & 192u) << 1, sc, C_STAT + Rq * 512u);
      else if (k < 4) gc[k - 1] = ld8<1>(vl8, sc, csb + (k - 1) * 1024u);
      else pf = ld4<1>((vl8 & 248u) << 4, sc, C_PF + wave * 4096u);
    };
    auto split3 = [&](unsigned g) {
      const float c = __uint_as_float(g), r1_ = c - rbf(c);
      const unsigned a0 = pk2(c, r1_);
      u32x4 a = {a0, pk2(r1_ - bhi(a0), 0.f), 0u, 0u};
      PIN(a);
      return a;
    };
    auto mk_bc = [&]() { u32x4 b = {nm | nm << 16, nm, 0u, 0u}; PIN(b); return b; };
    auto prep = [&](int k) {
      if (k == 0) {
        if (any_lane((gs[1] != tag) | (gc[0][1] != tag) | (gc[1][1] != tag) | (gc[2][1] != tag))) {
          for (int it = 0; it < SPIN; it++) {
            __builtin_amdgcn_s_sleep(1);
#pragma unroll
            for (int i = 0; i < 4; i++) ld_gran(i);
            asm volatile("s_waitcnt vmcnt(0)" : "+v"(gs), "+v"(gc[0]), "+v"(gc[1]), "+v"(gc[2]) :: "memory");
            if (!any_lane((gs[1] != tag) | (gc[0][1] != tag) | (gc[1][1] != tag) | (gc[2][1] != tag))) break;
          }
        }
      } else if (k == 1) {
        nm = (gs[0] & 0xffffu) ^ 0x8000u; PIN(nm);
        Bc = mk_bc();
        sd = bhi(gs[0]); PIN(sd); rs = __builtin_amdgcn_rcpf(sd); PIN(rs);
      } else if (k == 8) { rg = -1.44269504089f * rs; PIN(rg); }
      else if (k < (SP ? 4 : 8)) { if (k & 1) mfma32a(As, Bc, acc[(k - 3) >> 1]); else As = split3(gc[(k - 2) >> 1][0]); }
    };
#pragma unroll
    for (int r = 0; r < 16; r++) cs[r] = 0.f;
    PIN(cs);
    u32x4 on1 = ones;
    PIN(on1);
    wait_vm<(WARM ? 9 : 8)>();
    bar();
    TS(2);
    rdx(0);
#pragma unroll
    for (int t = 0; t < 3; t++) rdw(0, t);
    rdx(1);
#pragma unroll
    for (int t = 0; t < 3; t++) rdw(1, t);
    rdc(0);
    SB();
    sfor<0, 120>([&](auto I_) {
      constexpr int n = I_.v / 3, t = I_.v % 3, s = n >> 2, ks = n & 3;
      constexpr int sb = (I_.v + 4) / 12 - 1, m = (I_.v + 4) % 12;
      const u32x4 xf = n < 32 ? X[n < 32 ? n : 0] : xt[n & 1];
      if constexpr (SP && t > 0) { if constexpr (n == 0) mfma32z(on1, wfr(n, t), acc[t]); else mfma32a(on1, wfr(n, t), acc[t]); }
      else { if constexpr (n == 0) mfma32z(wfr(n, t), xf, acc[t]); else mfma32a(wfr(n, t), xf, acc[t]); }
      if constexpr (m == 11) {
        SB();
        WAIT_LGKM0();
        if constexpr (s == 0) { asm volatile("s_waitcnt vmcnt(%3)" : "+v"(ep), "+v"(r0), "+v"(r1) : "i"(WARM ? 7 : 6) : "memory"); tag = __builtin_amdgcn_readfirstlane(ep) + 1u; }
        else if constexpr (s == 1) wait_vm<(WARM & 4 ? 6 : 5)>();
        else if constexpr (s == 2) asm volatile("s_waitcnt vmcnt(6)" : "+v"(wrm) :: "memory");
        else if constexpr (s == 3) wait_vm<7>();
        else if constexpr (s == 4) wait_vm<6>();
        else if constexpr (s == 5) wait_vm<7>();
        else if constexpr (s == 6) wait_vm<5>();
        else if constexpr (s == 7) wait_vm<12>();
        else if constexpr (s == 8) asm volatile("s_waitcnt vmcnt(6)" : "+v"(gs), "+v"(gc[0]), "+v"(gc[1]), "+v"(gc[2]) :: "memory");
        else asm volatile("s_waitcnt vmcnt(4)" : "+v"(tch) :: "memory");
        bar();
        TS(3 + s);
        SB();
        if constexpr (n < 38) { rdw(n + 2, 0); rdw(n + 2, 1); }
      } else if constexpr (n < 38) {
        if constexpr (m != 10) rdw(n + 2, t);
        if constexpr (t == 2) rdx(n + 2);
      } else if constexpr (n == 39) { w2[t][0] = trd(wr[t & 1] + (t >> 1) * 2048); w2[t][1] = trd(wr[t & 1] + ((t >> 1) * 2048 + 512)); }
      if constexpr (sb >= 0 && sb < 4 && m == 0) rdc(sb + 1);
      if constexpr ((sb == -1 && m == 6) || (sb >= 0 && sb < 4 && m == 3))
        asm volatile("s_cmp_gt_u32 %3, 2\n\ts_cbranch_scc1 .Lce%=\n\tv_mfma_f32_32x32x16_bf16 %0, %1, %2, %0\n.Lce%=:"
                     : "+v"(cs) : "v"(on1), "v"(cat(pk[0], pk[1])), "s"(cst) : "scc");
      if constexpr (sb == 3 && m == 8) { PIN(cs); *(LAS float*)(sm + L_CSR + wave * 128 + (lane & 31) * 4) = cs[0]; }
      if constexpr (sb == 4 && m < 4) cp[m] = *(LAS float*)(sm + L_CSR + cw * 512 + m * 128 + (lane & 31) * 4);
      if constexpr (sb == 4 && m == 4)
        st8<1>(u32x2{__float_as_uint((cp[0] + cp[1]) + (cp[2] + cp[3])), tag}, sel(vl8 < (cst < 3 && mw == 0 ? 256u : 0u), vl8, OOB), sc, csb + cst * 1024u + (xcd & 1) * 256u);
      if constexpr (sb == 0 || sb == 1) stat(sb * 12 + m);
      if constexpr (sb == 2 && m == 1) *(LAS unsigned*)(sm + L_CSR + 2048 + wave * 256 + lane * 4) = pk2(mean, sacc);
      if constexpr (sb == 3 && m == 1) pf = *(LAS unsigned*)(sm + L_CSR + 2048 + (lane & 7) * 256);
      if constexpr (sb == 3 && m == 4) st8<1>(u32x2{pf, tag}, sel(vl8 < (wave == 0 ? 64u : 0u), vl8, OOB), sc, C_STAT + (rb * 16 + jg) * 128u);
      if constexpr ((sb == 1 || sb == 4) && (m == 5 || m == 6)) pk[m - 5] = trd(pkr + (m - 5) * 1024);
      if constexpr (sb == 1 && m == 10) st16<1>(cat(pk[0], pk[1]), vl, sc, WS_W3 + (kA * 40u + cA * 8u + wave) * 1024u);
      if constexpr (sb == 4 && m == 10) st16<1>(cat(pk[0], pk[1]), vl | offB, sc, WS_W3 + (kB * 40u + cB * 8u + wave) * 1024u);
      if constexpr (sb == 2 && m == 10) dma16(ldw + L_PK, vpk | offB, mk_srd(p.wo, WO_BYTES), (kB - 48) * 40960u + cB * 256u);
      if constexpr (sb == -1 && m < 9) issue(2, m - 4);
      if constexpr ((WARM & 4) && sb == 0 && m == 10)
        asm volatile("buffer_load_dword %0, %1, %2, %3 offen" : "+v"(wrm) : "v"(sel(vl8 < 160u, vl8 * 384u, OOB)), "s"(sel_srd(cw, su, sg)),
                     "s"(((loc & 7) * 80 + mw * 20) * 3072u + gl2 * 128u) : "memory");
      if constexpr (sb == -1 && m == 9) dma16(ldw + L_PK, vpk, sel_srd(kA < 48, sdn, so), (kA < 48 ? kA : kA - 48) * 40960u + cA * 256u);
      if constexpr (sb == 6 && m == 0) st4s(tag, sel(vl8 == 0, wave == 0 ? C_PF + bid * 128u : OOB, OOB), sc);
      if constexpr (sb >= 0 && sb < 7 && (m & 1) && m < 10) issue(sb + 3, m >> 1);
      if constexpr (sb == 6 && m == 10) { issue_g(0, 0); issue_g(0, 1); ld_gran(0); ld_gran(1); ld_gran(2); ld_gran(3); }
      if constexpr (sb == 7 && !(m & 1)) issue_g(1 + m / 4, (m >> 1) & 1);
      if constexpr (sb == 8 && (m & 1) && m < 9) issue_g(4 + m / 4, (m >> 1) & 1);
#define TOUCH 0
      if constexpr (TOUCH && sb == 8 && m == 0) tch = ld4<1>(0u, sc, C_PF + (wave * 32 + loc) * 128u);
      if constexpr (sb == 9 && m == 0) ld_gran(4);
      if constexpr (sb == 9 && m > 0 && m < 3) { issue_g(5 + m, 0); issue_g(5 + m, 1); }
      if constexpr (sb == 8 && m < 9) prep(m);
      SB();
    });
    TS(13);
    auto round2 = [&](auto TY_) {
      constexpr int TY = TY_.v;
      f32x16 ac2[2];
      u32x4 qv, hv[2], A4[2];
      u32x2 gc2[2];
      float t[8], rq;
      unsigned fq, vf, vh, kve;
      bool late;
      auto qop = [&](auto U_, auto K_) {
        constexpr int u = U_.v, k = K_.v;
        const unsigned ln = 4 * pr + tyB + u;
        if constexpr (k == 0) {
          const float qs = ln < 10u ? 0.22360679775f * 1.44269504089f : 1.0f;
          asm volatile("v_mul_f32 %0, %1, %2" : "=v"(rq) : "s"(qs), "v"(rs));
        } else {
          constexpr int h = (k - 1) / 13, j = (k - 1) % 13;
          if constexpr (j < 8) t[j] = v_mul(acc[u][8 * h + j], rq);
          else if constexpr (j < 12) qv[j - 8] = v_cvt(t[2 * j - 16], t[2 * j - 15]);
          else st16<1>(qv, vl, sc, WS_QKV + (Rq * 240u + ln * 8u + cw * 4u + h * 2u) * 512u);
        }
      };
      auto gop = [&](f32x16& a, u32x4& o, auto K_) {
        constexpr int k = K_.v, ph = k >> 3, j = k & 7;
        if constexpr (ph == 0) t[j] = v_mul(a[j], rg);
        else if constexpr (ph == 1) t[j] = v_exp(t[j]);
        else if constexpr (ph == 2) t[j] = v_fma(t[j], sd, sd);
        else if constexpr (ph == 3) t[j] = v_rcp(t[j]);
        else if constexpr (ph == 4) t[j] = v_mul(t[j], a[8 + j]);
        else o[j] = v_cvt(t[2 * j], t[2 * j + 1]);
      };
      auto st_h = [&](u32x4 v, unsigned gl, int a) { st16<1>(v, vh, sc, WS_AF + ((rb * 8 + 2 * mw) * 68u + 2 * gl + cw) * 1024u + a * 512u); };
      auto epi = [&](auto K_) {
        constexpr int k = K_.v;
        if constexpr (k < 27) qop(IC<0>{}, K_);
        else if constexpr (TY == 1) { if constexpr (k < 81) qop(IC<k / 27>{}, IC<k % 27>{}); }
        else if constexpr (TY == 0) {
          if constexpr (k < 117) {
            constexpr int u = (k - 27) / 45, q = (k - 27) % 45;
            if constexpr (q < 44) gop(acc[1 + u], hv[u], IC<q>{}); else st_h(hv[u], gl1, u);
          }
        } else if constexpr (k < 29) {
          PIN(acc[k - 26]);
          st8<1>(u32x2{__float_as_uint(acc[k - 26][0] * 0.5f), tag}, sel(mw == 0, vl8, OOB), sc, C_CS + (rb * 10 + cw) * 512u + (k - 24) * 1024u);
        }
      };
      constexpr auto EK = [](int q) { return q < 1 ? 0 : (TY == 0 && q > 7) ? cl(30 + (q - 7) * 3, 0, 117) : cl((q - 1) * 5, 0, 117); };
      sfor<0, 80>([&](auto M_) {
        constexpr int m = M_.v;
        constexpr auto KK = [](int q) { return q < 64 ? q >> 1 : 32 + (q & 7); };
        constexpr auto AA = [](int q) { return q < 64 ? q & 1 : (q >> 3) & 1; };
        constexpr int kk = KK(m), a = AA(m), g = kk >> 2;
        if constexpr (TOUCH && m == 1) {
          const unsigned un = wave * 32 + loc, vt = sel(tch == tag, vl8 << 4, OOB);
          asm volatile("buffer_load_dword %0, %1, %2, %3 offen sc1" : "=&v"(tch) : "v"(vt), "s"(sc), "s"(WS_W3 + un * 8192u) : "memory");
          asm volatile("buffer_load_dword %0, %1, %2, %3 offen sc1" : "+v"(tch) : "v"(vt | (un < 84 ? 0u : OOB)), "s"(sc), "s"(WS_W3 + (256 + un) * 8192u) : "memory");
        }
        if constexpr (m == 16) {
          asm volatile("s_waitcnt vmcnt(%1)" : "+v"(pf) : "i"((TY == 1 ? 5 : TY == 0 ? 2 : 4) + 2 * TOUCH) : "memory");
          poll_rest(pf, true, tag, C_PF + wave * 4096u + ((vl8 & 248u) << 4), sc);
          bar();
          TS(14);
          st4s(tag, sel(vl8 == 0, (bid | wave) == 0 ? 0u : OOB, OOB), sc);
          SB();
        }
        if constexpr (m == 44) {
          asm volatile("s_waitcnt vmcnt(%1)" : "+v"(tch) : "i"(TY == 0 ? 11 : 10) : "memory");
          st4s(tag, sel(vl8 == 0, C_FQ + fme, OOB), sc);
          TS(15);
          SB();
        }
        if constexpr (m == 48) { bar(); SB(); }
        if constexpr (m == 50) {
          {
            const int l = lane_id(), sec = (l + 2) >> 2, fr = sel(l < 2, ch * 8 + wave, ch * 8 - 8 + 2 * wave + ((l >> 1) & 1));
            const unsigned oc = sec * 80u + o0 + (l & 1) * 2u, jp = (oc >> 5) * 2 + ((oc & 24u) != 0);
            vf = sel((l < 10) & (fr >= 0), C_FQ + ((jp * 16 + (fr >> 2)) * 8 + (oc & 4u) + (fr & 3)) * 128u, OOB);
          }
          gc2[0] = ld8<1>(vl8, sc, csb + 3072u); gc2[1] = ld8<1>(vl8, sc, csb + 4096u);
          fq = ld4<1>(vf, sc, 0);
          SB();
        }
        if constexpr (m == 59) {
          asm volatile("s_waitcnt vmcnt(0)" : "+v"(fq), "+v"(gc2[0]), "+v"(gc2[1]) :: "memory");
          TS(16);
          if constexpr (TY == 0) st4s(tag, sel(vl8 == 0, C_FE + fme, OOB), sc);
          if (any_lane((gc2[0][1] != tag) | (gc2[1][1] != tag))) {
            for (int it = 0; it < SPIN; it++) {
              __builtin_amdgcn_s_sleep(1);
              gc2[0] = ld8<1>(vl8, sc, csb + 3072u); gc2[1] = ld8<1>(vl8, sc, csb + 4096u);
              asm volatile("s_waitcnt vmcnt(0)" : "+v"(gc2[0]), "+v"(gc2[1]) :: "memory");
              if (!any_lane((gc2[0][1] != tag) | (gc2[1][1] != tag))) break;
            }
          }
          late = any_lane((vf != OOB) & (fq != tag));
          fq = ld4<1>(vf, sc, 0);
          kve = sel(late, OOB, kvv);
          PIN(kve);
          SB();
        }
        if constexpr (m == 66) {
          if (late) {
            asm volatile("s_waitcnt vmcnt(4)" : "+v"(fq) :: "memory");
            poll_rest(fq, vf != OOB, tag, vf, sc);
#pragma unroll
            for (int i = 0; i < 4; i++) kv1(i, kvv);
          }
          TS(17);
          SB();
        }
        const u32x4 xf = (g == 2 || g == 3) ? xt[kk & 1] : X[(g == 2 || g == 3) ? 0 : KS(g) * 4 + (kk & 3)];
        if constexpr (m < 2) mfma32z(cat(w2[m & 3][0], w2[m & 3][1]), xf, ac2[a]);
        else mfma32a(cat(w2[m & 3][0], w2[m & 3][1]), xf, ac2[a]);
        if constexpr (m + 3 < 80) {
          constexpr int k2 = KK(m + 3), a2 = AA(m + 3), g2 = k2 >> 2, gp = (k2 - 1) >> 2;
          if constexpr ((LG(g2) >> 16) != (LG(gp) >> 16)) { wr[a2] += (int)(LG(g2) & ~65535u) - (int)(LG(gp) & ~65535u); PIN(wr[a2]); }
          w2[(m + 3) & 3][0] = trd(wr[a2] + ((LG(g2) & 65535u) + (k2 & 3) * 2048));
          w2[(m + 3) & 3][1] = trd(wr[a2] + ((LG(g2) & 65535u) + (k2 & 3) * 2048 + 512));
          if constexpr (a2 == 0 && (g2 == 2 || g2 == 3)) xt[k2 & 1] = *(LAS u32x4*)(xa[k2 & 3] + (KS(g2) % 3) * 16384);
        }
        if constexpr (m == 8) { const unsigned l = lane_id(); vh = (l & 15) * 16u + ((l >> 4) & 1) * 69632u + (l >> 5) * 256u; PIN(vh); }
        sfor<EK(m), EK(m + 1)>(epi);
        if constexpr (m == 16 || m == 17) { issue_g(m - 8, 0); issue_g(m - 8, 1); }
        if constexpr (m >= 24 && m < 44 && !(m & 1)) ld_w3((m - 24) / 10, (m - 24) / 10, ((m - 24) >> 1) % 5);
        if constexpr (m >= 52 && m < 55) att_addr(m - 52);
        if constexpr (m >= 65 && m < 73) att_mask(m - 65);
        if constexpr (m == 60 || m == 61) A4[m - 60] = split3(gc2[m - 60][0]);
        if constexpr (m == 62) Bc = mk_bc();
        if constexpr (m == 64 || m == 65) mfma32a(A4[m - 64], Bc, ac2[m - 64]);
        if constexpr (m == 56) kv_addr();
        if constexpr (m >= 59 && m < 66 && (m & 1)) kv1((m - 59) >> 1, kve);
        if constexpr (m >= 67 && m < 74 && (m & 1)) kv1((m - 59) >> 1, kvv);
        if constexpr (m == 68 || m == 70) q_issue((m - 68) >> 1);
        if constexpr (m == 72 || (m >= 74 && m < 78)) ld_w3(2, 2, m == 72 ? 0 : m - 73);
        if constexpr (m >= 73) sfor<(m - 73) * 7, cl((m - 72) * 7, 0, 44)>([&](auto K_) { gop(ac2[0], hv[0], K_); });
        SB();
      });
      TS(18);
      fe = ld4<1>(sel(vl8 < 32u, C_FE + fpo(), OOB), sc, 0);
      st_h(hv[0], gl2, 0);
      asm volatile("s_nop 7" ::: "memory");
      sfor<0, 44>([&](auto K_) { gop(ac2[1], hv[1], K_); });
      st_h(hv[1], gl2, 1);
      u32x4 (&rw)[6][5] = RW; u32x4 &q1 = Q1, &q2 = Q2;
      asm volatile("s_waitcnt vmcnt(7)" : "+v"(q1), "+v"(q2), "+v"(fq), "+v"(rw[0][0]), "+v"(rw[0][1]), "+v"(rw[0][2]), "+v"(rw[0][3]), "+v"(rw[0][4]),
                   "+v"(rw[1][0]), "+v"(rw[1][1]), "+v"(rw[1][2]), "+v"(rw[1][3]), "+v"(rw[1][4]) :: "memory");
      TS(19);
      rest(TY_);
    };
    if constexpr (SP) round2(IC<2>{});
    else { if (tyB) round2(IC<1>{}); else round2(IC<0>{}); }
  };
  if (spec) phase1(IC<1>{}); else phase1(IC<0>{});
}

extern "C" void kernel_launch(void* const* d_in, const int* in_sizes, int n_in, void* d_out, int out_size, void* d_ws,
                              size_t ws_size, hipStream_t stream) {
  Params p;
  p.x = (const char*)d_in[0]; p.wqkv = (const char*)d_in[1]; p.wo = (const char*)d_in[2];
  p.wg = (const char*)d_in[4]; p.wu = (const char*)d_in[5]; p.wd = (const char*)d_in[6];
  p.out = (char*)d_out; p.ws = (char*)d_ws;
  (void)hipFuncSetAttribute((const void*)k_fused, hipFuncAttributeMaxDynamicSharedMemorySize, LDS_BYTES);
  k_fused<<<256, 512, LDS_BYTES, stream>>>(p);
}
```

```cpp
#include <hip/hip_runtime.h>

typedef int i32x4 __attribute__((ext_vector_type(4)));
typedef unsigned u32x4 __attribute__((ext_vector_type(4)));
typedef unsigned u32x2 __attribute__((ext_vector_type(2)));
typedef float f32x4 __attribute__((ext_vector_type(4)));
typedef float f32x2 __attribute__((ext_vector_type(2)));
typedef float f32x16 __attribute__((ext_vector_type(16)));
typedef __bf16 bf16x8 __attribute__((ext_vector_type(8)));
typedef __bf16 bf16x2 __attribute__((ext_vector_type(2)));
typedef short s16x4 __attribute__((ext_vector_type(4)));
#define LAS __attribute__((address_space(3)))
#define DEVI __device__ __forceinline__

constexpr int S_ = 2048, D_ = 640, F_ = 1536;
constexpr unsigned X_BYTES = S_ * D_ * 2, WQ_BYTES = D_ * 3 * D_ * 2, WF_BYTES = D_ * F_ * 2, WO_BYTES = D_ * D_ * 2;
constexpr unsigned QKV_BYTES = S_ * 3 * D_ * 2;
constexpr unsigned AF_BYTES = 128u * 68u * 1024u;
constexpr unsigned W3_BYTES = 68u * 40u * 1024u;
constexpr unsigned WS_QKV = 2u << 20, WS_AF = 16u << 20, WS_W3 = 32u << 20, WS_BYTES = 40u << 20;
constexpr unsigned C_PF = 0x8000, C_STAT = 0x10000, C_CS = 0x20000, C_FQ = 0x40000, C_FH = 0x80000, C_FO = 0xC0000, C_FE = 0x100000;
constexpr unsigned LDS_BYTES = 163840;
constexpr unsigned OOB = 0x80000000u;

DEVI i32x4 mk_srd(const void* p, unsigned n) {
  unsigned long long a = (unsigned long long)p;
  i32x4 r;
  r.x = __builtin_amdgcn_readfirstlane((int)(unsigned)a);
  r.y = __builtin_amdgcn_readfirstlane((int)(unsigned)(a >> 32));
  r.z = (int)n; r.w = 0x00020000;
  return r;
}
DEVI i32x4 sel_srd(bool c, i32x4 a, i32x4 b) { i32x4 r; r.x = c ? a.x : b.x; r.y = c ? a.y : b.y; r.z = c ? a.z : b.z; r.w = a.w; return r; }
template <int SC = 0> DEVI void dma16(unsigned ldsaddr, unsigned voff, i32x4 srd, unsigned soff) {
  if (SC) asm volatile("s_mov_b32 m0, %0\n\ts_nop 0\n\tbuffer_load_dwordx4 %1, %2, %3 offen sc1 lds"
                       :: "s"(ldsaddr), "v"(voff), "s"(srd), "s"(soff) : "memory");
  else    asm volatile("s_mov_b32 m0, %0\n\ts_nop 0\n\tbuffer_load_dwordx4 %1, %2, %3 offen lds"
                       :: "s"(ldsaddr), "v"(voff), "s"(srd), "s"(soff) : "memory");
}
template <int SC, int OFF = 0> DEVI u32x4 ld16(unsigned voff, i32x4 srd, unsigned soff) {
  u32x4 r;
  if (SC) asm volatile("buffer_load_dwordx4 %0, %1, %2, %3 offen offset:%4 sc1" : "=v"(r) : "v"(voff), "s"(srd), "s"(soff), "i"(OFF) : "memory");
  else    asm volatile("buffer_load_dwordx4 %0, %1, %2, %3 offen offset:%4" : "=v"(r) : "v"(voff), "s"(srd), "s"(soff), "i"(OFF) : "memory");
  return r;
}
template <int SC, int OFF = 0> DEVI u32x2 ld8(unsigned voff, i32x4 srd, unsigned soff) {
  u32x2 r;
  if (SC) asm volatile("buffer_load_dwordx2 %0, %1, %2, %3 offen offset:%4 sc1" : "=v"(r) : "v"(voff), "s"(srd), "s"(soff), "i"(OFF) : "memory");
  else    asm volatile("buffer_load_dwordx2 %0, %1, %2, %3 offen offset:%4" : "=v"(r) : "v"(voff), "s"(srd), "s"(soff), "i"(OFF) : "memory");
  return r;
}
template <int SC, int OFF = 0> DEVI unsigned ld4(unsigned voff, i32x4 srd, unsigned soff) {
  unsigned r;
  if (SC) asm volatile("buffer_load_dword %0, %1, %2, %3 offen offset:%4 sc1" : "=v"(r) : "v"(voff), "s"(srd), "s"(soff), "i"(OFF) : "memory");
  else    asm volatile("buffer_load_dword %0, %1, %2, %3 offen offset:%4" : "=v"(r) : "v"(voff), "s"(srd), "s"(soff), "i"(OFF) : "memory");
  return r;
}
template <int SC> DEVI void st16(u32x4 v, unsigned voff, i32x4 srd, unsigned soff) {
  if (SC) asm volatile("buffer_store_dwordx4 %0, %1, %2, %3 offen sc1\n\ts_nop 1" :: "v"(v), "v"(voff), "s"(srd), "s"(soff) : "memory");
  else    asm volatile("buffer_store_dwordx4 %0, %1, %2, %3 offen\n\ts_nop 1" :: "v"(v), "v"(voff), "s"(srd), "s"(soff) : "memory");
}
template <int SC, int OFF = 0> DEVI void st8(u32x2 v, unsigned voff, i32x4 srd, unsigned soff) {
  if (SC) asm volatile("buffer_store_dwordx2 %0, %1, %2, %3 offen offset:%4 sc1" :: "v"(v), "v"(voff), "s"(srd), "s"(soff), "i"(OFF) : "memory");
  else    asm volatile("buffer_store_dwordx2 %0, %1, %2, %3 offen offset:%4" :: "v"(v), "v"(voff), "s"(srd), "s"(soff), "i"(OFF) : "memory");
}
DEVI void st4s(unsigned v, unsigned voff, i32x4 srd) {
  asm volatile("buffer_store_dword %0, %1, %2, 0 offen sc1" :: "v"(v), "v"(voff), "s"(srd) : "memory");
}
template <int N> DEVI void wait_vm() { asm volatile("s_waitcnt vmcnt(%0)" :: "i"(N) : "memory"); }
#define WAIT_LGKM0() asm volatile("s_waitcnt lgkmcnt(0)" ::: "memory")
DEVI void bar() { asm volatile("s_barrier" ::: "memory"); }
#define SB() __builtin_amdgcn_sched_barrier(0)
#define PIN(x) asm volatile("" : "+v"(x))

DEVI unsigned pk2(float a, float b) { bf16x2 v = {(__bf16)a, (__bf16)b}; return __builtin_bit_cast(unsigned, v); }
DEVI float rbf(float a) { return (float)(__bf16)a; }
DEVI bf16x8 asbf(u32x4 v) { return __builtin_bit_cast(bf16x8, v); }
DEVI u32x2 trd(LAS char* p) { return __builtin_bit_cast(u32x2, __builtin_amdgcn_ds_read_tr16_b64_v4i16((LAS s16x4*)p)); }
DEVI u32x4 cat(u32x2 a, u32x2 b) { return __builtin_shufflevector(a, b, 0, 1, 2, 3); }
DEVI f32x4 mfma16(u32x4 a, u32x4 b, f32x4 c) { return __builtin_amdgcn_mfma_f32_16x16x32_bf16(asbf(a), asbf(b), c, 0, 0, 0); }
DEVI void mfma16a(u32x4 a, u32x4 b, f32x4& c) { asm volatile("v_mfma_f32_16x16x32_bf16 %0, %1, %2, %0" : "+v"(c) : "v"(a), "v"(b)); }
DEVI void mfma16z(u32x4 a, u32x4 b, f32x4& c) { asm volatile("v_mfma_f32_16x16x32_bf16 %0, %1, %2, 0" : "=&v"(c) : "v"(a), "v"(b)); }
DEVI void mfma32a(u32x4 a, u32x4 b, f32x16& c) { asm volatile("v_mfma_f32_32x32x16_bf16 %0, %1, %2, %0" : "+v"(c) : "v"(a), "v"(b)); }
DEVI void mfma32z(u32x4 a, u32x4 b, f32x16& c) { asm volatile("v_mfma_f32_32x32x16_bf16 %0, %1, %2, 0" : "=&v"(c) : "v"(a), "v"(b)); }
DEVI int lane_id() { int l; asm volatile("v_mbcnt_lo_u32_b32 %0, -1, 0\n\tv_mbcnt_hi_u32_b32 %0, -1, %0" : "=v"(l)); return l; }
DEVI f32x16 mfma32(u32x4 a, u32x4 b, f32x16 c) { return __builtin_amdgcn_mfma_f32_32x32x16_bf16(asbf(a), asbf(b), c, 0, 0, 0); }
DEVI float blo(unsigned r) { return __uint_as_float(r << 16); }
DEVI float bhi(unsigned r) { return __uint_as_float(r & 0xffff0000u); }
template <int C> DEVI float dppf(float v) { return __uint_as_float(__builtin_amdgcn_update_dpp(0, __float_as_uint(v), C, 0xf, 0xf, false)); }
DEVI float wave_sum(float v) {
  v += dppf<0x128>(v); v += dppf<0x124>(v); v += dppf<0x122>(v); v += dppf<0x121>(v);
  auto a = __builtin_amdgcn_permlane16_swap(__float_as_uint(v), __float_as_uint(v), false, false);
  v = __uint_as_float(a[0]) + __uint_as_float(a[1]);
  auto b = __builtin_amdgcn_permlane32_swap(__float_as_uint(v), __float_as_uint(v), false, false);
  return __uint_as_float(b[0]) + __uint_as_float(b[1]);
}
DEVI float v_fma(float a, float b, float c) { float r; asm volatile("v_fma_f32 %0, %1, %2, %3" : "=v"(r) : "v"(a), "v"(b), "v"(c)); return r; }
DEVI float v_mul(float a, float b) { float r; asm volatile("v_mul_f32 %0, %1, %2" : "=v"(r) : "v"(a), "v"(b)); return r; }
DEVI float v_exp(float a) { float r; asm volatile("v_exp_f32 %0, %1" : "=v"(r) : "v"(a)); return r; }
DEVI float v_rcp(float a) { float r; asm volatile("v_rcp_f32 %0, %1" : "=v"(r) : "v"(a)); return r; }
DEVI unsigned v_cvt(float a, float b) { unsigned r; asm volatile("v_cvt_pk_bf16_f32 %0, %1, %2" : "=v"(r) : "v"(a), "v"(b)); return r; }
DEVI float dot2(unsigned w, unsigned sel, float c) { return __builtin_amdgcn_fdot2_f32_bf16(__builtin_bit_cast(bf16x2, w), __builtin_bit_cast(bf16x2, sel), c, false); }
DEVI unsigned sel(bool c, unsigned a, unsigned b) { return c ? a : b; }
DEVI bool any_lane(bool c) { return __builtin_amdgcn_ballot_w64(c) != 0; }
constexpr int SPIN = 1 << 15;
DEVI void poll_rest(unsigned v, bool need, unsigned tag, unsigned voff, i32x4 srd) {
  if (any_lane(need & (v != tag))) {
    for (int it = 0; it < SPIN; it++) {
      __builtin_amdgcn_s_sleep(1);
      v = ld4<1>(voff, srd, 0);
      asm volatile("s_waitcnt vmcnt(0)" : "+v"(v) :: "memory");
      if (!any_lane(need & (v != tag))) break;
    }
  }
}

#define TS(i)
#define TSDECL
#define TSOUT()
template <int I> struct IC { static constexpr int v = I; };
template <int B, int E, class F> DEVI void sfor(F&& f) { if constexpr (B < E) { f(IC<B>{}); sfor<B + 1, E>(f); } }

struct Params {
  const char *x, *wqkv, *wo, *wg, *wu, *wd;
  char *out, *ws;
};

extern __shared__ __attribute__((aligned(16))) char smem_[];

constexpr unsigned UN = 8192;
constexpr unsigned LW(int s) { return s % 3 == 0 ? 0 : s % 3 == 1 ? 4 * UN : 7 * UN; }
constexpr unsigned L_PK = 3 * UN, L_X = 10 * UN, L_CSR = 16 * UN;
constexpr unsigned LG(int g) { return (g == 0 || g == 8 ? 16 : g == 1 || g == 9 ? 18 : g == 2 ? 4 : g == 3 ? 12 : g == 4 ? 6 : g == 5 ? 8 : g == 6 ? 0 : 2) * UN; }
constexpr int KS(int g) { return g < 2 ? g : g < 4 ? g + 6 : g - 2; }
constexpr unsigned AT_K = 4 * UN, AT_V = 8 * UN, R_A = 12 * UN;
constexpr int cl(int v, int lo, int hi) { return v < lo ? lo : v > hi ? hi : v; }

__global__ __launch_bounds__(512, 2) void k_fused(Params p) {
  TSDECL;
  LAS char* sm = (LAS char*)smem_;
  const unsigned lb = (unsigned)(size_t)sm;
  const int bid = blockIdx.x, xcd = bid & 7, loc = bid >> 3;
  const int wave = __builtin_amdgcn_readfirstlane(threadIdx.x >> 6);
  int lane = threadIdx.x & 63;
  const int rb = (xcd & 1) * 8 + (loc & 7), jg = (xcd >> 1) * 4 + (loc >> 3), pr = jg >> 1, tyB = jg & 1;
  const int mw = wave & 3, cw = wave >> 2;
  const bool spec = jg == 15, likeA = !tyB || spec;
  const i32x4 sx = mk_srd(p.x, X_BYTES), sq = mk_srd(p.wqkv, WQ_BYTES), sg = mk_srd(p.wg, WF_BYTES), su = mk_srd(p.wu, WF_BYTES);
  const int rot = (xcd & 1) * 5;
#define W3E 0
#define FHQ 3
  auto sidx = [&](int s) { return (unsigned)(s < 5 ? s + rot : s - rot); };
  const unsigned drow = 8 * wave + (lane >> 3), wcol = ((lane & 7) ^ ((lane >> 2) & 4)) * 16u;
  const unsigned vx = (rb * 128 + drow) * 1280u + (((lane & 7) ^ (((wave & 1) << 2) | (lane >> 4))) * 16u);
  const unsigned vq = drow * 3840u + wcol, vg = drow * 3072u + wcol, vu = vg ^ 32u;
  const unsigned v1 = likeA ? vg : vq, v2 = likeA ? vu : vq;
  const i32x4 s1 = sel_srd(likeA, sg, sq), s2 = sel_srd(likeA, su, sq);
  const unsigned gl1 = spec ? 3 * (rb >> 1) + 1 + (rb & 1) : 3 * pr, gl2 = 3 * pr + 1 + tyB;
  const unsigned c0 = (4 * pr + tyB) * 128u, c1 = likeA ? gl1 * 128u : c0 + 128u, c2 = likeA ? c1 : c0 + 256u;
  const unsigned st1 = likeA ? 196608u : 245760u;
  const unsigned ldw = lb + wave * 1024u;
  auto issue = [&](int s, int b) {
    if (b < 2) dma16(ldw + L_X + (s % 3) * 16384u + b * UN, vx, sx, b * 81920u + sidx(s) * 128u);
    else if (b == 2) dma16(ldw + LW(s), vq, sq, sidx(s) * 245760u + c0);
    else if (b == 3) dma16(ldw + LW(s) + UN, v1, s1, sidx(s) * st1 + c1);
    else dma16(ldw + LW(s) + 2 * UN, v2, s2, sidx(s) * st1 + c2);
  };
  auto issue_g = [&](int g, int b) { dma16(ldw + LG(g) + b * UN, b ? vu : vg, b ? su : sg, sidx(KS(g)) * 196608u + gl2 * 128u); };
#pragma unroll
  for (int b = 0; b < 5; b++) issue(0, b);
  const i32x4 sc = mk_srd(p.ws, WS_BYTES);
  const unsigned srow = rb * 128 + jg * 8 + wave;
  unsigned vl = lane * 16u;
  unsigned ep = ld4<1>(0u, sc, 0);
  u32x4 r0 = ld16<0>(vl, sx, srow * 1280u);
  unsigned r1 = ld4<0, 1024>(lane * 4u, sx, srow * 1280u);
  bar();
#pragma unroll
  for (int b = 0; b < 5; b++) issue(1, b);
  TS(1);
  const i32x4 sdn = mk_srd(p.wd, WF_BYTES), so = mk_srd(p.wo, WO_BYTES);
  const unsigned kA = bid * 205u >> 10, cA = bid - kA * 5u, kB = (bid + 256u) * 205u >> 10, cB = bid + 256u - kB * 5u;
  const unsigned offB = bid < 84 ? 0u : OOB;
  const unsigned vpk = (4 * wave + (lane >> 4)) * 1280u + (lane & 15) * 16u;
  LAS char* pkr = sm + (L_PK + (8 * (lane >> 4) + ((lane & 15) >> 2)) * 256u + wave * 32u + (lane & 3) * 8u);
  int hi = lane >> 5;
  LAS char* xa[4];
#pragma unroll
  for (int ks = 0; ks < 4; ks++) { xa[ks] = sm + (L_X + (32 * mw + (lane & 31)) * 128u + (((2 * ks + hi) ^ ((lane >> 1) & 7)) * 16u)); PIN(xa[ks]); }
  LAS char *wa[3], *wr[2];
  {
    const unsigned ch = (lane >> 4) & 1, r = (lane >> 2) & 3, c = lane & 3;
    const unsigned wl = hi * 1024u + r * 128u + ((cw ^ (r >> 1)) * 64u) + (((c & 1) << 1 | c >> 1) * 8u);
    const unsigned q = wl + ch * 32u, g0 = wl + ch * (UN + 32u), g1 = wl + ch * UN + (ch ^ 1) * 32u;
    wa[0] = sm + q; wa[1] = sm + (UN + (likeA ? g0 : q)); wa[2] = sm + (likeA ? UN + g1 : 2 * UN + q);
    wr[0] = sm + (g0 + LG(0)); wr[1] = sm + (g1 + LG(0));
    PIN(wa[0]); PIN(wa[1]); PIN(wa[2]); PIN(wr[0]); PIN(wr[1]);
  }
  const int cst = loc & 7;
  LAS char* wc = (cst == 1 ? wa[1] : cst == 2 ? wa[2] : wa[0]) + mw * 2048;
  PIN(wc);
  const unsigned csb = C_CS + (jg * 10 + cw) * 512u;
  const u32x4 ones = {0x3f803f80u, 0x3f803f80u, 0x3f803f80u, 0x3f803f80u};
  const unsigned Rq = rb * 4 + mw;
  const unsigned fme = ((jg * 16 + rb) * 8 + wave) * 128u;
  const int head = xcd * 4 + (loc & 3), ch = loc >> 2, par = head & 1, o0 = 5 * head >> 1;
  const unsigned w3b = WS_W3 + (wave * 240 + ch * 5) * 1024u;
  unsigned w3c = 0;
#define SROT 1
  auto jr = [&](int j) {
    if (!SROT || j == 8) return (unsigned)(j < 6 ? j : j - 6);
    unsigned c = bid;
    asm volatile("" : "+s"(c));
    c >>= 5;
    return j < 2 ? j ^ (c & 1) : j < 6 ? 2 + ((j + 2 + c) & 3) : (j - 6) ^ (c & 1);
  };
  unsigned wbs = 0;
  auto w3a = [&](int j, int u) {
    if (u == 0) wbs = j < 6 ? w3b + jr(j) * 40960u : w3c + jr(j) * 327680u;
    return wbs + u * 1024u;
  };
  u32x4 RW[6][5], Q1, Q2;
  unsigned tag = 0, fe = 0;
  auto fpo = [&]() {
    int b3 = bid;
    asm volatile("" : "+s"(b3));
    const unsigned l = lane_id(), rt = (b3 & 7) * 4 + ((b3 >> 3) & 3);
    return (((2 * wave + (l >> 2)) * 16 + (rt >> 1)) * 8 + (l & 2) * 2 + (rt & 1) * 2 + (l & 1)) * 128u;
  };
  auto ld_w3 = [&](int buf, int j, int u) { RW[buf][u] = ld16<1>(vl, sc, w3a(j, u)); };
  unsigned kvv = 0, kvb = 0;
  auto kv_addr = [&]() {
    const int l = lane_id(), oi = ((l >> 2) & 3) ^ (l >> 4), R = ch * 8 - 8 + 2 * wave;
    kvv = sel(oi < (R >= 0 ? 3 : 0), oi * 512u + ((l >> 4) * 4 + (l & 3)) * 16u, OOB);
    kvb = WS_QKV + (unsigned)((R >= 0 ? R : 0) * 240 + 80 + o0) * 512u;
    PIN(kvv);
  };
  auto kv1 = [&](int i, unsigned v) {
    dma16<1>(lb + AT_K + (i >> 2) * 32768u + wave * 4096u + (i & 3) * 1024u, v, sc, kvb + (i >> 2) * 40960u + ((i >> 1) & 1) * 122880u + (i & 1) * 256u);
  };
  auto q_issue = [&](int i) {
    const unsigned qb = WS_QKV + (unsigned)((ch * 8 + wave) * 240 + o0) * 512u;
    if (i == 0) Q1 = ld16<1>(vl, sc, qb);
    else Q2 = ld16<1, 1024>(sel(vl < 512u, vl, OOB), sc, qb);
  };
  LAS char *kp, *kp1, *vp[2], *cw_;
  f32x16 sa, sb;
  unsigned nmk[8];
  auto att_addr = [&](int k) {
    const int l = lane_id(), h = l >> 5, q = l & 31;
    if (k == 0) {
      kp = sm + (AT_K + wave * 2048 + (q >> 2) * 256 + ((h ^ ((q >> 2) & 3)) * 64) + (q & 3) * 16);
      kp1 = sm + (((unsigned)(size_t)kp - lb) ^ 128u);
      PIN(kp); PIN(kp1);
    } else if (k == 1) {
      cw_ = sm + (AT_K + wave * 4096 + h * 2048 + (q >> 2) * 256 + ((3 ^ ((q >> 2) & 3)) * 64) + (q & 3) * 16);
      PIN(cw_);
    } else {
      const int mq = 4 * ((l >> 4) & 1) + (l & 3), iq = sel(mq < 4, 2 * par + ((mq & 1) << 1 | mq >> 1), sel(mq == 4, par ? 1 : 4, 6));
#pragma unroll
      for (int g = 0; g < 2; g++) {
        vp[g] = sm + (AT_V + wave * 2048 + h * 256 + g * 512 + (((iq >> 1) ^ (2 * g + h)) * 64) + ((l >> 2) & 3) * 16 + (iq & 1) * 8);
        PIN(vp[g]);
      }
    }
  };
  float dlf;
  auto att_mask = [&](int k) {
    if (k == 0) { const int l = lane_id(); dlf = (float)((l & 31) - 4 * (l >> 5)) * 1.0e36f; PIN(dlf); }
#pragma unroll
    for (int r = 2 * k; r < 2 * k + 2; r++) {
      const float d = dlf + (0.5f - (float)((r & 3) + 8 * (r >> 2))) * 1.0e36f;
      float a = __builtin_fminf(-d, 0.f), b = __builtin_fminf(d, 0.f);
      PIN(a); PIN(b);
      sa[r] = a; sb[r] = b;
    }
    asm volatile("v_pk_ashrrev_i16 %0, 15, %1 op_sel_hi:[0,1]" : "=v"(nmk[k]) : "v"(v_cvt(sb[2 * k], sb[2 * k + 1])));
  };

#define PINA(n) "+v"(A[n][0]), "+v"(A[n][1]), "+v"(A[n][2]), "+v"(A[n][3])
#define PINW(n) "+v"(RW[n][0]), "+v"(RW[n][1]), "+v"(RW[n][2]), "+v"(RW[n][3]), "+v"(RW[n][4])
  auto rest = [&]() __attribute__((always_inline)) {
  lane = lane_id(); hi = lane >> 5; vl = lane * 16u;
  const int q32 = lane & 31;
  int bid2 = bid;
  asm volatile("" : "+s"(bid2));
  const int rt = (bid2 & 7) * 4 + ((bid2 >> 3) & 3), cg = bid2 >> 5;
  w3c = WS_W3 + ((48 + wave) * 40 + cg * 5) * 1024u;
  const unsigned ab = WS_AF + (rt * 4 * 68 + wave * 6) * 1024u, ac = WS_AF + (rt * 4 * 68 + 48 + wave) * 1024u;
  const unsigned r7 = wave < 4 ? R_A + 40960u + wave * 5120u : (wave - 4) * 5120u;
  u32x4 A[4][4];
  unsigned abs_ = 0;
  auto ld_a = [&](int buf, int j, int i, unsigned v) {
    if (i == 0) abs_ = j < 6 ? ab + jr(j) * 1024u : ac + jr(j) * 8192u;
    A[buf][i] = ld16<1>(v, sc, abs_ + i * 69632u);
  };
  auto dma_w3 = [&](unsigned dst, int j, int u) { dma16<1>(lb + dst + u * 1024u, vl, sc, w3a(j, u)); };
  f32x16 o;
  unsigned hf;
  const unsigned vfl = C_FO + ((cg * 8 + wave) * 32u + rt) * 128u;
  {
    *(LAS u32x4*)cw_ = u32x4{(ch == 0 && wave < 4) ? 0x3f80fe00u : 0x3f800000u, 0u, 0u, 0u};
    *(LAS u32x4*)(cw_ + 32768) = ones;
    {
      const bool z1 = par & !hi;
      Q1[0] = sel(z1, 0u, Q1[0]); Q1[1] = sel(z1, 0u, Q1[1]);
      Q2[2] = sel(par, Q2[2], 0u); Q2[3] = sel(par, Q2[3], 0u);
      Q2[0] = sel(hi, 0x00003f80u, Q2[0]);
    }
    const unsigned fp_ = fpo();
    unsigned vfe = sel(lane < 4, C_FE + fp_, OOB), vfh = sel(lane < 8, C_FH + fp_, OOB);
    PIN(vfe); PIN(vfh); PIN(Q1); PIN(Q2);
    WAIT_LGKM0();
    bar();
    TS(20);
#define PRIO 1
    if (PRIO) __builtin_amdgcn_s_setprio(2);
    f32x16 z;
#pragma unroll
    for (int r = 0; r < 16; r++) z[r] = 0.f;
    auto scq = [&](int off, u32x4 q2) {
      const u32x4 a = *(LAS u32x4*)(kp + off), b = *(LAS u32x4*)(kp1 + off);
      return mfma32(b, q2, mfma32(a, Q1, z));
    };
    auto pv = [&](int off, const unsigned* pk_) {
      const u32x4 V1 = cat(trd(vp[0] + off), trd(vp[1] + off)), V2 = cat(trd(vp[0] + off + 1024), trd(vp[1] + off + 1024));
      o = mfma32(V1, u32x4{pk_[0], pk_[1], pk_[2], pk_[3]}, o);
      o = mfma32(V2, u32x4{pk_[4], pk_[5], pk_[6], pk_[7]}, o);
    };
    const int dl = q32 - 4 * hi;
    auto xmax = [&](float m) {
      auto r = __builtin_amdgcn_permlane32_swap(__float_as_uint(m), __float_as_uint(m), false, false);
      return __builtin_fmaxf(__uint_as_float(r[0]), __uint_as_float(r[1]));
    };
    auto edge = [&](const f32x16& ee, float m) {
      unsigned pa[8], pb[8];
#pragma unroll
      for (int r = 0; r < 16; r += 2) {
        const bool b0 = (r & 3) + 8 * (r >> 2) <= dl, b1 = ((r + 1) & 3) + 8 * (r >> 2) <= dl;
        const unsigned x = pk2(__builtin_amdgcn_exp2f(ee[r] - m), __builtin_amdgcn_exp2f(ee[r + 1] - m));
        const unsigned mk = (b0 ? 0xffffu : 0u) | (b1 ? 0xffff0000u : 0u);
        pb[r >> 1] = x & mk; pa[r >> 1] = x & ~mk;
      }
      pv(0, pa); pv(8 * 2048, pb);
    };
    auto merged = [&]() {
      const f32x16 s0 = scq(0, Q2), s8 = scq(8 * 2048, Q2);
      f32x16 ee;
#pragma unroll
      for (int r = 0; r < 16; r++) ee[r] = ((r & 3) + 8 * (r >> 2) <= dl) ? s8[r] : s0[r];
      return ee;
    };
    auto vmax = [&](const f32x16& ee, float m) {
#pragma unroll
      for (int r = 0; r < 16; r += 2) m = __builtin_fmaxf(__builtin_fmaxf(m, ee[r]), ee[r + 1]);
      return m;
    };
    unsigned va = 0;
    bool early = false;
    {
      u32x4 Ka, Kb, P0, P1;
      u32x2 Va[2], Vb[2];
      f32x16 S[2];
      float t[8];
      auto rdk = [&](int j) { Ka = *(LAS u32x4*)(kp + j * 2048); Kb = *(LAS u32x4*)(kp1 + j * 2048); };
      u32x4 Q3;
      {
        const u32x4 k0 = *(LAS u32x4*)kp, k8 = *(LAS u32x4*)(kp + 8 * 2048), k0b = *(LAS u32x4*)kp1, k8b = *(LAS u32x4*)(kp1 + 8 * 2048);
        rdk(1);
        const u32x4 v0 = cat(trd(vp[0]), trd(vp[1])), v0b = cat(trd(vp[0] + 1024), trd(vp[1] + 1024));
        SB();
        mfma32a(k0, Q1, sa); if (!W3E) ld_w3(3, 3, 0);
        mfma32a(k8, Q1, sb); if (!W3E) ld_w3(3, 3, 1);
        mfma32a(k0b, Q2, sa); if (!W3E) ld_w3(3, 3, 2);
        mfma32a(k8b, Q2, sb); if (!W3E) ld_w3(3, 3, 3);
        mfma32z(Ka, Q1, S[1]); if (!W3E) ld_w3(3, 3, 4);
        const u32x4 v8 = cat(trd(vp[0] + 8 * 2048), trd(vp[1] + 8 * 2048)), v8b = cat(trd(vp[0] + (8 * 2048 + 1024)), trd(vp[1] + (8 * 2048 + 1024)));
        asm volatile("s_nop 7" : "+v"(sa), "+v"(sb) :: "memory");
        SB();
        f32x16 ee;
#pragma unroll
        for (int r = 0; r < 16; r++) { float x; asm volatile("v_max_f32 %0, %1, %2" : "=v"(x) : "v"(sa[r]), "v"(sb[r])); ee[r] = x; }
        const unsigned mb = pk2(xmax(vmax(ee, -3.0e38f)), 0.f) << 16;
        Q3 = u32x4{sel(hi, (mb ^ 0x80000000u) | 0x3f80u, Q2[0]), Q2[1], Q2[2], Q2[3]};
        PIN(Q3);
        f32x2 nm2 = {__uint_as_float(mb ^ 0x80000000u), __uint_as_float(mb ^ 0x80000000u)};
        PIN(nm2);
        SB();
        mfma32a(Kb, Q3, S[1]);
        rdk(2);
        u32x4 pa, pb;
        sfor<0, 2>([&](auto H) {
          constexpr int h = H.v;
#pragma unroll
          for (int r = 0; r < 4; r++) {
            f32x2 d = {ee[8 * h + 2 * r], ee[8 * h + 2 * r + 1]};
            asm volatile("v_pk_add_f32 %0, %0, %1" : "+v"(d) : "v"(nm2));
            t[2 * r] = d[0]; t[2 * r + 1] = d[1];
          }
#pragma unroll
          for (int r = 0; r < 8; r++) t[r] = v_exp(t[r]);
#pragma unroll
          for (int r = 0; r < 4; r++) pb[r] = v_cvt(t[2 * r], t[2 * r + 1]);
#pragma unroll
          for (int r = 0; r < 4; r++) pa[r] = pb[r] & nmk[4 * h + r];
          PIN(pa);
#pragma unroll
          for (int r = 0; r < 4; r++) pb[r] ^= pa[r];
          PIN(pb);
          SB();
          if constexpr (h == 0) mfma32z(v0, pa, o); else mfma32a(v0b, pa, o);
          mfma32a(h ? v8b : v8, pb, o);
          if constexpr (h == 0) { Va[0] = trd(vp[0] + 2048); Va[1] = trd(vp[1] + 2048); }
          else { Vb[0] = trd(vp[0] + 3072); Vb[1] = trd(vp[1] + 3072); }
          SB();
        });
      }
      auto vm = [&](auto Q_) {
        constexpr int q = Q_.v;
        if constexpr (q < 5) ld_w3(4, 4, q);
        else if constexpr (q < 10) dma_w3(r7, 7, q - 5);
        else if constexpr (q < 18) {
          if constexpr (q == 10) {
            asm volatile("s_waitcnt vmcnt(16)" : "+v"(fe) :: "memory");
            early = !any_lane((lane < 4) & (fe != tag));
            va = sel(early, vl, OOB);
            PIN(va);
          }
          ld_a((q - 10) >> 2, (q - 10) >> 2, (q - 10) & 3, va);
          if constexpr (q == 15) fe = ld4<1>(vfe, sc, 0);
        } else if constexpr (q < 23) dma_w3(R_A + wave * 5120u, 6, q - 18);
        else if constexpr (q == 23) {
          if (!early) {
            asm volatile("s_waitcnt vmcnt(5)" : "+v"(fe) :: "memory");
            poll_rest(fe, lane < 4, tag, vfe, sc);
#pragma unroll
            for (int i = 0; i < 8; i++)
              asm volatile("buffer_load_dwordx4 %0, %1, %2, %3 offen sc1" : "+v"(A[i >> 2][i & 3]) : "v"(vl), "s"(sc), "s"(ab + jr(i >> 2) * 1024u + (i & 3) * 69632u) : "memory");
          }
        } else if constexpr (q == 26) hf = ld4<1>(vfh, sc, 0);
        if constexpr (q == FHQ) {
          static_assert(FHQ < 10, "");
          TS(21);
          wait_vm<(W3E ? 1 : 6) + FHQ>();
          st4s(tag, sel(lane == 0, C_FH + fme, OOB), sc);
          TS(22);
        }
        SB();
      };
      sfor<1, 8>([&](auto J) {
        constexpr int j = J.v;
        f32x16 &cu = S[j & 1], &nx = S[(j + 1) & 1];
        if constexpr (j < 7) mfma32z(Ka, Q1, nx);
        vm(IC<4 * j - 4>{});
#pragma unroll
        for (int r = 0; r < 4; r++) t[r] = v_exp(cu[r]);
        if constexpr (j < 7) mfma32a(Kb, Q3, nx);
        if constexpr (j < 6) rdk(j + 2);
        SB();
#pragma unroll
        for (int r = 4; r < 8; r++) t[r] = v_exp(cu[r]);
        vm(IC<4 * j - 3>{});
#pragma unroll
        for (int r = 0; r < 4; r++) P0[r] = v_cvt(t[2 * r], t[2 * r + 1]);
        SB();
#pragma unroll
        for (int r = 0; r < 2; r++) t[r] = v_exp(cu[8 + r]);
        mfma32a(cat(Va[0], Va[1]), P0, o);
        if constexpr (j < 7) { Va[0] = trd(vp[0] + (j + 1) * 2048); Va[1] = trd(vp[1] + (j + 1) * 2048); }
        vm(IC<4 * j - 2>{});
#pragma unroll
        for (int r = 2; r < 8; r++) t[r] = v_exp(cu[8 + r]);
        vm(IC<4 * j - 1>{});
#pragma unroll
        for (int r = 0; r < 4; r++) P1[r] = v_cvt(t[2 * r], t[2 * r + 1]);
        asm volatile("s_nop 0" ::: "memory");
        SB();
        mfma32a(cat(Vb[0], Vb[1]), P1, o);
        if constexpr (j < 7) { Vb[0] = trd(vp[0] + ((j + 1) * 2048 + 1024)); Vb[1] = trd(vp[1] + ((j + 1) * 2048 + 1024)); }
        SB();
      });
      asm volatile("s_nop 7\n\ts_nop 7" : "+v"(o) :: "memory");
    }
    {
      float mx = 0.f;
#pragma unroll
      for (int r = 0; r < 12; r += 2) mx = __builtin_fmaxf(__builtin_fmaxf(mx, __builtin_fabsf(o[r])), __builtin_fabsf(o[r + 1]));
      if (any_lane(!((o[12] >= 5.9604645e-8f) & (o[12] <= 16777216.f) & (mx < 3.0e38f)))) {
        float m = vmax(merged(), -3.0e38f);
#pragma nounroll
        for (int j = 1; j < 8; j++) m = vmax(scq(j * 2048, Q2), m);
        m = xmax(m);
        o = z;
        edge(merged(), m);
#pragma nounroll
        for (int j = 1; j < 8; j++) {
          const f32x16 s = scq(j * 2048, Q2);
          unsigned pk_[8];
#pragma unroll
          for (int r = 0; r < 16; r += 2) pk_[r >> 1] = pk2(__builtin_amdgcn_exp2f(s[r] - m), __builtin_amdgcn_exp2f(s[r + 1] - m));
          pv(j * 2048, pk_);
        }
      }
    }
    TS(23);
    if (PRIO) asm volatile("s_setprio 1\n\ts_cmp_gt_u32 %0, 3\n\ts_cbranch_scc1 .Lpr%=\n\ts_setprio 0\n.Lpr%=:" :: "s"(wave) : "scc");
    const float inv = __builtin_amdgcn_rcpf(o[12]);
    const unsigned tb_ = (unsigned)((cg * 8 + wave) * 2 + (q32 >> 4)) * 68u + 48u;
    const unsigned oa = o0 + par + hi, ob = o0 + (par ? 0 : 2);
    st16<1>(u32x4{pk2(o[0] * inv, o[1] * inv), pk2(o[2] * inv, o[3] * inv), pk2(o[4] * inv, o[5] * inv), pk2(o[6] * inv, o[7] * inv)},
            (tb_ + (oa >> 2)) * 1024u + (oa & 3) * 256u + (q32 & 15) * 16u, sc, WS_AF);
    st8<1>(u32x2{pk2(o[8] * inv, o[9] * inv), pk2(o[10] * inv, o[11] * inv)},
           sel(hi, OOB, (tb_ + (ob >> 2)) * 1024u + (ob & 3) * 256u + (q32 & 15) * 16u + par * 8u), sc, WS_AF);
    asm volatile("s_waitcnt vmcnt(2)" : "+v"(hf), "+v"(fe), PINA(0), PINA(1), PINW(2), PINW(3), PINW(4) :: "memory");
    if (any_lane((lane < 8) & (hf != tag))) {
      wait_vm<0>();
      st4s(tag, sel(vl == 0, vfl, OOB), sc);
      poll_rest(hf, lane < 8, tag, vfh, sc);
    }
#pragma unroll
    for (int i = 0; i < 4; i++) ld_a(2, 2, i, vl);
  }
  const unsigned vl8 = sel(wave < 4, vl, OOB);
  unsigned vfo, vo[3], lo_[3], lo2[3];
  f32x4 fa[4][5];
  unsigned of = 0;
  u32x2 xr[3];
  TS(24);
  LAS char* pw[2];
  auto wr_part = [&](int k) {
    const int u = k >> 2, i = k & 3;
    *(LAS u32x2*)(pw[i >> 1] + ((i & 1) * 2560 + u * 32)) = u32x2{pk2(fa[i][u][0], fa[i][u][1]), pk2(fa[i][u][2], fa[i][u][3])};
  };
  auto step = [&](auto J_) {
    constexpr bool LAST = J_.v == 7;
    constexpr int j = J_.v, wb = j < 6 ? j : j == 6 ? 1 : j == 7 ? 2 : 0, abuf = j & 3;
    if constexpr (j == 1) {
      wait_vm<4>();
      st4s(tag, sel(vl == 0, vfl, OOB), sc);
      TS(25);
    }
    if constexpr (j == 2) asm volatile("s_waitcnt vmcnt(10)" : PINA(2) :: "memory");
    if constexpr (j == 3) asm volatile("s_waitcnt vmcnt(14)" : PINA(3) :: "memory");
    if constexpr (j == 4) {
      asm volatile("s_waitcnt vmcnt(0)" : PINA(0), PINA(1), "+v"(of), PINW(0), PINW(5) :: "memory");
      TS(26);
      poll_rest(of, vfo != OOB, tag, vfo, sc);
      TS(27);
    }
    if constexpr (j == 6) asm volatile("s_waitcnt vmcnt(8)" : PINA(2) :: "memory");
    if constexpr (j == 7) asm volatile("s_waitcnt vmcnt(7)" : PINA(3) :: "memory");
    SB();
    sfor<0, 20>([&](auto K_) {
      constexpr int k = K_.v, u = k >> 2, i = k & 3;
      if constexpr (j == 0) mfma16z(RW[wb][u], A[abuf][i], fa[i][u]);
      else mfma16a(RW[wb][u], A[abuf][i], fa[i][u]);
      if constexpr (j >= 1 && j <= 3 && k < 4) ld_a((j + 2) & 3, j + 2, k, vl);
      if constexpr (j == 1 && k >= 4 && k < 9) ld_w3(5, 5, k - 4);
      if constexpr (j == 2 && k >= 4 && k < 9) RW[0][k - 4] = ld16<1>(vl8, sc, w3a(8, k - 4));
      if constexpr (j == 3 && k == 5) {
        const int l = lane_id(), h5 = 5 * (l & 31);
        auto ov = [&](int q) { return (h5 + 5 > 8 * q) & (h5 < 8 * q + 8); };
        vfo = sel(ov(wave) | ov(wave + 8) | ov(wave + 16), C_FO + (rt * 64u + l) * 128u, OOB);
        PIN(vfo);
      }
      if constexpr (j == 3 && k == 6) of = ld4<1>(vfo, sc, 0);
      if constexpr (j == 4 && k < 8) ld_a(2 + (k >> 2), 6 + (k >> 2), k & 3, vl);
      if constexpr (j == 5 && k < 4) ld_a(0, 8, k, vl8);
      if constexpr (j == 5 && k >= 8 && k < 13) RW[1][k - 8] = *(LAS u32x4*)(sm + (R_A + wave * 5120 + vl) + (k - 8) * 1024);
      if constexpr (j == 5 && k >= 13 && k < 18) RW[2][k - 13] = *(LAS u32x4*)(sm + (r7 + vl) + (k - 13) * 1024);
      if constexpr (j == 6 && k >= 2 && k < 14 && (k & 3) == 2) {
        constexpr int q = k >> 2;
        const unsigned c = sel(wave < 4, 3 * wave + q, 4 + 2 * wave + (q > 1 ? 1 : q)) * 64u + lane_id(), row = c * 3277u >> 16, pos = c - row * 20u;
        vo[q] = sel(q == 2 && wave >= 4, OOB, (rt * 64u + row) * 1280u + cg * 160u + pos * 8u);
        const unsigned in = (row & 31) * 160u + ((pos ^ ((row >> 2) & 3)) * 8u);
        lo_[q] = R_A + (row >> 5) * 40960u + in;
        lo2[q] = sel(row >> 5, 0u, R_A + 20480u) + in;
        PIN(lo_[q]); PIN(lo2[q]);
        xr[q] = ld8<0>(vo[q], sx, 0);
      }
      if constexpr (j == 6 && k == 16) {
        const int l = lane_id(), t16 = l & 15, g = l >> 4;
        const unsigned pin = t16 * 160 + ((g ^ (t16 >> 2)) * 8);
        pw[0] = sm + (R_A + wave * 5120 + pin); pw[1] = sm + (r7 + pin);
        PIN(pw[0]); PIN(pw[1]);
      }
      if constexpr (LAST && k >= 8) wr_part(k - 8);
      SB();
    });
  };
  sfor<0, 7>(step);
  const i32x4 sout = mk_srd(p.out, X_BYTES);
  unsigned slo = 0x00003f80u, shi = 0x3f800000u;
  asm volatile("" : "+s"(slo), "+s"(shi));
  asm volatile("s_waitcnt vmcnt(3)" : PINA(0) :: "memory");
  if (wave < 4) step(IC<8>{});
  step(IC<7>{});
  asm volatile("s_nop 7" ::: "memory");
  SB();
  TS(28);
#pragma unroll
  for (int k = 12; k < 20; k++) wr_part(k);
  asm volatile("s_waitcnt vmcnt(0) lgkmcnt(0)" : "+v"(xr[0]), "+v"(xr[1]), "+v"(xr[2]) :: "memory");
  bar();
  u32x2 v[3][8];
#pragma unroll
  for (int q = 0; q < 3; q++)
#pragma unroll
    for (int w = 0; w < 8; w++) v[q][w] = *(LAS u32x2*)(sm + (w < 4 ? lo_[q] : lo2[q]) + (w & 3) * 5120);
  asm volatile("" : "+v"(v[2][0]), "+v"(v[2][1]), "+v"(v[2][2]), "+v"(v[2][3]), "+v"(v[2][4]), "+v"(v[2][5]), "+v"(v[2][6]), "+v"(v[2][7]));
  auto fin = [&](int q) {
    float a0 = blo(xr[q][0]), a1 = bhi(xr[q][0]), a2 = blo(xr[q][1]), a3 = bhi(xr[q][1]);
#pragma unroll
    for (int w = 0; w < 8; w++) {
      a0 = dot2(v[q][w][0], slo, a0); a1 = dot2(v[q][w][0], shi, a1); a2 = dot2(v[q][w][1], slo, a2); a3 = dot2(v[q][w][1], shi, a3);
    }
    st8<0>(u32x2{pk2(a0, a1), pk2(a2, a3)}, vo[q], sout, 0);
  };
  fin(0); fin(1);
  if (wave < 4) fin(2);
  TSOUT();
  __builtin_amdgcn_endpgm();
  };

  auto phase1 = [&](auto SP_) {
    constexpr int SP = SP_.v;
    u32x4 X[32], xt[2];
    f32x16 acc[3], cs;
    u32x2 wf[2][3][2], w2[4][2], pk[2], gs, gc[3];
    u32x4 As, Bc;
    float sd, rs, rg;
    unsigned pf, nm;
    float e[10], sacc = 0.f, mean = 0.f, cp[4];
    auto stat = [&](int k) {
      if (k < 5) { const unsigned w = k < 4 ? r0[k & 3] : r1; e[2 * k] = blo(w); e[2 * k + 1] = bhi(w); sacc += e[2 * k] + e[2 * k + 1]; }
      else if (k >= 11 && k < 16) {
        const float d0 = e[2 * k - 22] - mean, d1 = e[2 * k - 21] - mean;
        sacc = __builtin_fmaf(d0, d0, sacc); PIN(sacc); sacc = __builtin_fmaf(d1, d1, sacc);
      } else if (k < 22) {
        const int q = k < 11 ? k - 5 : k - 16;
        if (q == 0) sacc += dppf<0x128>(sacc);
        else if (q == 1) sacc += dppf<0x124>(sacc);
        else if (q == 2) sacc += dppf<0x122>(sacc);
        else if (q == 3) sacc += dppf<0x121>(sacc);
        else if (q == 4) { auto a = __builtin_amdgcn_permlane16_swap(__float_as_uint(sacc), __float_as_uint(sacc), false, false); sacc = __uint_as_float(a[0]) + __uint_as_float(a[1]); }
        else {
          auto a = __builtin_amdgcn_permlane32_swap(__float_as_uint(sacc), __float_as_uint(sacc), false, false);
          sacc = (__uint_as_float(a[0]) + __uint_as_float(a[1])) * (1.0f / 640.0f);
          if (k == 10) { mean = sacc; sacc = 0.f; PIN(mean); }
        }
      } else if (k == 22) sacc = __builtin_amdgcn_sqrtf(rbf(rbf(sacc) + 1e-5f));
      PIN(sacc);
    };
    auto rdw = [&](int n, int t) {
      wf[n & 1][t][0] = trd(wa[t] + (LW(n >> 2) + (n & 3) * 2048));
      wf[n & 1][t][1] = trd(wa[t] + (LW(n >> 2) + (n & 3) * 2048 + 512));
    };
    auto rdx = [&](int n) {
      const u32x4 v = *(LAS u32x4*)(xa[n & 3] + ((n >> 2) % 3) * 16384);
      if (n < 32) X[n] = v; else xt[n & 1] = v;
    };
    auto rdc = [&](int s) { pk[0] = trd(wc + LW(s)); pk[1] = trd(wc + (LW(s) + 512)); };
    auto wfr = [&](int n, int t) { return cat(wf[n & 1][t][0], wf[n & 1][t][1]); };
    const unsigned vl8 = lane * 8u;
    auto ld_gran = [&](int k) {
      if (k == 0) gs = ld8<1>((vl8 & 56u) | (vl8 & 192u) << 1, sc, C_STAT + Rq * 512u);
      else if (k < 4) gc[k - 1] = ld8<1>(vl8, sc, csb + (k - 1) * 1024u);
      else pf = ld4<1>((vl8 & 248u) << 4, sc, C_PF + wave * 4096u);
    };
    auto split3 = [&](unsigned g) {
      const float c = __uint_as_float(g), r1_ = c - rbf(c);
      const unsigned a0 = pk2(c, r1_);
      u32x4 a = {a0, pk2(r1_ - bhi(a0), 0.f), 0u, 0u};
      PIN(a);
      return a;
    };
    auto mk_bc = [&]() { u32x4 b = {nm | nm << 16, nm, 0u, 0u}; PIN(b); return b; };
    auto prep = [&](int k) {
      if (k == 0) {
        if (any_lane((gs[1] != tag) | (gc[0][1] != tag) | (gc[1][1] != tag) | (gc[2][1] != tag))) {
          for (int it = 0; it < SPIN; it++) {
            __builtin_amdgcn_s_sleep(1);
#pragma unroll
            for (int i = 0; i < 4; i++) ld_gran(i);
            asm volatile("s_waitcnt vmcnt(0)" : "+v"(gs), "+v"(gc[0]), "+v"(gc[1]), "+v"(gc[2]) :: "memory");
            if (!any_lane((gs[1] != tag) | (gc[0][1] != tag) | (gc[1][1] != tag) | (gc[2][1] != tag))) break;
          }
        }
      } else if (k == 1) {
        nm = (gs[0] & 0xffffu) ^ 0x8000u; PIN(nm);
        Bc = mk_bc();
        sd = bhi(gs[0]); PIN(sd); rs = __builtin_amdgcn_rcpf(sd); PIN(rs);
      } else if (k == 8) { rg = -1.44269504089f * rs; PIN(rg); }
      else if (k < (SP ? 4 : 8)) { if (k & 1) mfma32a(As, Bc, acc[(k - 3) >> 1]); else As = split3(gc[(k - 2) >> 1][0]); }
    };
#pragma unroll
    for (int r = 0; r < 16; r++) cs[r] = 0.f;
    PIN(cs);
    u32x4 on1 = ones;
    PIN(on1);
    wait_vm<8>();
    bar();
    TS(2);
    rdx(0);
#pragma unroll
    for (int t = 0; t < 3; t++) rdw(0, t);
    rdx(1);
#pragma unroll
    for (int t = 0; t < 3; t++) rdw(1, t);
    rdc(0);
    SB();
    sfor<0, 120>([&](auto I_) {
      constexpr int n = I_.v / 3, t = I_.v % 3, s = n >> 2, ks = n & 3;
      constexpr int sb = (I_.v + 4) / 12 - 1, m = (I_.v + 4) % 12;
      const u32x4 xf = n < 32 ? X[n < 32 ? n : 0] : xt[n & 1];
      if constexpr (SP && t > 0) { if constexpr (n == 0) mfma32z(on1, wfr(n, t), acc[t]); else mfma32a(on1, wfr(n, t), acc[t]); }
      else { if constexpr (n == 0) mfma32z(wfr(n, t), xf, acc[t]); else mfma32a(wfr(n, t), xf, acc[t]); }
      if constexpr (m == 11) {
        SB();
        WAIT_LGKM0();
        if constexpr (s == 0) { asm volatile("s_waitcnt vmcnt(6)" : "+v"(ep), "+v"(r0), "+v"(r1) :: "memory"); tag = __builtin_amdgcn_readfirstlane(ep) + 1u; }
        else if constexpr (s == 1) wait_vm<5>();
        else if constexpr (s == 2) wait_vm<6>();
        else if constexpr (s == 3) wait_vm<7>();
        else if constexpr (s == 4) wait_vm<6>();
        else if constexpr (s == 5) wait_vm<7>();
        else if constexpr (s == 6) wait_vm<5>();
        else if constexpr (s == 7) wait_vm<12>();
        else if constexpr (s == 8) asm volatile("s_waitcnt vmcnt(6)" : "+v"(gs), "+v"(gc[0]), "+v"(gc[1]), "+v"(gc[2]) :: "memory");
        else wait_vm<4>();
        bar();
        TS(3 + s);
        SB();
        if constexpr (n < 38) { rdw(n + 2, 0); rdw(n + 2, 1); }
      } else if constexpr (n < 38) {
        if constexpr (m != 10) rdw(n + 2, t);
        if constexpr (t == 2) rdx(n + 2);
      } else if constexpr (n == 39) { w2[t][0] = trd(wr[t & 1] + (t >> 1) * 2048); w2[t][1] = trd(wr[t & 1] + ((t >> 1) * 2048 + 512)); }
      if constexpr (sb >= 0 && sb < 4 && m == 0) rdc(sb + 1);
      if constexpr ((sb == -1 && m == 6) || (sb >= 0 && sb < 4 && m == 3))
        asm volatile("s_cmp_gt_u32 %3, 2\n\ts_cbranch_scc1 .Lce%=\n\tv_mfma_f32_32x32x16_bf16 %0, %1, %2, %0\n.Lce%=:"
                     : "+v"(cs) : "v"(on1), "v"(cat(pk[0], pk[1])), "s"(cst) : "scc");
      if constexpr (sb == 3 && m == 8) { PIN(cs); *(LAS float*)(sm + L_CSR + wave * 128 + (lane & 31) * 4) = cs[0]; }
      if constexpr (sb == 4 && m < 4) cp[m] = *(LAS float*)(sm + L_CSR + cw * 512 + m * 128 + (lane & 31) * 4);
      if constexpr (sb == 4 && m == 4)
        st8<1>(u32x2{__float_as_uint((cp[0] + cp[1]) + (cp[2] + cp[3])), tag}, sel(vl8 < (cst < 3 && mw == 0 ? 256u : 0u), vl8, OOB), sc, csb + cst * 1024u + (xcd & 1) * 256u);
      if constexpr (sb == 0 || sb == 1) stat(sb * 12 + m);
      if constexpr (sb == 2 && m == 1) *(LAS unsigned*)(sm + L_CSR + 2048 + wave * 256 + lane * 4) = pk2(mean, sacc);
      if constexpr (sb == 3 && m == 1) pf = *(LAS unsigned*)(sm + L_CSR + 2048 + (lane & 7) * 256);
      if constexpr (sb == 3 && m == 4) st8<1>(u32x2{pf, tag}, sel(vl8 < (wave == 0 ? 64u : 0u), vl8, OOB), sc, C_STAT + (rb * 16 + jg) * 128u);
      if constexpr ((sb == 1 || sb == 4) && (m == 5 || m == 6)) pk[m - 5] = trd(pkr + (m - 5) * 1024);
      if constexpr (sb == 1 && m == 10) st16<1>(cat(pk[0], pk[1]), vl, sc, WS_W3 + (kA * 40u + cA * 8u + wave) * 1024u);
      if constexpr (sb == 4 && m == 10) st16<1>(cat(pk[0], pk[1]), vl | offB, sc, WS_W3 + (kB * 40u + cB * 8u + wave) * 1024u);
      if constexpr (sb == 2 && m == 10) dma16(ldw + L_PK, vpk | offB, mk_srd(p.wo, WO_BYTES), (kB - 48) * 40960u + cB * 256u);
      if constexpr (sb == -1 && m < 9) issue(2, m - 4);
      if constexpr (sb == -1 && m == 9) dma16(ldw + L_PK, vpk, sel_srd(kA < 48, sdn, so), (kA < 48 ? kA : kA - 48) * 40960u + cA * 256u);
      if constexpr (sb == 6 && m == 0) st4s(tag, sel(vl8 == 0, wave == 0 ? C_PF + bid * 128u : OOB, OOB), sc);
      if constexpr (sb >= 0 && sb < 7 && (m & 1) && m < 10) issue(sb + 3, m >> 1);
      if constexpr (sb == 6 && m == 10) { issue_g(0, 0); issue_g(0, 1); ld_gran(0); ld_gran(1); ld_gran(2); ld_gran(3); }
      if constexpr (sb == 7 && !(m & 1)) issue_g(1 + m / 4, (m >> 1) & 1);
      if constexpr (sb == 8 && (m & 1) && m < 9) issue_g(4 + m / 4, (m >> 1) & 1);
      if constexpr (sb == 9 && m == 0) ld_gran(4);
      if constexpr (sb == 9 && m > 0 && m < 3) { issue_g(5 + m, 0); issue_g(5 + m, 1); }
      if constexpr (sb == 8 && m < 9) prep(m);
      SB();
    });
    TS(13);
    auto round2 = [&](auto TY_) {
      constexpr int TY = TY_.v;
      f32x16 ac2[2];
      u32x4 qv, hv[2], A4[2];
      u32x2 gc2[2];
      float t[8], rq;
      unsigned fq, vf, vh, kve;
      bool late;
      auto qop = [&](auto U_, auto K_) {
        constexpr int u = U_.v, k = K_.v;
        const unsigned ln = 4 * pr + tyB + u;
        if constexpr (k == 0) {
          const float qs = ln < 10u ? 0.22360679775f * 1.44269504089f : 1.0f;
          asm volatile("v_mul_f32 %0, %1, %2" : "=v"(rq) : "s"(qs), "v"(rs));
        } else {
          constexpr int h = (k - 1) / 13, j = (k - 1) % 13;
          if constexpr (j < 8) t[j] = v_mul(acc[u][8 * h + j], rq);
          else if constexpr (j < 12) qv[j - 8] = v_cvt(t[2 * j - 16], t[2 * j - 15]);
          else st16<1>(qv, vl, sc, WS_QKV + (Rq * 240u + ln * 8u + cw * 4u + h * 2u) * 512u);
        }
      };
      auto gop = [&](f32x16& a, u32x4& o, auto K_) {
        constexpr int k = K_.v, ph = k >> 3, j = k & 7;
        if constexpr (ph == 0) t[j] = v_mul(a[j], rg);
        else if constexpr (ph == 1) t[j] = v_exp(t[j]);
        else if constexpr (ph == 2) t[j] = v_fma(t[j], sd, sd);
        else if constexpr (ph == 3) t[j] = v_rcp(t[j]);
        else if constexpr (ph == 4) t[j] = v_mul(t[j], a[8 + j]);
        else o[j] = v_cvt(t[2 * j], t[2 * j + 1]);
      };
      auto st_h = [&](u32x4 v, unsigned gl, int a) { st16<1>(v, vh, sc, WS_AF + ((rb * 8 + 2 * mw) * 68u + 2 * gl + cw) * 1024u + a * 512u); };
      auto epi = [&](auto K_) {
        constexpr int k = K_.v;
        if constexpr (k < 27) qop(IC<0>{}, K_);
        else if constexpr (TY == 1) { if constexpr (k < 81) qop(IC<k / 27>{}, IC<k % 27>{}); }
        else if constexpr (TY == 0) {
          if constexpr (k < 117) {
            constexpr int u = (k - 27) / 45, q = (k - 27) % 45;
            if constexpr (q < 44) gop(acc[1 + u], hv[u], IC<q>{}); else st_h(hv[u], gl1, u);
          }
        } else if constexpr (k < 29) {
          PIN(acc[k - 26]);
          st8<1>(u32x2{__float_as_uint(acc[k - 26][0] * 0.5f), tag}, sel(mw == 0, vl8, OOB), sc, C_CS + (rb * 10 + cw) * 512u + (k - 24) * 1024u);
        }
      };
      constexpr auto EK = [](int q) { return q < 1 ? 0 : (TY == 0 && q > 7) ? cl(30 + (q - 7) * 3, 0, 117) : cl((q - 1) * 5, 0, 117); };
      sfor<0, 80>([&](auto M_) {
        constexpr int m = M_.v;
        constexpr auto KK = [](int q) { return q < 64 ? q >> 1 : 32 + (q & 7); };
        constexpr auto AA = [](int q) { return q < 64 ? q & 1 : (q >> 3) & 1; };
        constexpr int kk = KK(m), a = AA(m), g = kk >> 2;
        if constexpr (m == 16) {
          asm volatile("s_waitcnt vmcnt(%1)" : "+v"(pf) : "i"(TY == 1 ? 5 : TY == 0 ? 2 : 4) : "memory");
          poll_rest(pf, true, tag, C_PF + wave * 4096u + ((vl8 & 248u) << 4), sc);
          bar();
          TS(14);
          st4s(tag, sel(vl8 == 0, (bid | wave) == 0 ? 0u : OOB, OOB), sc);
          SB();
        }
        if constexpr (m == 44) {
          wait_vm<(TY == 0 ? 11 : 10)>();
          st4s(tag, sel(vl8 == 0, C_FQ + fme, OOB), sc);
          TS(15);
          SB();
        }
        if constexpr (m == 48) { bar(); SB(); }
        if constexpr (m == 50) {
          {
            const int l = lane_id(), sec = (l + 2) >> 2, fr = sel(l < 2, ch * 8 + wave, ch * 8 - 8 + 2 * wave + ((l >> 1) & 1));
            const unsigned oc = sec * 80u + o0 + (l & 1) * 2u, jp = (oc >> 5) * 2 + ((oc & 24u) != 0);
            vf = sel((l < 10) & (fr >= 0), C_FQ + ((jp * 16 + (fr >> 2)) * 8 + (oc & 4u) + (fr & 3)) * 128u, OOB);
          }
          gc2[0] = ld8<1>(vl8, sc, csb + 3072u); gc2[1] = ld8<1>(vl8, sc, csb + 4096u);
          fq = ld4<1>(vf, sc, 0);
          SB();
        }
        if constexpr (m == 59) {
          asm volatile("s_waitcnt vmcnt(0)" : "+v"(fq), "+v"(gc2[0]), "+v"(gc2[1]) :: "memory");
          TS(16);
          if constexpr (TY == 0) st4s(tag, sel(vl8 == 0, C_FE + fme, OOB), sc);
          if (any_lane((gc2[0][1] != tag) | (gc2[1][1] != tag))) {
            for (int it = 0; it < SPIN; it++) {
              __builtin_amdgcn_s_sleep(1);
              gc2[0] = ld8<1>(vl8, sc, csb + 3072u); gc2[1] = ld8<1>(vl8, sc, csb + 4096u);
              asm volatile("s_waitcnt vmcnt(0)" : "+v"(gc2[0]), "+v"(gc2[1]) :: "memory");
              if (!any_lane((gc2[0][1] != tag) | (gc2[1][1] != tag))) break;
            }
          }
          late = any_lane((vf != OOB) & (fq != tag));
          fq = ld4<1>(vf, sc, 0);
          kve = sel(late, OOB, kvv);
          PIN(kve);
          SB();
        }
        if constexpr (m == 66) {
          if (late) {
            asm volatile("s_waitcnt vmcnt(4)" : "+v"(fq) :: "memory");
            poll_rest(fq, vf != OOB, tag, vf, sc);
#pragma unroll
            for (int i = 0; i < 4; i++) kv1(i, kvv);
          }
          TS(17);
          SB();
        }
        const u32x4 xf = (g == 2 || g == 3) ? xt[kk & 1] : X[(g == 2 || g == 3) ? 0 : KS(g) * 4 + (kk & 3)];
        if constexpr (m < 2) mfma32z(cat(w2[m & 3][0], w2[m & 3][1]), xf, ac2[a]);
        else mfma32a(cat(w2[m & 3][0], w2[m & 3][1]), xf, ac2[a]);
        if constexpr (m + 3 < 80) {
          constexpr int k2 = KK(m + 3), a2 = AA(m + 3), g2 = k2 >> 2, gp = (k2 - 1) >> 2;
          if constexpr ((LG(g2) >> 16) != (LG(gp) >> 16)) { wr[a2] += (int)(LG(g2) & ~65535u) - (int)(LG(gp) & ~65535u); PIN(wr[a2]); }
          w2[(m + 3) & 3][0] = trd(wr[a2] + ((LG(g2) & 65535u) + (k2 & 3) * 2048));
          w2[(m + 3) & 3][1] = trd(wr[a2] + ((LG(g2) & 65535u) + (k2 & 3) * 2048 + 512));
          if constexpr (a2 == 0 && (g2 == 2 || g2 == 3)) xt[k2 & 1] = *(LAS u32x4*)(xa[k2 & 3] + (KS(g2) % 3) * 16384);
        }
        if constexpr (m == 8) { const unsigned l = lane_id(); vh = (l & 15) * 16u + ((l >> 4) & 1) * 69632u + (l >> 5) * 256u; PIN(vh); }
        sfor<EK(m), EK(m + 1)>(epi);
        if constexpr (m == 16 || m == 17) { issue_g(m - 8, 0); issue_g(m - 8, 1); }
        if constexpr (m >= 24 && m < 44 && !(m & 1)) ld_w3((m - 24) / 10, (m - 24) / 10, ((m - 24) >> 1) % 5);
        if constexpr (m >= 52 && m < 55) att_addr(m - 52);
        if constexpr (m >= 65 && m < 73) att_mask(m - 65);
        if constexpr (m == 60 || m == 61) A4[m - 60] = split3(gc2[m - 60][0]);
        if constexpr (m == 62) Bc = mk_bc();
        if constexpr (m == 64 || m == 65) mfma32a(A4[m - 64], Bc, ac2[m - 64]);
        if constexpr (m == 56) kv_addr();
        if constexpr (m >= 59 && m < 66 && (m & 1)) kv1((m - 59) >> 1, kve);
        if constexpr (m >= 67 && m < 74 && (m & 1)) kv1((m - 59) >> 1, kvv);
        if constexpr (m == 68 || m == 70) q_issue((m - 68) >> 1);
        if constexpr (m >= 45 && m < 50) ld_w3(2, 2, m - 45);
        if constexpr (W3E && (m == 72 || (m >= 74 && m < 78))) ld_w3(3, 3, m == 72 ? 0 : m - 73);
        if constexpr (m >= 73) sfor<(m - 73) * 7, cl((m - 72) * 7, 0, 44)>([&](auto K_) { gop(ac2[0], hv[0], K_); });
        SB();
      });
      TS(18);
      fe = ld4<1>(sel(vl8 < 32u, C_FE + fpo(), OOB), sc, 0);
      st_h(hv[0], gl2, 0);
      asm volatile("s_nop 7" ::: "memory");
      sfor<0, 44>([&](auto K_) { gop(ac2[1], hv[1], K_); });
      st_h(hv[1], gl2, 1);
      u32x4 (&rw)[6][5] = RW; u32x4 &q1 = Q1, &q2 = Q2;
      asm volatile("s_waitcnt vmcnt(%13)" : "+v"(q1), "+v"(q2), "+v"(fq), "+v"(rw[0][0]), "+v"(rw[0][1]), "+v"(rw[0][2]), "+v"(rw[0][3]), "+v"(rw[0][4]),
                   "+v"(rw[1][0]), "+v"(rw[1][1]), "+v"(rw[1][2]), "+v"(rw[1][3]), "+v"(rw[1][4]) : "i"(W3E ? 7 : 3) : "memory");
      TS(19);
      rest();
    };
    if constexpr (SP) round2(IC<2>{});
    else { if (tyB) round2(IC<1>{}); else round2(IC<0>{}); }
  };
  if (spec) phase1(IC<1>{}); else phase1(IC<0>{});
}

extern "C" void kernel_launch(void* const* d_in, const int* in_sizes, int n_in, void* d_out, int out_size, void* d_ws,
                              size_t ws_size, hipStream_t stream) {
  Params p;
  p.x = (const char*)d_in[0]; p.wqkv = (const char*)d_in[1]; p.wo = (const char*)d_in[2];
  p.wg = (const char*)d_in[4]; p.wu = (const char*)d_in[5]; p.wd = (const char*)d_in[6];
  p.out = (char*)d_out; p.ws = (char*)d_ws;
  (void)hipFuncSetAttribute((const void*)k_fused, hipFuncAttributeMaxDynamicSharedMemorySize, LDS_BYTES);
  k_fused<<<256, 512, LDS_BYTES, stream>>>(p);
}
```
